```python
import torch

def _softmax(x, axis=-1):
    return torch.softmax(x, dim=axis)
def _gelu(x):
    return torch.nn.functional.gelu(x, approximate="tanh")
def _silu(x):
    return torch.nn.functional.silu(x)
def _sigmoid(x):
    return torch.sigmoid(x)
def _layernorm(x, axis=-1, eps=1e-5):
    m = x.mean(dim=axis, keepdim=True)
    v = x.var(dim=axis, keepdim=True, unbiased=False)
    return (x - m) / torch.sqrt(v + eps)
def _scatter_add(tgt, idx, src, axis=0):
    return tgt.clone().index_add_(axis, idx, src)
def _segment_sum(src, seg, n):
    out = torch.zeros((n,) + src.shape[1:], dtype=src.dtype)
    return out.index_add_(0, seg, src)
def _pad(x, pw):
    flat = tuple(v for pair in reversed(pw) for v in pair)
    return torch.nn.functional.pad(x, flat)

kernel_name = 'gated_mix__ssm_conv_s4__swiglu_ffn__prompt'

def setup_inputs():

    _w = (lambda a: (lambda t: t.to(torch.bfloat16) if t.is_floating_point() else t)(torch.as_tensor(a)))
    def _A_setup():
        B, L, D, N = 1, 2048, 512, 128
        A_re = -(lambda _x: torch.exp(torch.as_tensor(_x)))((lambda *s: torch.randn(*s, dtype=torch.bfloat16) * 3.2)(D, N))
        A_im = torch.pi * (lambda *s: torch.randn(*s, dtype=torch.bfloat16) * 0.05)(D, N)
        B_re = (lambda *s: torch.randn(*s, dtype=torch.bfloat16) * 0.1)(D, N)
        B_im = (lambda *s: torch.randn(*s, dtype=torch.bfloat16) * 0.1)(D, N)
        C_re = (lambda *s: torch.randn(*s, dtype=torch.bfloat16) * 0.1)(D, N)
        C_im = (lambda *s: torch.randn(*s, dtype=torch.bfloat16) * 0.1)(D, N)
        log_dt = (lambda *s: torch.randn(*s, dtype=torch.bfloat16) * 0.05)(D) * 0.5 - 4.0
        Dskip = (lambda *s: torch.randn(*s, dtype=torch.bfloat16) * 0.05)(D)
        return {
            "u": _w((lambda *s: torch.randn(*s, dtype=torch.bfloat16) * 0.05)(B, L, D)),
            "A_re": _w(A_re),
            "A_im": _w(A_im),
            "B_re": _w(B_re),
            "B_im": _w(B_im),
            "C_re": _w(C_re),
            "C_im": _w(C_im),
            "log_dt": _w(log_dt),
            "Dskip": _w(Dskip),
        }
    _A_in = _A_setup()

    def _B_setup():
        B, S, D, F = 1, 2048, 512, 1792
        return {
            "x": _w((lambda *s: torch.randn(*s, dtype=torch.bfloat16) * 0.05)(B, S, D)),
            "w_gate": _w((lambda *s: torch.randn(*s, dtype=torch.bfloat16) * 0.05)(D, F)),
            "w_up": _w((lambda *s: torch.randn(*s, dtype=torch.bfloat16) * 0.05)(D, F)),
            "w_down": _w((lambda *s: torch.randn(*s, dtype=torch.bfloat16) * 0.05)(F, D)),
        }
    _B_in = _B_setup()

    return {
        "A_u": _A_in["u"],
        "A_A_re": _A_in["A_re"],
        "A_A_im": _A_in["A_im"],
        "A_B_re": _A_in["B_re"],
        "A_B_im": _A_in["B_im"],
        "A_C_re": _A_in["C_re"],
        "A_C_im": _A_in["C_im"],
        "A_log_dt": _A_in["log_dt"],
        "A_Dskip": _A_in["Dskip"],
        "B_x": _B_in["x"],
        "B_w_gate": _B_in["w_gate"],
        "B_w_up": _B_in["w_up"],
        "B_w_down": _B_in["w_down"],
        "gate_w": _w((lambda *s: torch.randn(*s, dtype=torch.bfloat16) * 0.05)(512) * 16.0),
    }

def reference(A_u, A_A_re, A_A_im, A_B_re, A_B_im, A_C_re, A_C_im, A_log_dt, A_Dskip, B_x, B_w_gate, B_w_up, B_w_down, gate_w):
    def _A_ref(u, A_re, A_im, B_re, B_im, C_re, C_im, log_dt, Dskip):
        Bsz, L, D = u.shape
        dt = torch.exp(log_dt)[:, None]
        A = A_re + 1j * A_im
        Bc = B_re + 1j * B_im
        Cc = C_re + 1j * C_im
        logdA = dt * A
        dB = dt * Bc
        l = (lambda x, dt: x.to(dt))(torch.arange(L), A_re.dtype)
        P = torch.exp(logdA[:, :, None] * l[None, None, :])
        K = torch.einsum("dn,dnl,dn->dl", Cc, P, dB).real
        L2 = 2 * L
        Kp = _pad(K, ((0, 0), (0, L)))
        up = (lambda x, *d: x.permute(*d))(u, 0, 2, 1)
        up = _pad(up, ((0, 0), (0, 0), (0, L)))
        Kf = (lambda x, axes=(-2,-1): torch.fft.rfft2(x.float(), dim=axes))(Kp[:, None, :], axes=(-2, -1))
        Uf = (lambda x, axes=(-2,-1): torch.fft.rfft2(x.float(), dim=axes))(up[:, :, None, :], axes=(-2, -1))
        Yf = Uf * Kf[None, :, :, :]
        y = (lambda x, s, axes=(-2,-1): torch.fft.irfft2(x, s=s, dim=axes).to(torch.bfloat16))(Yf, (1, L2), axes=(-2, -1))[:, :, 0, :L]
        y = (lambda x, *d: x.permute(*d))(y, 0, 2, 1) + u * Dskip[None, None, :]
        return y

    def _B_ref(x, w_gate, w_up, w_down):
        g = _silu(x @ w_gate)
        u = x @ w_up
        return (g * u) @ w_down

    x = A_u
    g = _sigmoid((x * gate_w).sum(-1, keepdims=True))
    a = _A_ref(x, A_A_re, A_A_im, A_B_re, A_B_im, A_C_re, A_C_im, A_log_dt, A_Dskip)
    b = _B_ref(x, B_w_gate, B_w_up, B_w_down)
    return g * a + (1 - g) * b
```

```cpp
#include <hip/hip_runtime.h>
#include <hip/hip_bf16.h>
#include <stdint.h>

typedef unsigned short u16;
__device__ __forceinline__ float bf2f(u16 v){ return __uint_as_float(((uint32_t)v)<<16); }
__device__ __forceinline__ u16 f2bf(float f){ uint32_t u=__float_as_uint(f); return (u16)((u+0x7FFF+((u>>16)&1))>>16); }
__device__ __forceinline__ float rbf(float f){ return bf2f(f2bf(f)); }

constexpr int L=2048, D=512, NM=128, F=1792;

__global__ void kgen_naive(const u16* Are,const u16* Aim,const u16* Bre,const u16* Bim,const u16* Cre,const u16* Cim,const u16* logdt,float* K){
  int d=blockIdx.x;
  float dt=rbf(expf(bf2f(logdt[d])));
  for(int l=threadIdx.x;l<L;l+=blockDim.x){
    float lf=rbf((float)l);
    float acc=0.f;
    for(int n=0;n<NM;n++){
      float ar=dt*bf2f(Are[d*NM+n]), ai=dt*bf2f(Aim[d*NM+n]);
      float br=dt*bf2f(Bre[d*NM+n]), bi=dt*bf2f(Bim[d*NM+n]);
      float cr=bf2f(Cre[d*NM+n]), ci=bf2f(Cim[d*NM+n]);
      float e=expf(ar*lf); float s,c; sincosf(ai*lf,&s,&c);
      float pr=e*c, pi=e*s;
      float wr=cr*br-ci*bi, wi=cr*bi+ci*br;
      acc+=wr*pr-wi*pi;
    }
    K[d*L+l]=acc;
  }
}
__global__ void conv_naive(const u16* u,const float* K,const u16* Dskip,float* a){
  int t=blockIdx.x;
  for(int d=threadIdx.x;d<D;d+=blockDim.x){
    float acc=0.f;
    for(int s=0;s<=t;s++) acc+=K[d*L+(t-s)]*bf2f(u[s*D+d]);
    float y=rbf(acc);
    float sk=rbf(bf2f(u[t*D+d])*bf2f(Dskip[d]));
    a[t*D+d]=rbf(y+sk);
  }
}
__global__ void ffn1_naive(const u16* x,const u16* wg,const u16* wu,u16* h){
  int t=blockIdx.x;
  __shared__ float xs[D];
  for(int k=threadIdx.x;k<D;k+=blockDim.x) xs[k]=bf2f(x[t*D+k]);
  __syncthreads();
  for(int f=threadIdx.x;f<F;f+=blockDim.x){
    float g=0,uu=0;
    for(int k=0;k<D;k++){ g+=xs[k]*bf2f(wg[k*F+f]); uu+=xs[k]*bf2f(wu[k*F+f]); }
    g=rbf(g); uu=rbf(uu);
    float s=rbf(g/(1.f+expf(-g)));
    h[t*F+f]=f2bf(s*uu);
  }
}
__global__ void ffn2_naive(const u16* x,const u16* h,const u16* wd,const u16* gw,const float* a,u16* out){
  int t=blockIdx.x;
  __shared__ float hs[F];
  __shared__ float red[256];
  for(int k=threadIdx.x;k<F;k+=blockDim.x) hs[k]=bf2f(h[t*F+k]);
  float p=0; for(int k=threadIdx.x;k<D;k+=blockDim.x) p+=rbf(bf2f(x[t*D+k])*bf2f(gw[k]));
  red[threadIdx.x]=p;
  __syncthreads();
  for(int s=128;s>0;s>>=1){ if(threadIdx.x<s) red[threadIdx.x]+=red[threadIdx.x+s]; __syncthreads(); }
  float gs=rbf(red[0]);
  float g=rbf(1.f/(1.f+expf(-gs)));
  float omg=rbf(1.f-g);
  for(int d=threadIdx.x;d<D;d+=blockDim.x){
    float b=0; for(int k=0;k<F;k++) b+=hs[k]*bf2f(wd[k*D+d]);
    b=rbf(b);
    out[t*D+d]=f2bf(rbf(g*a[t*D+d])+rbf(omg*b));
  }
}

extern "C" void kernel_launch(void* const* d_in, const int* in_sizes, int n_in,
                              void* d_out, int out_size, void* d_ws, size_t ws_size,
                              hipStream_t stream) {
  const u16* u=(const u16*)d_in[0];
  float* K=(float*)d_ws; float* a=K+D*L; u16* h=(u16*)(a+L*D);
  kgen_naive<<<D,256,0,stream>>>((const u16*)d_in[1],(const u16*)d_in[2],(const u16*)d_in[3],(const u16*)d_in[4],(const u16*)d_in[5],(const u16*)d_in[6],(const u16*)d_in[7],K);
  conv_naive<<<L,256,0,stream>>>(u,K,(const u16*)d_in[8],a);
  ffn1_naive<<<L,256,0,stream>>>(u,(const u16*)d_in[10],(const u16*)d_in[11],h);
  ffn2_naive<<<L,256,0,stream>>>(u,h,(const u16*)d_in[12],(const u16*)d_in[13],a,(u16*)d_out);
}
```

```cpp
#include <hip/hip_runtime.h>
#include <hip/hip_bf16.h>
#include <stdint.h>

typedef unsigned short u16;
typedef __attribute__((ext_vector_type(4))) short s16x4;
typedef __attribute__((ext_vector_type(8))) short s16x8;
typedef __attribute__((ext_vector_type(8))) __bf16 bf16x8;
typedef __attribute__((ext_vector_type(2))) __bf16 bf16x2;
typedef __attribute__((ext_vector_type(2))) float f32x2;
typedef __attribute__((ext_vector_type(4))) float f32x4;
typedef __attribute__((ext_vector_type(4))) unsigned u32x4;
typedef __attribute__((ext_vector_type(2))) unsigned u32x2;
typedef __attribute__((address_space(3))) s16x4* lds_s16x4_ptr;
typedef __attribute__((address_space(1))) unsigned gu32;
typedef __attribute__((address_space(1))) unsigned long long gu64;
#define RLX_AGENT __ATOMIC_RELAXED, __HIP_MEMORY_SCOPE_AGENT
#define DEV __device__ __forceinline__

constexpr int L = 2048, D = 512, NM = 128, F = 1792;
constexpr int KS2 = F / 32;

constexpr size_t WS_H = 0;
constexpr size_t WS_WDT = WS_H + (size_t)L * F * 2;
constexpr size_t WS_UT = WS_WDT + (size_t)F * D * 2;
constexpr size_t WS_YT = WS_UT + (size_t)L * D * 2;
constexpr size_t WS_KC = WS_YT + (size_t)L * D * 2;
constexpr size_t WS_G = WS_KC + (size_t)D * 1024 * 2;
constexpr size_t WS_CNT = WS_G + (size_t)L * 4;

DEV float bf2f(u16 v) { return __uint_as_float(((uint32_t)v) << 16); }
DEV float bflo(uint32_t v) { return __uint_as_float(v << 16); }
DEV float bfhi(uint32_t v) { return __uint_as_float(v & 0xffff0000u); }
DEV uint32_t pk2(float a, float b) { f32x2 v = {a, b}; return __builtin_bit_cast(uint32_t, __builtin_convertvector(v, bf16x2)); }
DEV float rbf(float a) { return bflo(pk2(a, 0.f) & 0xffffu); }
DEV f32x4 mfma16(u32x4 a, u32x4 b, f32x4 c) {
  return __builtin_amdgcn_mfma_f32_16x16x32_bf16(__builtin_bit_cast(bf16x8, a), __builtin_bit_cast(bf16x8, b), c, 0, 0, 0);
}
DEV u32x2 tr_read(const char* p) {
  return __builtin_bit_cast(u32x2, __builtin_amdgcn_ds_read_tr16_b64_v4i16((lds_s16x4_ptr)p));
}
DEV u32x4 tr_read2(const char* p0, const char* p1) {
  u32x2 a = tr_read(p0), b = tr_read(p1);
  u32x4 r = {a.x, a.y, b.x, b.y};
  return r;
}
DEV void glds16(const void* g, char* lds) {
  __builtin_amdgcn_global_load_lds((const __attribute__((address_space(1))) unsigned*)g, (__attribute__((address_space(3))) unsigned*)lds, 16, 0, 0);
}
#define WAIT_V(n) asm volatile("s_waitcnt vmcnt(" #n ")" ::: "memory")
#define WAIT_L(n) asm volatile("s_waitcnt lgkmcnt(" #n ")" ::: "memory")
#define BAR() __builtin_amdgcn_s_barrier()

struct P0 {
  const u16 *u, *Are, *Aim, *Bre, *Bim, *Cre, *Cim, *logdt, *wg, *wu, *wd, *gw;
  u16 *h, *wdt, *uT, *Kc;
  float* g;
  unsigned* cnt;
};

constexpr int STAGE_B = 49152;
constexpr int K0_LDS = 3 * STAGE_B;

DEV int wswz(int row) { return ((row & 3) << 2) | ((row >> 2) & 3); }

DEV void kgen(const P0& p, int ch0, char* scratch, int tid) {
  const int w = tid >> 6, lane = tid & 63, i = lane & 15, g = lane >> 4;
  const int c = w >> 2, d = ch0 + c;
  const float dt = rbf(__expf(bf2f(p.logdt[d])));
  const float p0 = (float)((i & 7) + 128 * (i >> 3));
  const float p1 = (i < 8) ? (float)(256 + 2 * (i & 3) + 128 * (i >> 2)) : (float)(512 + 4 * (i & 1) + 128 * ((i - 8) >> 1));
  const float p2 = (i < 8) ? (float)(1024 + 128 * i) : 2048.f;
  const float q = (float)(8 * i);
  f32x4 acc[3] = {{0, 0, 0, 0}, {0, 0, 0, 0}, {0, 0, 0, 0}};
#pragma unroll
  for (int kk = 0; kk < 2; ++kk) {
    const int n0 = d * NM + 16 * (2 * (w & 3) + kk) + 4 * g;
    const u32x2 are = *(const u32x2*)(p.Are + n0), aim = *(const u32x2*)(p.Aim + n0);
    const u32x2 bre = *(const u32x2*)(p.Bre + n0), bim = *(const u32x2*)(p.Bim + n0);
    const u32x2 cre = *(const u32x2*)(p.Cre + n0), cim = *(const u32x2*)(p.Cim + n0);
    u32x4 ah[3], al[3], bh, bl;
#pragma unroll
    for (int j = 0; j < 4; ++j) {
      const uint32_t wa = j < 2 ? are.x : are.y, wi = j < 2 ? aim.x : aim.y, wbr = j < 2 ? bre.x : bre.y, wbi = j < 2 ? bim.x : bim.y,
                     wcr = j < 2 ? cre.x : cre.y, wci = j < 2 ? cim.x : cim.y;
      const float ar = dt * ((j & 1) ? bfhi(wa) : bflo(wa)), ai = dt * ((j & 1) ? bfhi(wi) : bflo(wi));
      const float br = dt * ((j & 1) ? bfhi(wbr) : bflo(wbr)), bi = dt * ((j & 1) ? bfhi(wbi) : bflo(wbi));
      const float cr = (j & 1) ? bfhi(wcr) : bflo(wcr), ci = (j & 1) ? bfhi(wci) : bflo(wci);
      const float a2 = ar * 1.44269504088896340736f, wv = ai * 0.15915494309189533577f;
      const float xr = cr * br - ci * bi, xi = cr * bi + ci * br;
      {
        const float e = __builtin_amdgcn_exp2f(a2 * q), r = __builtin_amdgcn_fractf(wv * q);
        const float s = __builtin_amdgcn_sinf(r) * e, co = __builtin_amdgcn_cosf(r) * e;
        const float vr = xr * co - xi * s, vi = -(xr * s + xi * co);
        const uint32_t hi = pk2(vr, vi);
        bh[j] = hi;
        bl[j] = pk2(vr - bflo(hi), vi - bfhi(hi));
      }
#pragma unroll
      for (int T = 0; T < 3; ++T) {
        const float pp = T == 0 ? p0 : (T == 1 ? p1 : p2);
        const float e = __builtin_amdgcn_exp2f(a2 * pp), r = __builtin_amdgcn_fractf(wv * pp);
        const float wr = __builtin_amdgcn_cosf(r) * e, wim = __builtin_amdgcn_sinf(r) * e;
        const uint32_t hi = pk2(wr, wim);
        ah[T][j] = hi;
        al[T][j] = pk2(wr - bflo(hi), wim - bfhi(hi));
      }
    }
#pragma unroll
    for (int T = 0; T < 3; ++T) {
      acc[T] = mfma16(al[T], bh, acc[T]);
      acc[T] = mfma16(ah[T], bl, acc[T]);
      acc[T] = mfma16(ah[T], bh, acc[T]);
    }
  }
  float* sc = (float*)scratch;
#pragma unroll
  for (int T = 0; T < 3; ++T) *(f32x4*)(sc + ((w * 3 + T) * 64 + lane) * 4) = acc[T];
  __syncthreads();
  {
    const int cc = tid >> 8, pos = tid & 255, ln = pos >> 2, j = pos & 3;
    const int ii = 4 * (ln >> 4) + j, b = ln & 15;
    u16* dst = p.Kc + (size_t)(ch0 + cc) * 1024;
#pragma unroll
    for (int T = 0; T < 3; ++T) {
      float s = 0.f;
#pragma unroll
      for (int ww = 0; ww < 4; ++ww) s += sc[((cc * 4 + ww) * 3 + T) * 256 + pos];
      int idx;
      if (T == 0) idx = (ii & 7) + 128 * (ii >> 3) + 8 * b;
      else if (T == 1) idx = ii < 8 ? 256 + (ii & 3) + 64 * (ii >> 2) + 4 * b : 384 + (ii & 1) + 32 * ((ii - 8) >> 1) + 2 * b;
      else idx = ii < 8 ? 512 + 16 * ii + b : ((ii == 8 && b == 0) ? 640 : -1);
      if (idx >= 0) dst[idx] = (u16)(pk2(s, 0.f) & 0xffffu);
    }
  }
  __syncthreads();
}

DEV void k0_stage(const P0& p, char* buf, int m0, int f0, int k0, int w, int lane) {
#pragma unroll
  for (int j = 0; j < 2; ++j) {
    const int row = 8 * (2 * w + j) + (lane >> 3), c = (lane & 7) ^ (row & 7);
    glds16(p.u + (size_t)(m0 + row) * D + k0 + 8 * c, buf + (2 * w + j) * 1024);
  }
#pragma unroll
  for (int j = 0; j < 2; ++j) {
    const int row = 4 * (2 * w + j) + (lane >> 4), c = (lane & 15) ^ wswz(row);
    const size_t off = (size_t)(k0 + row) * F + f0 + 8 * c;
    glds16(p.wg + off, buf + 16384 + (2 * w + j) * 1024);
    glds16(p.wu + off, buf + 32768 + (2 * w + j) * 1024);
  }
}

__global__ __launch_bounds__(512, 2) void k0_kernel(P0 p) {
  extern __shared__ __attribute__((aligned(16))) char smem[];
  const int tid = threadIdx.x, lane = tid & 63, w = __builtin_amdgcn_readfirstlane(tid >> 6);
  const int b = blockIdx.x, xcd = b & 7, idx = b >> 3;
  if (b == 0 && tid < 8) p.cnt[tid * 64] = 0u;
  if (idx < 28) {
    const int mt = (xcd >> 1) * 4 + idx / 7, ft = (xcd & 1) * 7 + idx % 7;
    const int m0 = mt * 128, f0 = ft * 128;
    k0_stage(p, smem, m0, f0, 0, w, lane);
    k0_stage(p, smem + STAGE_B, m0, f0, 64, w, lane);
    kgen(p, 2 * b, smem + 2 * STAGE_B, tid);
    k0_stage(p, smem + 2 * STAGE_B, m0, f0, 128, w, lane);
    const int wm = w >> 2, wn = w & 3, g = lane >> 4, q = (lane & 15) >> 2, pp = lane & 3;
    f32x4 acc[2][2][4];
#pragma unroll
    for (int a = 0; a < 2; ++a)
#pragma unroll
      for (int c = 0; c < 2; ++c)
#pragma unroll
        for (int m = 0; m < 4; ++m) acc[a][c][m] = (f32x4){0, 0, 0, 0};
#pragma unroll
    for (int s = 0; s < 8; ++s) {
      if (s < 6) { if (s == 0) WAIT_V(12); else WAIT_V(6); } else if (s == 6) WAIT_V(6); else WAIT_V(0);
      BAR();
      if (s >= 1 && s + 2 < 8) k0_stage(p, smem + ((s + 2) % 3) * STAGE_B, m0, f0, 64 * (s + 2), w, lane);
      const char* buf = smem + (s % 3) * STAGE_B;
#pragma unroll
      for (int ss = 0; ss < 2; ++ss) {
        u32x4 xf[4], wf[2][2];
#pragma unroll
        for (int m = 0; m < 4; ++m) {
          const int row = 64 * wm + 16 * m + (lane & 15), c = 4 * ss + g;
          xf[m] = *(const u32x4*)(buf + row * 128 + ((c ^ (row & 7)) << 4));
        }
#pragma unroll
        for (int a = 0; a < 2; ++a)
#pragma unroll
          for (int c = 0; c < 2; ++c) {
            const int ch = 4 * wn + 2 * c + (pp >> 1);
            const int r0 = 32 * ss + 8 * g + q, r1 = r0 + 4;
            const char* wb = buf + 16384 + a * 16384 + 8 * (pp & 1);
            wf[a][c] = tr_read2(wb + 256 * r0 + 16 * (ch ^ wswz(r0)), wb + 256 * r1 + 16 * (ch ^ wswz(r1)));
          }
#pragma unroll
        for (int a = 0; a < 2; ++a)
#pragma unroll
          for (int c = 0; c < 2; ++c)
#pragma unroll
            for (int m = 0; m < 4; ++m) acc[a][c][m] = mfma16(wf[a][c], xf[m], acc[a][c][m]);
      }
      WAIT_L(0);
    }
#pragma unroll
    for (int m = 0; m < 4; ++m) {
      u32x4 o;
#pragma unroll
      for (int c = 0; c < 2; ++c)
#pragma unroll
        for (int jj = 0; jj < 2; ++jj) {
          float hv[2];
#pragma unroll
          for (int e = 0; e < 2; ++e) {
            const float gt = rbf(acc[0][c][m][2 * jj + e]), up = rbf(acc[1][c][m][2 * jj + e]);
            const float sl = rbf(gt * __builtin_amdgcn_rcpf(1.f + __expf(-gt)));
            hv[e] = sl * up;
          }
          o[2 * c + jj] = pk2(hv[0], hv[1]);
        }
      const int mtile = (m0 + 64 * wm + 16 * m) >> 4, ks = 4 * ft + wn;
      *(u32x4*)(p.h + ((size_t)(mtile * KS2 + ks) * 64 + lane) * 8) = o;
    }
  } else {
    const int a = xcd * 4 + (idx - 28);
    kgen(p, 2 * b, smem, tid);
    constexpr int RS = 1056;
    const int t0 = 64 * a;
    {
      u32x4 gwv = *(const u32x4*)(p.gw + lane * 8);
#pragma unroll
      for (int r = 0; r < 8; ++r) {
        const int row = 8 * w + r;
        const u32x4 v = *(const u32x4*)(p.u + (size_t)(t0 + row) * D + lane * 8);
        *(u32x4*)(smem + row * RS + lane * 16) = v;
        float s = 0.f;
#pragma unroll
        for (int e = 0; e < 4; ++e) {
          const uint32_t pr = pk2(bflo(v[e]) * bflo(gwv[e]), bfhi(v[e]) * bfhi(gwv[e]));
          s += bflo(pr) + bfhi(pr);
        }
#pragma unroll
        for (int o = 32; o > 0; o >>= 1) s += __shfl_xor(s, o);
        if (lane == 0) { const float gs = rbf(s); p.g[t0 + row] = rbf(1.f / (1.f + __expf(-gs))); }
      }
    }
    __syncthreads();
    {
      const int g = lane >> 4, q = (lane & 15) >> 2, pp = lane & 3;
#pragma unroll
      for (int c = 0; c < 4; ++c)
#pragma unroll
        for (int hh = 0; hh < 2; ++hh) {
          const int dt_ = 4 * w + c;
          const int r0 = 32 * hh + 8 * g + q;
          const char* base = smem + (16 * dt_ + 4 * pp) * 2;
          const u32x4 v = tr_read2(base + r0 * RS, base + (r0 + 4) * RS);
          *(u32x4*)(p.uT + (size_t)(16 * dt_ + (lane & 15)) * L + t0 + 32 * hh + 8 * g) = v;
        }
    }
    __syncthreads();
    for (int ks = a; ks < KS2; ks += 32) {
#pragma unroll
      for (int r = 0; r < 4; ++r) {
        const int row = 4 * w + r;
        *(u32x4*)(smem + row * RS + lane * 16) = *(const u32x4*)(p.wd + (size_t)(32 * ks + row) * D + lane * 8);
      }
      __syncthreads();
      const int g = lane >> 4, q = (lane & 15) >> 2, pp = lane & 3;
#pragma unroll
      for (int c = 0; c < 4; ++c) {
        const int nt = 4 * w + c;
        const char* base = smem + (16 * nt + 4 * pp) * 2;
        const u32x4 v = tr_read2(base + (4 * g + q) * RS, base + (16 + 4 * g + q) * RS);
        *(u32x4*)(p.wdt + ((size_t)(nt * KS2 + ks) * 64 + lane) * 8) = v;
      }
      __syncthreads();
    }
  }
}

struct P1 {
  const u16 *u, *Dskip, *h, *wdt, *uT, *Kc;
  const float* g;
  u16 *yT, *out;
  unsigned* cnt;
};

constexpr int CP_STRIDE = 4160;
constexpr int CW_COPIES = 0, CW_U = 4 * CP_STRIDE, CW_E = CW_U + 4608, CW_KC = CW_E + 4096 + 32, CW_SIZE = CW_KC + 1344;
constexpr int K1_YOFF = 4 * ((CW_SIZE + 15) & ~15);
constexpr int K1_LDS = K1_YOFF + 64 * 144;
constexpr int RED_RS = 272;

__global__ __launch_bounds__(512, 2) void k1_kernel(P1 p) {
  extern __shared__ __attribute__((aligned(16))) char smem[];
  const int tid = threadIdx.x, lane = tid & 63, w = __builtin_amdgcn_readfirstlane(tid >> 6);
  const int b = blockIdx.x, xcd = b & 7, idx = b >> 3;
  const int mt = (xcd >> 1) * 8 + (idx >> 2), nt = (xcd & 1) * 4 + (idx & 3);
  const int t0 = mt * 64, d0 = nt * 64;
  f32x4 acc[4][4];
  if (w < 4) {
#pragma unroll
    for (int a = 0; a < 4; ++a)
#pragma unroll
      for (int c = 0; c < 4; ++c) acc[a][c] = (f32x4){0, 0, 0, 0};
    const u16* hp = p.h + ((size_t)(mt * 4) * KS2 + 14 * w) * 512 + lane * 8;
    const u16* wp = p.wdt + ((size_t)(nt * 4) * KS2 + 14 * w) * 512 + lane * 8;
    constexpr int PF = 4;
    u32x4 hf[PF][4], wf[PF][4];
#pragma unroll
    for (int s = 0; s < PF; ++s)
#pragma unroll
      for (int a = 0; a < 4; ++a) {
        hf[s][a] = *(const u32x4*)(hp + ((size_t)a * KS2 + s) * 512);
        wf[s][a] = *(const u32x4*)(wp + ((size_t)a * KS2 + s) * 512);
      }
#pragma unroll
    for (int s = 0; s < 14; ++s) {
#pragma unroll
      for (int a = 0; a < 4; ++a)
#pragma unroll
        for (int c = 0; c < 4; ++c) acc[a][c] = mfma16(wf[s % PF][a], hf[s % PF][c], acc[a][c]);
      if (s + PF < 14) {
#pragma unroll
        for (int a = 0; a < 4; ++a) {
          hf[s % PF][a] = *(const u32x4*)(hp + ((size_t)a * KS2 + s + PF) * 512);
          wf[s % PF][a] = *(const u32x4*)(wp + ((size_t)a * KS2 + s + PF) * 512);
        }
      }
    }
  } else {
    const int cw = w - 4, c = cw >> 1, half = cw & 1, d = 2 * b + c;
    char* base = smem + cw * ((CW_SIZE + 15) & ~15);
    {
      const u16* kc = p.Kc + (size_t)d * 1024;
      for (int i = lane; i < 84; i += 64) *(u32x4*)(base + CW_KC + i * 16) = *(const u32x4*)(kc + i * 8);
      const u16* ut = p.uT + (size_t)d * L;
#pragma unroll
      for (int i = 0; i < 4; ++i) *(u32x4*)(base + CW_U + 512 + (i * 64 + lane) * 16) = *(const u32x4*)(ut + (i * 64 + lane) * 8);
      if (lane < 32) *(u32x4*)(base + CW_U + lane * 16) = (u32x4){0, 0, 0, 0};
    }
    WAIT_L(0);
    {
      const u16* kc = (const u16*)(base + CW_KC);
      if (lane < 2) *(u32x4*)(base + CW_E + lane * 16) = (u32x4){0, 0, 0, 0};
#pragma unroll
      for (int it = 0; it < 4; ++it) {
        const int l0 = (it * 64 + lane) * 8;
        u32x4 o;
#pragma unroll
        for (int e = 0; e < 4; ++e) {
          const int la = l0 + 2 * e, lb = la + 1;
          const uint32_t pb = pk2((float)la, (float)lb);
          const int ia = la < 128 ? la : (int)(pb & 0xffffu) - 17024, ib = lb < 128 ? lb : (int)(pb >> 16) - 17024;
          o[e] = (uint32_t)kc[ia] | ((uint32_t)kc[ib] << 16);
        }
        *(u32x4*)(base + CW_E + 32 + l0 * 2) = o;
      }
    }
    WAIT_L(0);
    for (int un = lane; un < 258; un += 64) {
      const u32x4 cur = *(const u32x4*)(base + CW_E + un * 16);
      u32x4 prv = {0, 0, 0, 0};
      if (un > 0) prv = *(const u32x4*)(base + CW_E + un * 16 - 16);
      u32x4 o1, o2, o3;
      o1[0] = __builtin_amdgcn_alignbyte(cur[0], prv[3], 2); o1[1] = __builtin_amdgcn_alignbyte(cur[1], cur[0], 2);
      o1[2] = __builtin_amdgcn_alignbyte(cur[2], cur[1], 2); o1[3] = __builtin_amdgcn_alignbyte(cur[3], cur[2], 2);
      o2[0] = prv[3]; o2[1] = cur[0]; o2[2] = cur[1]; o2[3] = cur[2];
      o3[0] = __builtin_amdgcn_alignbyte(prv[3], prv[2], 2); o3[1] = o1[0]; o3[2] = o1[1]; o3[3] = o1[2];
      *(u32x4*)(base + 0 * CP_STRIDE + un * 16) = cur;
      *(u32x4*)(base + 1 * CP_STRIDE + un * 16) = o1;
      *(u32x4*)(base + 2 * CP_STRIDE + un * 16) = o2;
      *(u32x4*)(base + 3 * CP_STRIDE + un * 16) = o3;
    }
    WAIT_L(0);
    const int g = lane >> 4, q = (lane & 15) >> 2, pp = lane & 3, n = lane & 15;
    f32x4 y[4];
#pragma unroll
    for (int a = 0; a < 4; ++a) y[a] = (f32x4){0, 0, 0, 0};
    const char* abase = base + q * CP_STRIDE + (16 - 8 * g + 4 * pp) * 2;
    const char* bbase = base + CW_U + 512 + (16 * n + 8 * g) * 2;
#pragma unroll 2
    for (int r = 1; r < 16; r += 2) {
      u32x4 af[8], bf[8];
#pragma unroll
      for (int m = 0; m < 8; ++m) {
        const char* ap = abase + (16 * (16 * m + r)) * 2;
        af[m] = tr_read2(ap, ap - 8);
        bf[m] = *(const u32x4*)(bbase + (256 * m - 16 * r) * 2);
      }
      if (half == 0) {
        y[0] = mfma16(af[0], bf[0], y[0]);
#pragma unroll
        for (int m = 0; m <= 3; ++m) y[1] = mfma16(af[m], bf[3 - m], y[1]);
#pragma unroll
        for (int m = 0; m <= 4; ++m) y[2] = mfma16(af[m], bf[4 - m], y[2]);
#pragma unroll
        for (int m = 0; m <= 7; ++m) y[3] = mfma16(af[m], bf[7 - m], y[3]);
      } else {
#pragma unroll
        for (int m = 0; m <= 1; ++m) y[0] = mfma16(af[m], bf[1 - m], y[0]);
#pragma unroll
        for (int m = 0; m <= 2; ++m) y[1] = mfma16(af[m], bf[2 - m], y[1]);
#pragma unroll
        for (int m = 0; m <= 5; ++m) y[2] = mfma16(af[m], bf[5 - m], y[2]);
#pragma unroll
        for (int m = 0; m <= 6; ++m) y[3] = mfma16(af[m], bf[6 - m], y[3]);
      }
    }
#pragma unroll
    for (int a = 0; a < 4; ++a) {
      const int ntile = half == 0 ? (a == 0 ? 0 : a == 1 ? 3 : a == 2 ? 4 : 7) : (a == 0 ? 1 : a == 1 ? 2 : a == 2 ? 5 : 6);
      const unsigned long long v = (unsigned long long)pk2(y[a][0], y[a][1]) | ((unsigned long long)pk2(y[a][2], y[a][3]) << 32);
      __hip_atomic_store((gu64*)(p.yT + (size_t)d * L + 256 * ntile + 16 * n + 4 * g), v, RLX_AGENT);
    }
    WAIT_V(0);
    if (lane == 0) __hip_atomic_fetch_add((gu32*)(p.cnt + (d >> 6) * 64), 1u, RLX_AGENT);
    if (cw == 0) {
      for (unsigned spins = 0; spins < (1u << 22); ++spins) {
        if ((unsigned)__builtin_amdgcn_readfirstlane(__hip_atomic_load((gu32*)(p.cnt + nt * 64), RLX_AGENT)) >= 128u) break;
        __builtin_amdgcn_s_sleep(2);
      }
    }
  }
  __syncthreads();
  {
    const int dd = tid >> 3, ch = tid & 7;
    const gu64* src = (const gu64*)(p.yT + (size_t)(d0 + dd) * L + t0 + ch * 8);
    const unsigned long long v0 = __hip_atomic_load(src, RLX_AGENT), v1 = __hip_atomic_load(src + 1, RLX_AGENT);
    *(unsigned long long*)(smem + K1_YOFF + dd * 144 + ch * 16) = v0;
    *(unsigned long long*)(smem + K1_YOFF + dd * 144 + ch * 16 + 8) = v1;
  }
  if (w < 4) {
    const int g = lane >> 4, n = lane & 15;
#pragma unroll
    for (int a = 0; a < 4; ++a)
#pragma unroll
      for (int c = 0; c < 4; ++c) *(f32x4*)(smem + (w * 64 + 16 * c + n) * RED_RS + (16 * a + 4 * g) * 4) = acc[a][c];
  }
  __syncthreads();
  {
    const int g = lane >> 4, q = (lane & 15) >> 2, pp = lane & 3;
    const int tl = 16 * (w & 3) + (lane & 15), dc = 4 * (w >> 2) + g;
    float bs[8];
#pragma unroll
    for (int e = 0; e < 8; ++e) bs[e] = 0.f;
#pragma unroll
    for (int ww = 0; ww < 4; ++ww) {
      const f32x4 v0 = *(const f32x4*)(smem + (ww * 64 + tl) * RED_RS + dc * 32), v1 = *(const f32x4*)(smem + (ww * 64 + tl) * RED_RS + dc * 32 + 16);
#pragma unroll
      for (int e = 0; e < 4; ++e) { bs[e] += v0[e]; bs[4 + e] += v1[e]; }
    }
    const char* yb = smem + K1_YOFF + (8 * dc + q) * 144 + (16 * (w & 3) + 4 * pp) * 2;
    const u32x4 yv = tr_read2(yb, yb + 4 * 144);
    const u32x4 uv = *(const u32x4*)(p.u + (size_t)(t0 + tl) * D + d0 + 8 * dc);
    const u32x4 dv = *(const u32x4*)(p.Dskip + d0 + 8 * dc);
    const float gt = p.g[t0 + tl], omg = rbf(1.f - gt);
    u32x4 o;
#pragma unroll
    for (int e = 0; e < 4; ++e) {
      const uint32_t sk = pk2(bflo(uv[e]) * bflo(dv[e]), bfhi(uv[e]) * bfhi(dv[e]));
      const uint32_t av = pk2(bflo(yv[e]) + bflo(sk), bfhi(yv[e]) + bfhi(sk));
      const uint32_t ga = pk2(gt * bflo(av), gt * bfhi(av));
      const uint32_t bb = pk2(bs[2 * e], bs[2 * e + 1]);
      const uint32_t gb = pk2(omg * bflo(bb), omg * bfhi(bb));
      o[e] = pk2(bflo(ga) + bflo(gb), bfhi(ga) + bfhi(gb));
    }
    *(u32x4*)(p.out + (size_t)(t0 + tl) * D + d0 + 8 * dc) = o;
  }
}

extern "C" void kernel_launch(void* const* d_in, const int* in_sizes, int n_in, void* d_out, int out_size, void* d_ws, size_t ws_size,
                              hipStream_t stream) {
  char* ws = (char*)d_ws;
  P0 p0;
  p0.u = (const u16*)d_in[0]; p0.Are = (const u16*)d_in[1]; p0.Aim = (const u16*)d_in[2]; p0.Bre = (const u16*)d_in[3];
  p0.Bim = (const u16*)d_in[4]; p0.Cre = (const u16*)d_in[5]; p0.Cim = (const u16*)d_in[6]; p0.logdt = (const u16*)d_in[7];
  p0.wg = (const u16*)d_in[10]; p0.wu = (const u16*)d_in[11]; p0.wd = (const u16*)d_in[12]; p0.gw = (const u16*)d_in[13];
  p0.h = (u16*)(ws + WS_H); p0.wdt = (u16*)(ws + WS_WDT); p0.uT = (u16*)(ws + WS_UT); p0.Kc = (u16*)(ws + WS_KC);
  p0.g = (float*)(ws + WS_G); p0.cnt = (unsigned*)(ws + WS_CNT);
  P1 p1;
  p1.u = p0.u; p1.Dskip = (const u16*)d_in[8]; p1.h = p0.h; p1.wdt = p0.wdt; p1.uT = p0.uT; p1.Kc = p0.Kc; p1.g = p0.g;
  p1.yT = (u16*)(ws + WS_YT); p1.out = (u16*)d_out; p1.cnt = p0.cnt;
  (void)hipFuncSetAttribute((const void*)k0_kernel, hipFuncAttributeMaxDynamicSharedMemorySize, K0_LDS);
  (void)hipFuncSetAttribute((const void*)k1_kernel, hipFuncAttributeMaxDynamicSharedMemorySize, K1_LDS);
  k0_kernel<<<256, 512, K0_LDS, stream>>>(p0);
  k1_kernel<<<256, 512, K1_LDS, stream>>>(p1);
}
```

```cpp
#include <hip/hip_runtime.h>
#include <hip/hip_bf16.h>
#include <stdint.h>

typedef unsigned short u16;
typedef __attribute__((ext_vector_type(4))) short s16x4;
typedef __attribute__((ext_vector_type(8))) short s16x8;
typedef __attribute__((ext_vector_type(8))) __bf16 bf16x8;
typedef __attribute__((ext_vector_type(2))) __bf16 bf16x2;
typedef __attribute__((ext_vector_type(2))) float f32x2;
typedef __attribute__((ext_vector_type(4))) float f32x4;
typedef __attribute__((ext_vector_type(4))) unsigned u32x4;
typedef __attribute__((ext_vector_type(2))) unsigned u32x2;
typedef __attribute__((address_space(3))) s16x4* lds_s16x4_ptr;
typedef __attribute__((address_space(1))) unsigned gu32;
typedef __attribute__((address_space(1))) unsigned long long gu64;
#define RLX_AGENT __ATOMIC_RELAXED, __HIP_MEMORY_SCOPE_AGENT
#define DEV __device__ __forceinline__

constexpr int L = 2048, D = 512, NM = 128, F = 1792;
constexpr int KS2 = F / 32;

constexpr size_t WS_H = 0;
constexpr size_t WS_WDT = WS_H + (size_t)L * F * 2;
constexpr size_t WS_UT = WS_WDT + (size_t)F * D * 2;
constexpr size_t WS_YT = WS_UT + (size_t)L * D * 2;
constexpr int ES = 2080;
constexpr size_t WS_E = WS_YT + (size_t)L * D * 2 + 64;
constexpr size_t WS_G = WS_E + (size_t)D * ES * 2;

#define TSDECL
#define TS(ph)
#define TSDUMP(K)
DEV float bf2f(u16 v) { return __uint_as_float(((uint32_t)v) << 16); }
DEV float bflo(uint32_t v) { return __uint_as_float(v << 16); }
DEV float bfhi(uint32_t v) { return __uint_as_float(v & 0xffff0000u); }
DEV uint32_t pk2(float a, float b) { f32x2 v = {a, b}; return __builtin_bit_cast(uint32_t, __builtin_convertvector(v, bf16x2)); }
DEV float rbf(float a) { return bflo(pk2(a, 0.f) & 0xffffu); }
DEV f32x4 mfma16(u32x4 a, u32x4 b, f32x4 c) {
  return __builtin_amdgcn_mfma_f32_16x16x32_bf16(__builtin_bit_cast(bf16x8, a), __builtin_bit_cast(bf16x8, b), c, 0, 0, 0);
}
DEV u32x2 tr_read(const char* p) {
  return __builtin_bit_cast(u32x2, __builtin_amdgcn_ds_read_tr16_b64_v4i16((lds_s16x4_ptr)p));
}
DEV u32x4 tr_read2(const char* p0, const char* p1) {
  u32x2 a = tr_read(p0), b = tr_read(p1);
  u32x4 r = {a.x, a.y, b.x, b.y};
  return r;
}
DEV uint32_t lds_addr(const void* p) { return (uint32_t)(uintptr_t)(__attribute__((address_space(3))) const char*)p; }
DEV void glds16(const void* sbase, uint32_t voff, uint32_t lds) {
  unsigned keep;
  asm volatile("s_mov_b32 %0, m0\n\ts_mov_b32 m0, %3\n\ts_nop 0\n\tglobal_load_lds_dwordx4 %1, %2\n\ts_mov_b32 m0, %0"
               : "=&s"(keep) : "v"(voff), "s"(sbase), "s"(lds) : "memory");
}
#define WAIT_V(n) asm volatile("s_waitcnt vmcnt(" #n ")" ::: "memory")
#define WAIT_L(n) asm volatile("s_waitcnt lgkmcnt(" #n ")" ::: "memory")
#define BAR() __builtin_amdgcn_s_barrier()

struct P0 {
  const u16 *u, *Are, *Aim, *Bre, *Bim, *Cre, *Cim, *logdt, *wg, *wu, *wd, *gw;
  u16 *h, *wdt, *uT, *E, *yT;
  float* g;
};

constexpr int STAGE_B = 49152;
constexpr int K0_LDS = 3 * STAGE_B;

DEV int wswz(int row) { return ((row & 3) << 2) | ((row >> 2) & 3); }

struct SsmPar { u32x4 are, aim, bre, bim, cre, cim; };

DEV void ssm_load(const P0& p, int ch0, int w, int lane, SsmPar& s) {
  const uint32_t off = (uint32_t)((ch0 + (w >> 2)) * NM + 32 * (w & 3) + 8 * (lane >> 4)) * 2u;
  asm volatile("global_load_dwordx4 %0, %1, %2" : "=&v"(s.are) : "v"(off), "s"(p.Are) : "memory");
  asm volatile("global_load_dwordx4 %0, %1, %2" : "=&v"(s.aim) : "v"(off), "s"(p.Aim) : "memory");
  asm volatile("global_load_dwordx4 %0, %1, %2" : "=&v"(s.bre) : "v"(off), "s"(p.Bre) : "memory");
  asm volatile("global_load_dwordx4 %0, %1, %2" : "=&v"(s.bim) : "v"(off), "s"(p.Bim) : "memory");
  asm volatile("global_load_dwordx4 %0, %1, %2" : "=&v"(s.cre) : "v"(off), "s"(p.Cre) : "memory");
  asm volatile("global_load_dwordx4 %0, %1, %2" : "=&v"(s.cim) : "v"(off), "s"(p.Cim) : "memory");
}
#define SSM_WAIT(n, s) asm volatile("s_waitcnt vmcnt(" #n ")" : "+v"(s.are), "+v"(s.aim), "+v"(s.bre), "+v"(s.bim), "+v"(s.cre), "+v"(s.cim)::"memory")

DEV void kgen(const P0& p, const SsmPar& sp, float dt, int ch0, char* scratch, int tid) {
  const int w = tid >> 6, lane = tid & 63, i = lane & 15;
  const float p0 = (float)((i & 7) + 128 * (i >> 3));
  const float p1 = (i < 8) ? (float)(256 + 2 * (i & 3) + 128 * (i >> 2)) : (float)(512 + 4 * (i & 1) + 128 * ((i - 8) >> 1));
  const float p2 = (i < 8) ? (float)(1024 + 128 * i) : 2048.f;
  const float q = (float)(8 * i);
  f32x4 acc[3] = {{0, 0, 0, 0}, {0, 0, 0, 0}, {0, 0, 0, 0}};
#pragma unroll
  for (int kk = 0; kk < 2; ++kk) {
    u32x4 ah[3], al[3], bh, bl;
#pragma unroll
    for (int j = 0; j < 4; ++j) {
      const int wi_ = 2 * kk + (j >> 1);
      const uint32_t wa = sp.are[wi_], wi = sp.aim[wi_], wbr = sp.bre[wi_], wbi = sp.bim[wi_], wcr = sp.cre[wi_], wci = sp.cim[wi_];
      const float ar = dt * ((j & 1) ? bfhi(wa) : bflo(wa)), ai = dt * ((j & 1) ? bfhi(wi) : bflo(wi));
      const float br = dt * ((j & 1) ? bfhi(wbr) : bflo(wbr)), bi = dt * ((j & 1) ? bfhi(wbi) : bflo(wbi));
      const float cr = (j & 1) ? bfhi(wcr) : bflo(wcr), ci = (j & 1) ? bfhi(wci) : bflo(wci);
      const float a2 = ar * 1.44269504088896340736f, wv = ai * 0.15915494309189533577f;
      const float xr = cr * br - ci * bi, xi = cr * bi + ci * br;
      {
        const float e = __builtin_amdgcn_exp2f(a2 * q), r = __builtin_amdgcn_fractf(wv * q);
        const float s = __builtin_amdgcn_sinf(r) * e, co = __builtin_amdgcn_cosf(r) * e;
        const float vr = xr * co - xi * s, vi = -(xr * s + xi * co);
        const uint32_t hi = pk2(vr, vi);
        bh[j] = hi;
        bl[j] = pk2(vr - bflo(hi), vi - bfhi(hi));
      }
#pragma unroll
      for (int T = 0; T < 3; ++T) {
        const float pp = T == 0 ? p0 : (T == 1 ? p1 : p2);
        const float e = __builtin_amdgcn_exp2f(a2 * pp), r = __builtin_amdgcn_fractf(wv * pp);
        const float wr = __builtin_amdgcn_cosf(r) * e, wim = __builtin_amdgcn_sinf(r) * e;
        const uint32_t hi = pk2(wr, wim);
        ah[T][j] = hi;
        al[T][j] = pk2(wr - bflo(hi), wim - bfhi(hi));
      }
    }
#pragma unroll
    for (int T = 0; T < 3; ++T) {
      acc[T] = mfma16(al[T], bh, acc[T]);
      acc[T] = mfma16(ah[T], bl, acc[T]);
      acc[T] = mfma16(ah[T], bh, acc[T]);
    }
  }
  float* sc = (float*)scratch;
  u16* kc = (u16*)(scratch + 24576);
#pragma unroll
  for (int T = 0; T < 3; ++T) *(f32x4*)(sc + ((w * 3 + T) * 64 + lane) * 4) = acc[T];
  WAIT_L(0);
  BAR();
  {
    const int cc = tid >> 8, pos = tid & 255, ln = pos >> 2, j = pos & 3;
    const int ii = 4 * (ln >> 4) + j, b = ln & 15;
#pragma unroll
    for (int T = 0; T < 3; ++T) {
      float s = 0.f;
#pragma unroll
      for (int ww = 0; ww < 4; ++ww) s += sc[((cc * 4 + ww) * 3 + T) * 256 + pos];
      int idx;
      if (T == 0) idx = (ii & 7) + 128 * (ii >> 3) + 8 * b;
      else if (T == 1) idx = ii < 8 ? 256 + (ii & 3) + 64 * (ii >> 2) + 4 * b : 384 + (ii & 1) + 32 * ((ii - 8) >> 1) + 2 * b;
      else idx = ii < 8 ? 512 + 16 * ii + b : ((ii == 8 && b == 0) ? 640 : 700);
      kc[cc * 768 + idx] = (u16)(pk2(s, 0.f) & 0xffffu);
    }
  }
  WAIT_L(0);
  BAR();
  {
    const int cc = tid >> 8, un = tid & 255, l0 = 8 * un;
    const u16* k = kc + cc * 768;
    u32x4 o;
#pragma unroll
    for (int e = 0; e < 4; ++e) {
      const int la = l0 + 2 * e, lb = la + 1;
      const uint32_t pb = pk2((float)la, (float)lb);
      const int ia = la < 128 ? la : (int)(pb & 0xffffu) - 17024, ib = lb < 128 ? lb : (int)(pb >> 16) - 17024;
      o[e] = (uint32_t)k[ia] | ((uint32_t)k[ib] << 16);
    }
    u16* dst = p.E + (size_t)(ch0 + cc) * ES;
    *(u32x4*)(dst + 16 + l0) = o;
    if (un < 2) *(u32x4*)(dst + 8 * un) = (u32x4){0, 0, 0, 0};
  }
}

DEV void k0_glds(const P0& p, uint32_t lb, const uint32_t (&voff)[3], int m0, int f0, int k0, int piece) {
  if (piece < 2) glds16(p.u + (size_t)m0 * D + k0, voff[0] + piece * (8 * D * 2), lb + piece * 1024);
  else if (piece < 4) glds16(p.wg + (size_t)k0 * F + f0, voff[piece - 1], lb + 16384 + (piece - 2) * 1024);
  else glds16(p.wu + (size_t)k0 * F + f0, voff[piece - 3], lb + 32768 + (piece - 4) * 1024);
}

__global__ __launch_bounds__(512, 2) void k0_kernel(P0 p) {
  extern __shared__ __attribute__((aligned(16))) char smem[];
  const int tid = threadIdx.x, lane = tid & 63, w = __builtin_amdgcn_readfirstlane(tid >> 6);
  const int b = blockIdx.x, xcd = b & 7, idx = b >> 3;
  TSDECL;
  TS(0);
  const uint32_t ld2 = *(const uint32_t*)(p.logdt + 2 * b);
  if (idx < 28) {
    SsmPar sp;
    ssm_load(p, 2 * b, w, lane, sp);
    const int mt = (xcd >> 1) * 4 + idx / 7, ft = (xcd & 1) * 7 + idx % 7;
    const int m0 = mt * 128, f0 = ft * 128;
    const int rot = 2 * (idx / 7) + idx % 7;
    uint32_t voff[3];
    {
      const int row = 16 * w + (lane >> 3);
      voff[0] = (uint32_t)(row * D + 8 * ((lane & 7) ^ (row & 7))) * 2u;
      const int r0 = 8 * w + (lane >> 4), r1 = r0 + 4;
      voff[1] = (uint32_t)(r0 * F + 8 * ((lane & 15) ^ wswz(r0))) * 2u;
      voff[2] = (uint32_t)(r1 * F + 8 * ((lane & 15) ^ wswz(r1))) * 2u;
    }
    const uint32_t lb = lds_addr(smem) + 2048 * w;
#pragma unroll
    for (int s = 0; s < 2; ++s)
#pragma unroll
      for (int pc = 0; pc < 6; ++pc) k0_glds(p, lb + s * STAGE_B, voff, m0, f0, 64 * ((rot + s) & 7), pc);
    const float dt = rbf(__expf((w >> 2) ? bfhi(ld2) : bflo(ld2)));
    *(u32x4*)(p.yT + (size_t)b * 4096 + tid * 8) = (u32x4){~0u, ~0u, ~0u, ~0u};
    SSM_WAIT(13, sp);
    TS(1);
    kgen(p, sp, dt, 2 * b, smem + 2 * STAGE_B, tid);
    WAIT_L(0);
    BAR();
    TS(2);
#pragma unroll
    for (int pc = 0; pc < 6; ++pc) k0_glds(p, lb + 2 * STAGE_B, voff, m0, f0, 64 * ((rot + 2) & 7), pc);
    const int wm = w >> 2, wn = w & 3, g = lane >> 4, q = (lane & 15) >> 2, pp = lane & 3;
    f32x4 acc[2][2][4];
#pragma unroll
    for (int a = 0; a < 2; ++a)
#pragma unroll
      for (int c = 0; c < 2; ++c)
#pragma unroll
        for (int m = 0; m < 4; ++m) acc[a][c][m] = (f32x4){0, 0, 0, 0};
    u32x4 xf[2][4], wf[2][2][2];
    auto frags = [&](const char* buf, int ss) {
#pragma unroll
      for (int m = 0; m < 4; ++m) {
        const int row = 64 * wm + 16 * m + (lane & 15), c = 4 * ss + g;
        xf[ss][m] = *(const u32x4*)(buf + row * 128 + ((c ^ (row & 7)) << 4));
      }
#pragma unroll
      for (int a = 0; a < 2; ++a)
#pragma unroll
        for (int c = 0; c < 2; ++c) {
          const int ch = 4 * wn + 2 * c + (pp >> 1);
          const int r0 = 32 * ss + 8 * g + q, r1 = r0 + 4;
          const char* wb = buf + 16384 + a * 16384 + 8 * (pp & 1);
          wf[ss][a][c] = tr_read2(wb + 256 * r0 + 16 * (ch ^ wswz(r0)), wb + 256 * r1 + 16 * (ch ^ wswz(r1)));
        }
    };
    auto mm = [&](int ss, bool stage, int sb, int k0, int pc0) {
#pragma unroll
      for (int ac = 0; ac < 4; ++ac) {
#pragma unroll
        for (int m = 0; m < 4; ++m) acc[ac >> 1][ac & 1][m] = mfma16(wf[ss][ac >> 1][ac & 1], xf[ss][m], acc[ac >> 1][ac & 1][m]);
        if (stage && ac < 3) {
          __builtin_amdgcn_sched_barrier(0);
          k0_glds(p, lb + sb * STAGE_B, voff, m0, f0, k0, pc0 + ac);
          __builtin_amdgcn_sched_barrier(0);
        }
      }
    };
#pragma unroll
    for (int s = 0; s < 8; ++s) {
      if (s == 7) WAIT_V(0); else WAIT_V(6);
      WAIT_L(0);
      BAR();
      TS(3 + s);
      const char* buf = smem + (s % 3) * STAGE_B;
      const bool st = (s >= 1 && s + 2 < 8);
      const int k0 = 64 * ((rot + s + 2) & 7);
      frags(buf, 0);
      __builtin_amdgcn_sched_barrier(0);
      if (s > 0) mm(1, st, (s + 2) % 3, k0, 0);
      __builtin_amdgcn_sched_barrier(0);
      frags(buf, 1);
      __builtin_amdgcn_sched_barrier(0);
      mm(0, st, (s + 2) % 3, k0, 3);
      __builtin_amdgcn_sched_barrier(0);
    }
    mm(1, false, 0, 0, 0);
    TS(11);
#pragma unroll
    for (int m = 0; m < 4; ++m) {
      u32x4 o;
#pragma unroll
      for (int c = 0; c < 2; ++c)
#pragma unroll
        for (int jj = 0; jj < 2; ++jj) {
          const uint32_t g2 = pk2(acc[0][c][m][2 * jj], acc[0][c][m][2 * jj + 1]), u2 = pk2(acc[1][c][m][2 * jj], acc[1][c][m][2 * jj + 1]);
          const float g0 = bflo(g2), g1 = bfhi(g2);
          const uint32_t s2 = pk2(g0 * __builtin_amdgcn_rcpf(1.f + __expf(-g0)), g1 * __builtin_amdgcn_rcpf(1.f + __expf(-g1)));
          o[2 * c + jj] = pk2(bflo(s2) * bflo(u2), bfhi(s2) * bfhi(u2));
        }
      const int mtile = (m0 + 64 * wm + 16 * m) >> 4, ks = 4 * ft + wn;
      *(u32x4*)(p.h + ((size_t)(mtile * KS2 + ks) * 64 + lane) * 8) = o;
    }
    TS(12);
  } else {
    SsmPar sp;
    ssm_load(p, 2 * b, w, lane, sp);
    const int a = xcd * 4 + (idx - 28);
    constexpr int RS = 1056;
    const int t0 = 64 * a;
    u32x4 uv[8], wv[8];
#pragma unroll
    for (int r = 0; r < 8; ++r) uv[r] = *(const u32x4*)(p.u + (size_t)(t0 + 8 * w + r) * D + lane * 8);
    const int ks2 = a + 32 < KS2 ? a + 32 : a;
#pragma unroll
    for (int r = 0; r < 4; ++r) {
      wv[r] = *(const u32x4*)(p.wd + (size_t)(32 * a + 4 * w + r) * D + lane * 8);
      wv[4 + r] = *(const u32x4*)(p.wd + (size_t)(32 * ks2 + 4 * w + r) * D + lane * 8);
    }
    const u32x4 gwv = *(const u32x4*)(p.gw + lane * 8);
    const float dt = rbf(__expf((w >> 2) ? bfhi(ld2) : bflo(ld2)));
    *(u32x4*)(p.yT + (size_t)b * 4096 + tid * 8) = (u32x4){~0u, ~0u, ~0u, ~0u};
    SSM_WAIT(18, sp);
    TS(1);
    kgen(p, sp, dt, 2 * b, smem + 2 * STAGE_B, tid);
    TS(2);
    {
      float s[8];
#pragma unroll
      for (int r = 0; r < 8; ++r) {
        *(u32x4*)(smem + (8 * w + r) * RS + lane * 16) = uv[r];
        s[r] = 0.f;
#pragma unroll
        for (int e = 0; e < 4; ++e) {
          const uint32_t pr = pk2(bflo(uv[r][e]) * bflo(gwv[e]), bfhi(uv[r][e]) * bfhi(gwv[e]));
          s[r] += bflo(pr) + bfhi(pr);
        }
      }
#pragma unroll
      for (int r = 0; r < 4; ++r) { const bool hi = lane & 32; const float keep = hi ? s[r + 4] : s[r], send = hi ? s[r] : s[r + 4]; s[r] = keep + __shfl_xor(send, 32); }
#pragma unroll
      for (int r = 0; r < 2; ++r) { const bool hi = lane & 16; const float keep = hi ? s[r + 2] : s[r], send = hi ? s[r] : s[r + 2]; s[r] = keep + __shfl_xor(send, 16); }
      { const bool hi = lane & 8; const float keep = hi ? s[1] : s[0], send = hi ? s[0] : s[1]; s[0] = keep + __shfl_xor(send, 8); }
      s[0] += __shfl_xor(s[0], 4); s[0] += __shfl_xor(s[0], 2); s[0] += __shfl_xor(s[0], 1);
      if ((lane & 7) == 0) {
        const int r = ((lane >> 5) & 1) * 4 + ((lane >> 4) & 1) * 2 + ((lane >> 3) & 1);
        const float gs = rbf(s[0]);
        p.g[t0 + 8 * w + r] = rbf(1.f / (1.f + __expf(-gs)));
      }
    }
    WAIT_L(0);
    BAR();
    const int g = lane >> 4, q = (lane & 15) >> 2, pp = lane & 3;
#pragma unroll
    for (int c = 0; c < 4; ++c)
#pragma unroll
      for (int hh = 0; hh < 2; ++hh) {
        const int dt_ = 4 * w + c;
        const int r0 = 32 * hh + 8 * g + q;
        const char* base = smem + (16 * dt_ + 4 * pp) * 2;
        const u32x4 v = tr_read2(base + r0 * RS, base + (r0 + 4) * RS);
        *(u32x4*)(p.uT + (size_t)(16 * dt_ + (lane & 15)) * L + t0 + 32 * hh + 8 * g) = v;
      }
    TS(3);
    WAIT_L(0);
    BAR();
#pragma unroll
    for (int r = 0; r < 4; ++r) {
      *(u32x4*)(smem + (4 * w + r) * RS + lane * 16) = wv[r];
      *(u32x4*)(smem + (32 + 4 * w + r) * RS + lane * 16) = wv[4 + r];
    }
    WAIT_L(0);
    BAR();
#pragma unroll
    for (int hh = 0; hh < 2; ++hh) {
      if (hh == 0 || a + 32 < KS2) {
        const int ks = a + 32 * hh;
#pragma unroll
        for (int c = 0; c < 4; ++c) {
          const int nt = 4 * w + c;
          const char* base = smem + 32 * hh * RS + (16 * nt + 4 * pp) * 2;
          const u32x4 v = tr_read2(base + (4 * g + q) * RS, base + (16 + 4 * g + q) * RS);
          *(u32x4*)(p.wdt + ((size_t)(nt * KS2 + ks) * 64 + lane) * 8) = v;
        }
      }
    }
    TS(12);
  }
  TSDUMP(0);
}

struct P1 {
  const u16 *u, *Dskip, *h, *wdt, *uT, *E;
  const float* g;
  u16 *yT, *out;
};

constexpr int CP_STRIDE = 4160;
constexpr int CW_U = 4 * CP_STRIDE, CW_SIZE = CW_U + 4608;
constexpr int K1_YOFF = 4 * CW_SIZE;
constexpr int K1_RED = K1_YOFF + 64 * 144;
constexpr int K1_LDS = K1_RED + 49152;

template <int HALF>
DEV void conv_loop(const char* abase, const char* bbase, f32x4 (&y)[4]) {
  constexpr int NM_ = HALF == 0 ? 8 : 7;
  u32x4 af[2][8], bf[2][8];
  auto ld = [&](int buf, int r) {
#pragma unroll
    for (int m = 0; m < NM_; ++m) {
      const char* ap = abase + (16 * (16 * m + r)) * 2;
      af[buf][m] = tr_read2(ap, ap - 8);
      bf[buf][m] = *(const u32x4*)(bbase + (256 * m - 16 * r) * 2);
    }
  };
  ld(0, 1);
#pragma unroll
  for (int it = 0; it < 8; ++it) {
    const int cur = it & 1;
    __builtin_amdgcn_sched_barrier(0);
    if (it < 7) ld(cur ^ 1, 2 * it + 3);
    __builtin_amdgcn_sched_barrier(0);
    constexpr int T0 = HALF == 0 ? 0 : 1, T1 = HALF == 0 ? 3 : 2, T2 = HALF == 0 ? 4 : 5, T3 = HALF == 0 ? 7 : 6;
#pragma unroll
    for (int m = 0; m <= T3; ++m) {
      y[3] = mfma16(af[cur][m], bf[cur][T3 - m], y[3]);
      if (m <= T2) y[2] = mfma16(af[cur][m], bf[cur][T2 - m], y[2]);
      if (m <= T1) y[1] = mfma16(af[cur][m], bf[cur][T1 - m], y[1]);
      if (m <= T0) y[0] = mfma16(af[cur][m], bf[cur][T0 - m], y[0]);
    }
  }
}

__global__ __launch_bounds__(512, 2) void k1_kernel(P1 p) {
  extern __shared__ __attribute__((aligned(16))) char smem[];
  const int tid = threadIdx.x, lane = tid & 63, w = __builtin_amdgcn_readfirstlane(tid >> 6);
  const int b = blockIdx.x, xcd = b & 7, idx = b >> 3;
  const int mt = (xcd >> 1) * 8 + (idx >> 2), nt = (xcd & 1) * 4 + (idx & 3);
  const int t0 = mt * 64, d0 = nt * 64;
  const int g = lane >> 4, n = lane & 15;
  TSDECL;
  TS(0);
  if (w < 4) {
    f32x4 acc[4][4];
#pragma unroll
    for (int a = 0; a < 4; ++a)
#pragma unroll
      for (int c = 0; c < 4; ++c) acc[a][c] = (f32x4){0, 0, 0, 0};
    const u16* hp = p.h + ((size_t)(mt * 4) * KS2 + 14 * w) * 512 + lane * 8;
    const u16* wp = p.wdt + ((size_t)(nt * 4) * KS2 + 14 * w) * 512 + lane * 8;
    constexpr int PF = 4;
    const int rot = ((idx & 3) * 7 / 2 + (idx >> 2) * 7 / 4) % 14;
    u32x4 hf[PF][4], wf[PF][4];
#pragma unroll
    for (int s = 0; s < PF; ++s)
#pragma unroll
      for (int a = 0; a < 4; ++a) {
        const int ks = (rot + s) >= 14 ? rot + s - 14 : rot + s;
        hf[s][a] = *(const u32x4*)(hp + ((size_t)((a + w) & 3) * KS2 + ks) * 512);
        wf[s][a] = *(const u32x4*)(wp + ((size_t)a * KS2 + ks) * 512);
      }
    const int t = t0 + 16 * w + n;
    u32x2 uv[4], dv[4];
#pragma unroll
    for (int a = 0; a < 4; ++a) {
      uv[a] = *(const u32x2*)(p.u + (size_t)t * D + d0 + 16 * a + 4 * g);
      dv[a] = *(const u32x2*)(p.Dskip + d0 + 16 * a + 4 * g);
    }
    const float gt = p.g[t];
    __builtin_amdgcn_sched_barrier(0);
#pragma unroll
    for (int s = 0; s < 14; ++s) {
#pragma unroll
      for (int a = 0; a < 4; ++a)
#pragma unroll
        for (int c = 0; c < 4; ++c) acc[a][c] = mfma16(wf[s % PF][a], hf[s % PF][c], acc[a][c]);
      __builtin_amdgcn_sched_barrier(0);
      if (s + PF < 14) {
#pragma unroll
        for (int a = 0; a < 4; ++a) {
          const int ks = (rot + s + PF) >= 14 ? rot + s + PF - 14 : rot + s + PF;
          hf[s % PF][a] = *(const u32x4*)(hp + ((size_t)((a + w) & 3) * KS2 + ks) * 512);
          wf[s % PF][a] = *(const u32x4*)(wp + ((size_t)a * KS2 + ks) * 512);
        }
      }
      __builtin_amdgcn_sched_barrier(0);
    }
    TS(1);
#pragma unroll
    for (int c = 1; c < 4; ++c)
#pragma unroll
      for (int a = 0; a < 4; ++a) *(f32x4*)(smem + K1_RED + ((((w + c) & 3) * 3 + (c - 1)) * 4 + a) * 1024 + lane * 16) = acc[a][c];
    WAIT_L(0);
    BAR();
    TS(7);
    const float omg = rbf(1.f - gt);
    const int q = n >> 2, pp = lane & 3;
#pragma unroll
    for (int a = 0; a < 4; ++a) {
      f32x4 bs = acc[a][0];
#pragma unroll
      for (int s = 0; s < 3; ++s) bs += *(const f32x4*)(smem + K1_RED + ((w * 3 + s) * 4 + a) * 1024 + lane * 16);
      const u32x2 yv = tr_read(smem + K1_YOFF + (16 * a + 4 * g + q) * 144 + (16 * w + 4 * pp) * 2);
      u32x2 o;
#pragma unroll
      for (int e = 0; e < 2; ++e) {
        const uint32_t sk = pk2(bflo(uv[a][e]) * bflo(dv[a][e]), bfhi(uv[a][e]) * bfhi(dv[a][e]));
        const uint32_t av = pk2(bflo(yv[e]) + bflo(sk), bfhi(yv[e]) + bfhi(sk));
        const uint32_t ga = pk2(gt * bflo(av), gt * bfhi(av));
        const uint32_t bb = pk2(bs[2 * e], bs[2 * e + 1]);
        const uint32_t gb = pk2(omg * bflo(bb), omg * bfhi(bb));
        o[e] = pk2(bflo(ga) + bflo(gb), bfhi(ga) + bfhi(gb));
      }
      *(u32x2*)(p.out + (size_t)t * D + d0 + 16 * a + 4 * g) = o;
    }
    TS(9);
  } else {
    const int cw = w - 4, c = cw >> 1, half = cw & 1, d = 2 * b + c;
    char* base = smem + cw * CW_SIZE;
    {
      const u16* eg = p.E + (size_t)d * ES;
      u32x4 cur[5];
      u32x2 prv[5];
#pragma unroll
      for (int it = 0; it < 5; ++it) {
        const int un = it < 4 ? it * 64 + lane : 256 + (lane & 1);
        cur[it] = *(const u32x4*)(eg + un * 8);
        prv[it] = *(const u32x2*)(eg + un * 8 - 4);
      }
      const uint32_t lb = lds_addr(base + CW_U + 512);
#pragma unroll
      for (int i = 0; i < 4; ++i) glds16(p.uT + (size_t)d * L + i * 512, lane * 16u, lb + i * 1024);
      if (lane < 32) *(u32x4*)(base + CW_U + lane * 16) = (u32x4){0, 0, 0, 0};
#pragma unroll
      for (int it = 0; it < 5; ++it) {
        const int un = it < 4 ? it * 64 + lane : 256 + (lane & 1);
        const u32x4 cu = cur[it];
        const u32x2 pv = prv[it];
        u32x4 o1, o2, o3;
        o1[0] = __builtin_amdgcn_alignbyte(cu[0], pv[1], 2); o1[1] = __builtin_amdgcn_alignbyte(cu[1], cu[0], 2);
        o1[2] = __builtin_amdgcn_alignbyte(cu[2], cu[1], 2); o1[3] = __builtin_amdgcn_alignbyte(cu[3], cu[2], 2);
        o2[0] = pv[1]; o2[1] = cu[0]; o2[2] = cu[1]; o2[3] = cu[2];
        o3[0] = __builtin_amdgcn_alignbyte(pv[1], pv[0], 2); o3[1] = o1[0]; o3[2] = o1[1]; o3[3] = o1[2];
        if (it < 4 || lane < 2) {
          *(u32x4*)(base + 0 * CP_STRIDE + un * 16) = cu;
          *(u32x4*)(base + 1 * CP_STRIDE + un * 16) = o1;
          *(u32x4*)(base + 2 * CP_STRIDE + un * 16) = o2;
          *(u32x4*)(base + 3 * CP_STRIDE + un * 16) = o3;
        }
      }
    }
    WAIT_V(0);
    WAIT_L(0);
    TS(3);
    const int q = n >> 2, pp = lane & 3;
    f32x4 y[4];
#pragma unroll
    for (int a = 0; a < 4; ++a) y[a] = (f32x4){0, 0, 0, 0};
    const char* abase = base + q * CP_STRIDE + (16 - 8 * g + 4 * pp) * 2;
    const char* bbase = base + CW_U + 512 + (16 * n + 8 * g) * 2;
    if (half == 0) conv_loop<0>(abase, bbase, y); else conv_loop<1>(abase, bbase, y);
    TS(4);
#pragma unroll
    for (int a = 0; a < 4; ++a) {
      const int ntile = half == 0 ? (a == 0 ? 0 : a == 1 ? 3 : a == 2 ? 4 : 7) : (a == 0 ? 1 : a == 1 ? 2 : a == 2 ? 5 : 6);
      uint32_t v0 = pk2(y[a][0], y[a][1]), v1 = pk2(y[a][2], y[a][3]);
      v0 = v0 == ~0u ? 0xfffffffeu : v0;
      v1 = v1 == ~0u ? 0xfffffffeu : v1;
      __hip_atomic_store((gu64*)(p.yT + (size_t)d * L + 256 * ntile + 16 * n + 4 * g), (unsigned long long)v0 | ((unsigned long long)v1 << 32), RLX_AGENT);
    }
    TS(5);
    {
      const int dd = 16 * cw + (lane >> 2), ch = lane & 3;
      const uint32_t off = (uint32_t)((d0 + dd) * L + t0 + ch * 8) * 2u;
      u32x4 v0, v1;
      for (unsigned spins = 0; spins < (1u << 20); ++spins) {
        asm volatile("global_load_dwordx4 %0, %2, %3 sc1\n\tglobal_load_dwordx4 %1, %2, %3 offset:64 sc1\n\ts_waitcnt vmcnt(0)"
                     : "=&v"(v0), "=&v"(v1) : "v"(off), "s"(p.yT) : "memory");
        const bool ok = v0[0] != ~0u && v0[1] != ~0u && v0[2] != ~0u && v0[3] != ~0u && v1[0] != ~0u && v1[1] != ~0u && v1[2] != ~0u && v1[3] != ~0u;
        if (__all(ok)) break;
        __builtin_amdgcn_s_sleep(1);
      }
      *(u32x4*)(smem + K1_YOFF + dd * 144 + ch * 16) = v0;
      *(u32x4*)(smem + K1_YOFF + dd * 144 + ch * 16 + 64) = v1;
    }
    TS(6);
    WAIT_L(0);
    BAR();
  }
  TSDUMP(1);
}

extern "C" void kernel_launch(void* const* d_in, const int* in_sizes, int n_in, void* d_out, int out_size, void* d_ws, size_t ws_size,
                              hipStream_t stream) {
  char* ws = (char*)d_ws;
  P0 p0;
  p0.u = (const u16*)d_in[0]; p0.Are = (const u16*)d_in[1]; p0.Aim = (const u16*)d_in[2]; p0.Bre = (const u16*)d_in[3];
  p0.Bim = (const u16*)d_in[4]; p0.Cre = (const u16*)d_in[5]; p0.Cim = (const u16*)d_in[6]; p0.logdt = (const u16*)d_in[7];
  p0.wg = (const u16*)d_in[10]; p0.wu = (const u16*)d_in[11]; p0.wd = (const u16*)d_in[12]; p0.gw = (const u16*)d_in[13];
  p0.h = (u16*)(ws + WS_H); p0.wdt = (u16*)(ws + WS_WDT); p0.uT = (u16*)(ws + WS_UT); p0.E = (u16*)(ws + WS_E);
  p0.yT = (u16*)(ws + WS_YT); p0.g = (float*)(ws + WS_G);
  P1 p1;
  p1.u = p0.u; p1.Dskip = (const u16*)d_in[8]; p1.h = p0.h; p1.wdt = p0.wdt; p1.uT = p0.uT; p1.E = p0.E; p1.g = p0.g;
  p1.yT = p0.yT; p1.out = (u16*)d_out;
  (void)hipFuncSetAttribute((const void*)k0_kernel, hipFuncAttributeMaxDynamicSharedMemorySize, K0_LDS);
  (void)hipFuncSetAttribute((const void*)k1_kernel, hipFuncAttributeMaxDynamicSharedMemorySize, K1_LDS);
  k0_kernel<<<256, 512, K0_LDS, stream>>>(p0);
  k1_kernel<<<256, 512, K1_LDS, stream>>>(p1);
}
```

```cpp
#include <hip/hip_runtime.h>
#include <hip/hip_bf16.h>
#include <stdint.h>

typedef unsigned short u16;
typedef __attribute__((ext_vector_type(4))) short s16x4;
typedef __attribute__((ext_vector_type(8))) short s16x8;
typedef __attribute__((ext_vector_type(8))) __bf16 bf16x8;
typedef __attribute__((ext_vector_type(2))) __bf16 bf16x2;
typedef __attribute__((ext_vector_type(2))) float f32x2;
typedef __attribute__((ext_vector_type(4))) float f32x4;
typedef __attribute__((ext_vector_type(4))) unsigned u32x4;
typedef __attribute__((ext_vector_type(2))) unsigned u32x2;
typedef __attribute__((address_space(3))) s16x4* lds_s16x4_ptr;
typedef __attribute__((address_space(1))) unsigned gu32;
typedef __attribute__((address_space(1))) unsigned long long gu64;
#define RLX_AGENT __ATOMIC_RELAXED, __HIP_MEMORY_SCOPE_AGENT
#define DEV __device__ __forceinline__

constexpr int L = 2048, D = 512, NM = 128, F = 1792;
constexpr int KS2 = F / 32;

constexpr size_t WS_H = 0;
constexpr size_t WS_WDT = WS_H + (size_t)L * F * 2;
constexpr size_t WS_UT = WS_WDT + (size_t)F * D * 2;
constexpr size_t WS_YT = WS_UT + (size_t)L * D * 2;
constexpr int ES = 2080;
constexpr size_t WS_E = WS_YT + (size_t)L * D * 2 + 64;
constexpr size_t WS_G = WS_E + (size_t)D * ES * 2;

#define TSDECL
#define TS(ph)
#define TSDUMP(K)
DEV float bf2f(u16 v) { return __uint_as_float(((uint32_t)v) << 16); }
DEV float bflo(uint32_t v) { return __uint_as_float(v << 16); }
DEV float bfhi(uint32_t v) { return __uint_as_float(v & 0xffff0000u); }
DEV uint32_t pk2(float a, float b) { f32x2 v = {a, b}; return __builtin_bit_cast(uint32_t, __builtin_convertvector(v, bf16x2)); }
DEV float rbf(float a) { return bflo(pk2(a, 0.f) & 0xffffu); }
DEV f32x4 mfma16(u32x4 a, u32x4 b, f32x4 c) {
  return __builtin_amdgcn_mfma_f32_16x16x32_bf16(__builtin_bit_cast(bf16x8, a), __builtin_bit_cast(bf16x8, b), c, 0, 0, 0);
}
DEV u32x2 tr_read(const char* p) {
  return __builtin_bit_cast(u32x2, __builtin_amdgcn_ds_read_tr16_b64_v4i16((lds_s16x4_ptr)p));
}
DEV u32x4 tr_read2(const char* p0, const char* p1) {
  u32x2 a = tr_read(p0), b = tr_read(p1);
  u32x4 r = {a.x, a.y, b.x, b.y};
  return r;
}
DEV uint32_t lds_addr(const void* p) { return (uint32_t)(uintptr_t)(__attribute__((address_space(3))) const char*)p; }
DEV void glds16(const void* sbase, uint32_t voff, uint32_t lds) {
  unsigned keep;
  asm volatile("s_mov_b32 %0, m0\n\ts_mov_b32 m0, %3\n\ts_nop 0\n\tglobal_load_lds_dwordx4 %1, %2\n\ts_mov_b32 m0, %0"
               : "=&s"(keep) : "v"(voff), "s"(sbase), "s"(lds) : "memory");
}
#define WAIT_V(n) asm volatile("s_waitcnt vmcnt(" #n ")" ::: "memory")
#define WAIT_L(n) asm volatile("s_waitcnt lgkmcnt(" #n ")" ::: "memory")
#define BAR() __builtin_amdgcn_s_barrier()

template <bool WT>
DEV void st16(void* base, size_t byte_off, u32x4 v) {
  if (WT) {
    __builtin_amdgcn_raw_buffer_store_b128(v, __builtin_amdgcn_make_buffer_rsrc(base, 0, 0x7fffffff, 0x00020000), (uint32_t)byte_off, 0, 16);
  } else {
    *(u32x4*)((char*)base + byte_off) = v;
  }
}
#define WT_AUX 1
#define WT_H 1

struct P0 {
  const u16 *u, *Are, *Aim, *Bre, *Bim, *Cre, *Cim, *logdt, *wg, *wu, *wd, *gw;
  u16 *h, *wdt, *uT, *E, *yT;
  float* g;
};

constexpr int STAGE_B = 49152;
constexpr int K0_LDS = 3 * STAGE_B;

DEV int wswz(int row) { return ((row & 3) << 2) | ((row >> 2) & 3); }

struct SsmPar { u32x4 are, aim, bre, bim, cre, cim; };

DEV void ssm_load(const P0& p, int ch0, int w, int lane, SsmPar& s) {
  const uint32_t off = (uint32_t)((ch0 + (w >> 2)) * NM + 32 * (w & 3) + 8 * (lane >> 4)) * 2u;
  asm volatile("global_load_dwordx4 %0, %1, %2" : "=&v"(s.are) : "v"(off), "s"(p.Are) : "memory");
  asm volatile("global_load_dwordx4 %0, %1, %2" : "=&v"(s.aim) : "v"(off), "s"(p.Aim) : "memory");
  asm volatile("global_load_dwordx4 %0, %1, %2" : "=&v"(s.bre) : "v"(off), "s"(p.Bre) : "memory");
  asm volatile("global_load_dwordx4 %0, %1, %2" : "=&v"(s.bim) : "v"(off), "s"(p.Bim) : "memory");
  asm volatile("global_load_dwordx4 %0, %1, %2" : "=&v"(s.cre) : "v"(off), "s"(p.Cre) : "memory");
  asm volatile("global_load_dwordx4 %0, %1, %2" : "=&v"(s.cim) : "v"(off), "s"(p.Cim) : "memory");
}
#define SSM_WAIT(n, s) asm volatile("s_waitcnt vmcnt(" #n ")" : "+v"(s.are), "+v"(s.aim), "+v"(s.bre), "+v"(s.bim), "+v"(s.cre), "+v"(s.cim)::"memory")

DEV void kgen(const P0& p, const SsmPar& sp, float dt, int ch0, char* scratch, int tid) {
  const int w = tid >> 6, lane = tid & 63, i = lane & 15;
  const float p0 = (float)((i & 7) + 128 * (i >> 3));
  const float p1 = (i < 8) ? (float)(256 + 2 * (i & 3) + 128 * (i >> 2)) : (float)(512 + 4 * (i & 1) + 128 * ((i - 8) >> 1));
  const float p2 = (i < 8) ? (float)(1024 + 128 * i) : 2048.f;
  const float q = (float)(8 * i);
  f32x4 acc[3] = {{0, 0, 0, 0}, {0, 0, 0, 0}, {0, 0, 0, 0}};
#pragma unroll
  for (int kk = 0; kk < 2; ++kk) {
    u32x4 ah[3], al[3], bh, bl;
#pragma unroll
    for (int j = 0; j < 4; ++j) {
      const int wi_ = 2 * kk + (j >> 1);
      const uint32_t wa = sp.are[wi_], wi = sp.aim[wi_], wbr = sp.bre[wi_], wbi = sp.bim[wi_], wcr = sp.cre[wi_], wci = sp.cim[wi_];
      const float ar = dt * ((j & 1) ? bfhi(wa) : bflo(wa)), ai = dt * ((j & 1) ? bfhi(wi) : bflo(wi));
      const float br = dt * ((j & 1) ? bfhi(wbr) : bflo(wbr)), bi = dt * ((j & 1) ? bfhi(wbi) : bflo(wbi));
      const float cr = (j & 1) ? bfhi(wcr) : bflo(wcr), ci = (j & 1) ? bfhi(wci) : bflo(wci);
      const float a2 = ar * 1.44269504088896340736f, wv = ai * 0.15915494309189533577f;
      const float xr = cr * br - ci * bi, xi = cr * bi + ci * br;
      {
        const float e = __builtin_amdgcn_exp2f(a2 * q), r = __builtin_amdgcn_fractf(wv * q);
        const float s = __builtin_amdgcn_sinf(r) * e, co = __builtin_amdgcn_cosf(r) * e;
        const float vr = xr * co - xi * s, vi = -(xr * s + xi * co);
        const uint32_t hi = pk2(vr, vi);
        bh[j] = hi;
        bl[j] = pk2(vr - bflo(hi), vi - bfhi(hi));
      }
#pragma unroll
      for (int T = 0; T < 3; ++T) {
        const float pp = T == 0 ? p0 : (T == 1 ? p1 : p2);
        const float e = __builtin_amdgcn_exp2f(a2 * pp), r = __builtin_amdgcn_fractf(wv * pp);
        const float wr = __builtin_amdgcn_cosf(r) * e, wim = __builtin_amdgcn_sinf(r) * e;
        const uint32_t hi = pk2(wr, wim);
        ah[T][j] = hi;
        al[T][j] = pk2(wr - bflo(hi), wim - bfhi(hi));
      }
    }
#pragma unroll
    for (int T = 0; T < 3; ++T) {
      acc[T] = mfma16(al[T], bh, acc[T]);
      acc[T] = mfma16(ah[T], bl, acc[T]);
      acc[T] = mfma16(ah[T], bh, acc[T]);
    }
  }
  float* sc = (float*)scratch;
  u16* kc = (u16*)(scratch + 24576);
#pragma unroll
  for (int T = 0; T < 3; ++T) *(f32x4*)(sc + ((w * 3 + T) * 64 + lane) * 4) = acc[T];
  WAIT_L(0);
  BAR();
  {
    const int cc = tid >> 8, pos = tid & 255, ln = pos >> 2, j = pos & 3;
    const int ii = 4 * (ln >> 4) + j, b = ln & 15;
#pragma unroll
    for (int T = 0; T < 3; ++T) {
      float s = 0.f;
#pragma unroll
      for (int ww = 0; ww < 4; ++ww) s += sc[((cc * 4 + ww) * 3 + T) * 256 + pos];
      int idx;
      if (T == 0) idx = (ii & 7) + 128 * (ii >> 3) + 8 * b;
      else if (T == 1) idx = ii < 8 ? 256 + (ii & 3) + 64 * (ii >> 2) + 4 * b : 384 + (ii & 1) + 32 * ((ii - 8) >> 1) + 2 * b;
      else idx = ii < 8 ? 512 + 16 * ii + b : ((ii == 8 && b == 0) ? 640 : 700);
      kc[cc * 768 + idx] = (u16)(pk2(s, 0.f) & 0xffffu);
    }
  }
  WAIT_L(0);
  BAR();
  {
    const int cc = tid >> 8, un = tid & 255, l0 = 8 * un;
    const u16* k = kc + cc * 768;
    u32x4 o;
#pragma unroll
    for (int e = 0; e < 4; ++e) {
      const int la = l0 + 2 * e, lb = la + 1;
      const uint32_t pb = pk2((float)la, (float)lb);
      const int ia = la < 128 ? la : (int)(pb & 0xffffu) - 17024, ib = lb < 128 ? lb : (int)(pb >> 16) - 17024;
      o[e] = (uint32_t)k[ia] | ((uint32_t)k[ib] << 16);
    }
    const size_t dst = (size_t)(ch0 + cc) * ES * 2;
    st16<WT_AUX>(p.E, dst + (16 + l0) * 2, o);
    if (un < 2) st16<WT_AUX>(p.E, dst + 16 * un, (u32x4){0, 0, 0, 0});
  }
}

DEV void k0_glds(const P0& p, uint32_t lb, const uint32_t (&voff)[3], int m0, int f0, int k0, int piece) {
  if (piece < 2) glds16(p.u + (size_t)m0 * D + k0, voff[0] + piece * (8 * D * 2), lb + piece * 1024);
  else if (piece < 4) glds16(p.wg + (size_t)k0 * F + f0, voff[piece - 1], lb + 16384 + (piece - 2) * 1024);
  else glds16(p.wu + (size_t)k0 * F + f0, voff[piece - 3], lb + 32768 + (piece - 4) * 1024);
}

__global__ __launch_bounds__(512, 2) void k0_kernel(P0 p) {
  extern __shared__ __attribute__((aligned(16))) char smem[];
  const int tid = threadIdx.x, lane = tid & 63, w = __builtin_amdgcn_readfirstlane(tid >> 6);
  const int b = blockIdx.x, xcd = b & 7, idx = b >> 3;
  TSDECL;
  TS(0);
  const uint32_t ld2 = *(const uint32_t*)(p.logdt + 2 * b);
  if (idx < 28) {
    SsmPar sp;
    ssm_load(p, 2 * b, w, lane, sp);
    const int mt = (xcd >> 1) * 4 + idx / 7, ft = (xcd & 1) * 7 + idx % 7;
    const int m0 = mt * 128, f0 = ft * 128;
    const int rot = 2 * (idx / 7) + idx % 7;
    uint32_t voff[3];
    {
      const int row = 16 * w + (lane >> 3);
      voff[0] = (uint32_t)(row * D + 8 * ((lane & 7) ^ (row & 7))) * 2u;
      const int r0 = 8 * w + (lane >> 4), r1 = r0 + 4;
      voff[1] = (uint32_t)(r0 * F + 8 * ((lane & 15) ^ wswz(r0))) * 2u;
      voff[2] = (uint32_t)(r1 * F + 8 * ((lane & 15) ^ wswz(r1))) * 2u;
    }
    const uint32_t lb = lds_addr(smem) + 2048 * w;
#pragma unroll
    for (int s = 0; s < 2; ++s)
#pragma unroll
      for (int pc = 0; pc < 6; ++pc) k0_glds(p, lb + s * STAGE_B, voff, m0, f0, 64 * ((rot + s) & 7), pc);
    const float dt = rbf(__expf((w >> 2) ? bfhi(ld2) : bflo(ld2)));
    st16<WT_AUX>(p.yT, ((size_t)b * 4096 + tid * 8) * 2, (u32x4){~0u, ~0u, ~0u, ~0u});
    SSM_WAIT(13, sp);
    TS(1);
    kgen(p, sp, dt, 2 * b, smem + 2 * STAGE_B, tid);
    WAIT_L(0);
    BAR();
    TS(2);
#pragma unroll
    for (int pc = 0; pc < 6; ++pc) k0_glds(p, lb + 2 * STAGE_B, voff, m0, f0, 64 * ((rot + 2) & 7), pc);
    const int wm = w >> 2, wn = w & 3, g = lane >> 4, q = (lane & 15) >> 2, pp = lane & 3;
    f32x4 acc[2][2][4];
#pragma unroll
    for (int a = 0; a < 2; ++a)
#pragma unroll
      for (int c = 0; c < 2; ++c)
#pragma unroll
        for (int m = 0; m < 4; ++m) acc[a][c][m] = (f32x4){0, 0, 0, 0};
    u32x4 xf[2][4], wf[2][2][2];
    auto frags = [&](const char* buf, int ss) {
#pragma unroll
      for (int m = 0; m < 4; ++m) {
        const int row = 64 * wm + 16 * m + (lane & 15), c = 4 * ss + g;
        xf[ss][m] = *(const u32x4*)(buf + row * 128 + ((c ^ (row & 7)) << 4));
      }
#pragma unroll
      for (int a = 0; a < 2; ++a)
#pragma unroll
        for (int c = 0; c < 2; ++c) {
          const int ch = 4 * wn + 2 * c + (pp >> 1);
          const int r0 = 32 * ss + 8 * g + q, r1 = r0 + 4;
          const char* wb = buf + 16384 + a * 16384 + 8 * (pp & 1);
          wf[ss][a][c] = tr_read2(wb + 256 * r0 + 16 * (ch ^ wswz(r0)), wb + 256 * r1 + 16 * (ch ^ wswz(r1)));
        }
    };
    auto mm = [&](int ss, bool stage, int sb, int k0, int pc0) {
#pragma unroll
      for (int ac = 0; ac < 4; ++ac) {
#pragma unroll
        for (int m = 0; m < 4; ++m) acc[ac >> 1][ac & 1][m] = mfma16(wf[ss][ac >> 1][ac & 1], xf[ss][m], acc[ac >> 1][ac & 1][m]);
        if (stage && ac < 3) {
          __builtin_amdgcn_sched_barrier(0);
          k0_glds(p, lb + sb * STAGE_B, voff, m0, f0, k0, pc0 + ac);
          __builtin_amdgcn_sched_barrier(0);
        }
      }
    };
#pragma unroll
    for (int s = 0; s < 8; ++s) {
      if (s == 7) WAIT_V(0); else WAIT_V(6);
      WAIT_L(0);
      BAR();
      TS(3 + s);
      const char* buf = smem + (s % 3) * STAGE_B;
      const bool st = (s >= 1 && s + 2 < 8);
      const int k0 = 64 * ((rot + s + 2) & 7);
      frags(buf, 0);
      __builtin_amdgcn_sched_barrier(0);
      if (s > 0) mm(1, st, (s + 2) % 3, k0, 0);
      __builtin_amdgcn_sched_barrier(0);
      frags(buf, 1);
      __builtin_amdgcn_sched_barrier(0);
      mm(0, st, (s + 2) % 3, k0, 3);
      __builtin_amdgcn_sched_barrier(0);
    }
    mm(1, false, 0, 0, 0);
    TS(11);
#pragma unroll
    for (int m = 0; m < 4; ++m) {
      u32x4 o;
#pragma unroll
      for (int c = 0; c < 2; ++c)
#pragma unroll
        for (int jj = 0; jj < 2; ++jj) {
          const uint32_t g2 = pk2(acc[0][c][m][2 * jj], acc[0][c][m][2 * jj + 1]), u2 = pk2(acc[1][c][m][2 * jj], acc[1][c][m][2 * jj + 1]);
          const float g0 = bflo(g2), g1 = bfhi(g2);
          const uint32_t s2 = pk2(g0 * __builtin_amdgcn_rcpf(1.f + __expf(-g0)), g1 * __builtin_amdgcn_rcpf(1.f + __expf(-g1)));
          o[2 * c + jj] = pk2(bflo(s2) * bflo(u2), bfhi(s2) * bfhi(u2));
        }
      const int mtile = (m0 + 64 * wm + 16 * m) >> 4, ks = 4 * ft + wn;
      st16<WT_H>(p.h, ((size_t)(mtile * KS2 + ks) * 64 + lane) * 16, o);
    }
    TS(12);
  } else {
    SsmPar sp;
    ssm_load(p, 2 * b, w, lane, sp);
    const int a = xcd * 4 + (idx - 28);
    constexpr int RS = 1056;
    const int t0 = 64 * a;
    u32x4 uv[8], wv[8];
#pragma unroll
    for (int r = 0; r < 8; ++r) uv[r] = *(const u32x4*)(p.u + (size_t)(t0 + 8 * w + r) * D + lane * 8);
    const int ks2 = a + 32 < KS2 ? a + 32 : a;
#pragma unroll
    for (int r = 0; r < 4; ++r) {
      wv[r] = *(const u32x4*)(p.wd + (size_t)(32 * a + 4 * w + r) * D + lane * 8);
      wv[4 + r] = *(const u32x4*)(p.wd + (size_t)(32 * ks2 + 4 * w + r) * D + lane * 8);
    }
    const u32x4 gwv = *(const u32x4*)(p.gw + lane * 8);
    const float dt = rbf(__expf((w >> 2) ? bfhi(ld2) : bflo(ld2)));
    st16<WT_AUX>(p.yT, ((size_t)b * 4096 + tid * 8) * 2, (u32x4){~0u, ~0u, ~0u, ~0u});
    SSM_WAIT(18, sp);
    TS(1);
    kgen(p, sp, dt, 2 * b, smem + 2 * STAGE_B, tid);
    TS(2);
    {
      float s[8];
#pragma unroll
      for (int r = 0; r < 8; ++r) {
        *(u32x4*)(smem + (8 * w + r) * RS + lane * 16) = uv[r];
        s[r] = 0.f;
#pragma unroll
        for (int e = 0; e < 4; ++e) {
          const uint32_t pr = pk2(bflo(uv[r][e]) * bflo(gwv[e]), bfhi(uv[r][e]) * bfhi(gwv[e]));
          s[r] += bflo(pr) + bfhi(pr);
        }
      }
#pragma unroll
      for (int r = 0; r < 4; ++r) { const bool hi = lane & 32; const float keep = hi ? s[r + 4] : s[r], send = hi ? s[r] : s[r + 4]; s[r] = keep + __shfl_xor(send, 32); }
#pragma unroll
      for (int r = 0; r < 2; ++r) { const bool hi = lane & 16; const float keep = hi ? s[r + 2] : s[r], send = hi ? s[r] : s[r + 2]; s[r] = keep + __shfl_xor(send, 16); }
      { const bool hi = lane & 8; const float keep = hi ? s[1] : s[0], send = hi ? s[0] : s[1]; s[0] = keep + __shfl_xor(send, 8); }
      s[0] += __shfl_xor(s[0], 4); s[0] += __shfl_xor(s[0], 2); s[0] += __shfl_xor(s[0], 1);
      if ((lane & 7) == 0) {
        const int r = ((lane >> 5) & 1) * 4 + ((lane >> 4) & 1) * 2 + ((lane >> 3) & 1);
        const float gs = rbf(s[0]);
        p.g[t0 + 8 * w + r] = rbf(1.f / (1.f + __expf(-gs)));
      }
    }
    WAIT_L(0);
    BAR();
    const int g = lane >> 4, q = (lane & 15) >> 2, pp = lane & 3;
#pragma unroll
    for (int c = 0; c < 4; ++c)
#pragma unroll
      for (int hh = 0; hh < 2; ++hh) {
        const int dt_ = 4 * w + c;
        const int r0 = 32 * hh + 8 * g + q;
        const char* base = smem + (16 * dt_ + 4 * pp) * 2;
        const u32x4 v = tr_read2(base + r0 * RS, base + (r0 + 4) * RS);
        st16<WT_AUX>(p.uT, ((size_t)(16 * dt_ + (lane & 15)) * L + t0 + 32 * hh + 8 * g) * 2, v);
      }
    TS(3);
    WAIT_L(0);
    BAR();
#pragma unroll
    for (int r = 0; r < 4; ++r) {
      *(u32x4*)(smem + (4 * w + r) * RS + lane * 16) = wv[r];
      *(u32x4*)(smem + (32 + 4 * w + r) * RS + lane * 16) = wv[4 + r];
    }
    WAIT_L(0);
    BAR();
#pragma unroll
    for (int hh = 0; hh < 2; ++hh) {
      if (hh == 0 || a + 32 < KS2) {
        const int ks = a + 32 * hh;
#pragma unroll
        for (int c = 0; c < 4; ++c) {
          const int nt = 4 * w + c;
          const char* base = smem + 32 * hh * RS + (16 * nt + 4 * pp) * 2;
          const u32x4 v = tr_read2(base + (4 * g + q) * RS, base + (16 + 4 * g + q) * RS);
          st16<WT_AUX>(p.wdt, ((size_t)(nt * KS2 + ks) * 64 + lane) * 16, v);
        }
      }
    }
    TS(12);
  }
  TSDUMP(0);
}

struct P1 {
  const u16 *u, *Dskip, *h, *wdt, *uT, *E;
  const float* g;
  u16 *yT, *out;
};

constexpr int CP_STRIDE = 4160;
constexpr int CW_U = 4 * CP_STRIDE, CW_SIZE = CW_U + 4608;
constexpr int K1_XCH = 2 * CW_SIZE;
constexpr int K1_YOFF = K1_XCH + 16384;
constexpr int K1_RED = K1_YOFF + 64 * 144;
constexpr int K1_FLAG = K1_RED + 49152;
constexpr int K1_LDS = K1_FLAG + 64;

DEV void conv_loop(const char* abase, const char* bbase, f32x4 (&y)[8]) {
  u32x4 af[2][8], bf[2][8];
  auto ld = [&](int buf, int it) {
#pragma unroll
    for (int m = 0; m < 8; ++m) bf[buf][m] = *(const u32x4*)(bbase + 512 * m - 64 * it);
#pragma unroll
    for (int m = 0; m < 8; ++m) {
      const char* ap = abase + 512 * m + 64 * it;
      af[buf][m] = tr_read2(ap, ap - 8);
    }
  };
  ld(0, 0);
#pragma unroll
  for (int it = 0; it < 4; ++it) {
    const int cur = it & 1;
    __builtin_amdgcn_sched_barrier(0);
    if (it < 3) ld(cur ^ 1, it + 1);
#pragma unroll
    for (int m = 0; m < 8; ++m)
#pragma unroll
      for (int T = m; T < 8; ++T) y[T] = mfma16(af[cur][m], bf[cur][T - m], y[T]);
    if (it < 3) {
#pragma unroll
      for (int i = 0; i < 12; ++i) {
        __builtin_amdgcn_sched_group_barrier(0x008, 3, 0);
        __builtin_amdgcn_sched_group_barrier(0x100, 2, 0);
      }
    }
  }
  __builtin_amdgcn_sched_barrier(0);
}
DEV void pair_sync(char* smem, int cw, int which) {
  const uint32_t mine = lds_addr(smem + K1_FLAG + cw * 8 + which * 4), other = mine ^ 8u;
  asm volatile("s_waitcnt lgkmcnt(0)\n\tds_write_b32 %0, %1" ::"v"(mine), "v"(1u) : "memory");
  for (unsigned spins = 0; spins < (1u << 24); ++spins) {
    uint32_t x;
    asm volatile("ds_read_b32 %0, %1\n\ts_waitcnt lgkmcnt(0)" : "=v"(x) : "v"(other) : "memory");
    if (__builtin_amdgcn_readfirstlane(x) == 1u) break;
  }
}

__global__ __launch_bounds__(512, 2) void k1_kernel(P1 p) {
  extern __shared__ __attribute__((aligned(16))) char smem[];
  const int tid = threadIdx.x, lane = tid & 63, w = __builtin_amdgcn_readfirstlane(tid >> 6);
  const int b = blockIdx.x, xcd = b & 7, idx = b >> 3;
  const int mt = (xcd >> 1) * 8 + (idx >> 2), nt = (xcd & 1) * 4 + (idx & 3);
  const int t0 = mt * 64, d0 = nt * 64;
  const int g = lane >> 4, n = lane & 15;
  TSDECL;
  TS(0);
  if (w < 4) {
    BAR();
    __builtin_amdgcn_sched_barrier(0);
    f32x4 acc[4][4];
#pragma unroll
    for (int a = 0; a < 4; ++a)
#pragma unroll
      for (int c = 0; c < 4; ++c) acc[a][c] = (f32x4){0, 0, 0, 0};
    const u16* hp = p.h + ((size_t)(mt * 4) * KS2 + 14 * w) * 512 + lane * 8;
    const u16* wp = p.wdt + ((size_t)(nt * 4) * KS2 + 14 * w) * 512 + lane * 8;
    constexpr int PF = 4;
    const int rot = ((idx & 3) * 7 / 2 + (idx >> 2) * 7 / 4) % 14;
    u32x4 hf[PF][4], wf[PF][4];
#pragma unroll
    for (int s = 0; s < PF; ++s)
#pragma unroll
      for (int a = 0; a < 4; ++a) {
        const int ks = (rot + s) >= 14 ? rot + s - 14 : rot + s;
        hf[s][a] = *(const u32x4*)(hp + ((size_t)((a + w) & 3) * KS2 + ks) * 512);
        wf[s][a] = *(const u32x4*)(wp + ((size_t)a * KS2 + ks) * 512);
      }
    const int t = t0 + 16 * w + n;
    u32x2 uv[4], dv[4];
#pragma unroll
    for (int a = 0; a < 4; ++a) {
      uv[a] = *(const u32x2*)(p.u + (size_t)t * D + d0 + 16 * a + 4 * g);
      dv[a] = *(const u32x2*)(p.Dskip + d0 + 16 * a + 4 * g);
    }
    const float gt = p.g[t];
    __builtin_amdgcn_sched_barrier(0);
    TS(15);
    __builtin_amdgcn_sched_barrier(0);
#pragma unroll
    for (int s = 0; s < 14; ++s) {
#pragma unroll
      for (int a = 0; a < 4; ++a)
#pragma unroll
        for (int c = 0; c < 4; ++c) acc[a][c] = mfma16(wf[s % PF][a], hf[s % PF][c], acc[a][c]);
      __builtin_amdgcn_sched_barrier(0);
      if (s + PF < 14) {
#pragma unroll
        for (int a = 0; a < 4; ++a) {
          const int ks = (rot + s + PF) >= 14 ? rot + s + PF - 14 : rot + s + PF;
          hf[s % PF][a] = *(const u32x4*)(hp + ((size_t)((a + w) & 3) * KS2 + ks) * 512);
          wf[s % PF][a] = *(const u32x4*)(wp + ((size_t)a * KS2 + ks) * 512);
        }
      }
      __builtin_amdgcn_sched_barrier(0);
    }
    TS(1);
#pragma unroll
    for (int c = 1; c < 4; ++c)
#pragma unroll
      for (int a = 0; a < 4; ++a) *(f32x4*)(smem + K1_RED + ((((w + c) & 3) * 3 + (c - 1)) * 4 + a) * 1024 + lane * 16) = acc[a][c];
    WAIT_L(0);
    BAR();
    TS(7);
    const float omg = rbf(1.f - gt);
    const int q = n >> 2, pp = lane & 3;
#pragma unroll
    for (int a = 0; a < 4; ++a) {
      f32x4 bs = acc[a][0];
#pragma unroll
      for (int s = 0; s < 3; ++s) bs += *(const f32x4*)(smem + K1_RED + ((w * 3 + s) * 4 + a) * 1024 + lane * 16);
      const u32x2 yv = tr_read(smem + K1_YOFF + (16 * a + 4 * g + q) * 144 + (16 * w + 4 * pp) * 2);
      u32x2 o;
#pragma unroll
      for (int e = 0; e < 2; ++e) {
        const uint32_t sk = pk2(bflo(uv[a][e]) * bflo(dv[a][e]), bfhi(uv[a][e]) * bfhi(dv[a][e]));
        const uint32_t av = pk2(bflo(yv[e]) + bflo(sk), bfhi(yv[e]) + bfhi(sk));
        const uint32_t ga = pk2(gt * bflo(av), gt * bfhi(av));
        const uint32_t bb = pk2(bs[2 * e], bs[2 * e + 1]);
        const uint32_t gb = pk2(omg * bflo(bb), omg * bfhi(bb));
        o[e] = pk2(bflo(ga) + bflo(gb), bfhi(ga) + bfhi(gb));
      }
      *(u32x2*)(p.out + (size_t)t * D + d0 + 16 * a + 4 * g) = o;
    }
    TS(9);
  } else {
    const int cw = w - 4, c = cw >> 1, half = cw & 1, d = 2 * b + c;
    char* base = smem + c * CW_SIZE;
    {
      const u16* eg = p.E + (size_t)d * ES;
      u32x4 cur[3];
      u32x2 prv[3];
#pragma unroll
      for (int it = 0; it < 3; ++it) {
        const int un = it < 2 ? (2 * half + it) * 64 + lane : 256 + (lane & 1);
        asm volatile("global_load_dwordx4 %0, %2, %3\n\tglobal_load_dwordx2 %1, %2, %3 offset:-8"
                     : "=&v"(cur[it]), "=&v"(prv[it]) : "v"(un * 16u), "s"(eg) : "memory");
      }
      const uint32_t lb = lds_addr(base + CW_U + 512) + half * 2048;
#pragma unroll
      for (int i = 0; i < 2; ++i) glds16(p.uT + (size_t)d * L + (2 * half + i) * 512, lane * 16u, lb + i * 1024);
      asm volatile("ds_write_b32 %0, %1" ::"v"(lds_addr(smem + K1_FLAG + cw * 8 + (lane & 1) * 4)), "v"(0u) : "memory");
      if (lane < 32 && half == 0) *(u32x4*)(base + CW_U + lane * 16) = (u32x4){0, 0, 0, 0};
      WAIT_L(0);
      __builtin_amdgcn_sched_barrier(0);
      BAR();
      __builtin_amdgcn_sched_barrier(0);
      asm volatile("s_waitcnt vmcnt(2)" : "+v"(cur[0]), "+v"(cur[1]), "+v"(cur[2]), "+v"(prv[0]), "+v"(prv[1]), "+v"(prv[2])::"memory");
      TS(2);
#pragma unroll
      for (int it = 0; it < 3; ++it) {
        const int un = it < 2 ? (2 * half + it) * 64 + lane : 256 + (lane & 1);
        const u32x4 cu = cur[it];
        const u32x2 pv = prv[it];
        u32x4 o1, o2, o3;
        o1[0] = __builtin_amdgcn_alignbyte(cu[0], pv[1], 2); o1[1] = __builtin_amdgcn_alignbyte(cu[1], cu[0], 2);
        o1[2] = __builtin_amdgcn_alignbyte(cu[2], cu[1], 2); o1[3] = __builtin_amdgcn_alignbyte(cu[3], cu[2], 2);
        o2[0] = pv[1]; o2[1] = cu[0]; o2[2] = cu[1]; o2[3] = cu[2];
        o3[0] = __builtin_amdgcn_alignbyte(pv[1], pv[0], 2); o3[1] = o1[0]; o3[2] = o1[1]; o3[3] = o1[2];
        if (it < 2 || (lane < 2 && half == 1)) {
          *(u32x4*)(base + 0 * CP_STRIDE + un * 16) = cu;
          *(u32x4*)(base + 1 * CP_STRIDE + un * 16) = o1;
          *(u32x4*)(base + 2 * CP_STRIDE + un * 16) = o2;
          *(u32x4*)(base + 3 * CP_STRIDE + un * 16) = o3;
        }
      }
    }
    WAIT_V(0);
    pair_sync(smem, cw, 0);
    TS(3);
    f32x4 y[8];
#pragma unroll
    for (int a = 0; a < 8; ++a) y[a] = (f32x4){0, 0, 0, 0};
    {
      const int q = n >> 2, pp = lane & 3;
      const char* abase = base + q * CP_STRIDE + (16 - 8 * g + 4 * pp) * 2 + 32 * (8 * half + 1);
      const char* bbase = base + CW_U + 512 + (16 * n + 8 * g) * 2 - 32 * (8 * half + 1);
      conv_loop(abase, bbase, y);
    }
    TS(4);
#pragma unroll
    for (int a = 0; a < 4; ++a) *(f32x4*)(smem + K1_XCH + (cw * 4 + a) * 1024 + lane * 16) = half ? y[a] : y[4 + a];
    pair_sync(smem, cw, 1);
#pragma unroll
    for (int a = 0; a < 4; ++a) {
      const f32x4 o = *(const f32x4*)(smem + K1_XCH + ((cw ^ 1) * 4 + a) * 1024 + lane * 16);
      const f32x4 v = half ? o + y[4 + a] : y[a] + o;
      const int ntile = 4 * half + a;
      uint32_t v0 = pk2(v[0], v[1]), v1 = pk2(v[2], v[3]);
      v0 = v0 == ~0u ? 0xfffffffeu : v0;
      v1 = v1 == ~0u ? 0xfffffffeu : v1;
      __hip_atomic_store((gu64*)(p.yT + (size_t)d * L + 256 * ntile + 16 * n + 4 * g), (unsigned long long)v0 | ((unsigned long long)v1 << 32), RLX_AGENT);
    }
    TS(5);
    {
      const int dd = 16 * cw + (lane >> 2), ch = lane & 3;
      const uint32_t off = (uint32_t)((d0 + dd) * L + t0 + ch * 8) * 2u;
      u32x4 v0, v1;
      for (unsigned spins = 0; spins < (1u << 20); ++spins) {
        asm volatile("global_load_dwordx4 %0, %2, %3 sc1\n\tglobal_load_dwordx4 %1, %2, %3 offset:64 sc1\n\ts_waitcnt vmcnt(0)"
                     : "=&v"(v0), "=&v"(v1) : "v"(off), "s"(p.yT) : "memory");
        const bool ok = v0[0] != ~0u && v0[1] != ~0u && v0[2] != ~0u && v0[3] != ~0u && v1[0] != ~0u && v1[1] != ~0u && v1[2] != ~0u && v1[3] != ~0u;
        if (__all(ok)) break;
        __builtin_amdgcn_s_sleep(1);
      }
      *(u32x4*)(smem + K1_YOFF + dd * 144 + ch * 16) = v0;
      *(u32x4*)(smem + K1_YOFF + dd * 144 + ch * 16 + 64) = v1;
    }
    TS(6);
    WAIT_L(0);
    BAR();
  }
  TSDUMP(1);
}

extern "C" void kernel_launch(void* const* d_in, const int* in_sizes, int n_in, void* d_out, int out_size, void* d_ws, size_t ws_size,
                              hipStream_t stream) {
  char* ws = (char*)d_ws;
  P0 p0;
  p0.u = (const u16*)d_in[0]; p0.Are = (const u16*)d_in[1]; p0.Aim = (const u16*)d_in[2]; p0.Bre = (const u16*)d_in[3];
  p0.Bim = (const u16*)d_in[4]; p0.Cre = (const u16*)d_in[5]; p0.Cim = (const u16*)d_in[6]; p0.logdt = (const u16*)d_in[7];
  p0.wg = (const u16*)d_in[10]; p0.wu = (const u16*)d_in[11]; p0.wd = (const u16*)d_in[12]; p0.gw = (const u16*)d_in[13];
  p0.h = (u16*)(ws + WS_H); p0.wdt = (u16*)(ws + WS_WDT); p0.uT = (u16*)(ws + WS_UT); p0.E = (u16*)(ws + WS_E);
  p0.yT = (u16*)(ws + WS_YT); p0.g = (float*)(ws + WS_G);
  P1 p1;
  p1.u = p0.u; p1.Dskip = (const u16*)d_in[8]; p1.h = p0.h; p1.wdt = p0.wdt; p1.uT = p0.uT; p1.E = p0.E; p1.g = p0.g;
  p1.yT = p0.yT; p1.out = (u16*)d_out;
  (void)hipFuncSetAttribute((const void*)k0_kernel, hipFuncAttributeMaxDynamicSharedMemorySize, K0_LDS);
  (void)hipFuncSetAttribute((const void*)k1_kernel, hipFuncAttributeMaxDynamicSharedMemorySize, K1_LDS);
  k0_kernel<<<256, 512, K0_LDS, stream>>>(p0);
  k1_kernel<<<256, 512, K1_LDS, stream>>>(p1);
}
```

```cpp
#include <hip/hip_runtime.h>
#include <hip/hip_bf16.h>
#include <stdint.h>

typedef unsigned short u16;
typedef __attribute__((ext_vector_type(4))) short s16x4;
typedef __attribute__((ext_vector_type(8))) short s16x8;
typedef __attribute__((ext_vector_type(8))) __bf16 bf16x8;
typedef __attribute__((ext_vector_type(2))) __bf16 bf16x2;
typedef __attribute__((ext_vector_type(2))) float f32x2;
typedef __attribute__((ext_vector_type(4))) float f32x4;
typedef __attribute__((ext_vector_type(4))) unsigned u32x4;
typedef __attribute__((ext_vector_type(2))) unsigned u32x2;
typedef __attribute__((address_space(3))) s16x4* lds_s16x4_ptr;
typedef __attribute__((address_space(1))) unsigned gu32;
typedef __attribute__((address_space(1))) unsigned long long gu64;
#define RLX_AGENT __ATOMIC_RELAXED, __HIP_MEMORY_SCOPE_AGENT
#define DEV __device__ __forceinline__

constexpr int L = 2048, D = 512, NM = 128, F = 1792;
constexpr int KS2 = F / 32;

constexpr size_t WS_H = 0;
constexpr size_t WS_WDT = WS_H + (size_t)L * F * 2;
constexpr size_t WS_UT = WS_WDT + (size_t)F * D * 2;
constexpr size_t WS_YT = WS_UT + (size_t)L * D * 2;
constexpr int ES = 2080;
constexpr size_t WS_E = WS_YT + (size_t)L * D * 2 + 64;
constexpr size_t WS_G = WS_E + (size_t)D * ES * 2;

#define TSDECL
#define TS(ph)
#define TSDUMP(K)
DEV float bf2f(u16 v) { return __uint_as_float(((uint32_t)v) << 16); }
DEV float bflo(uint32_t v) { return __uint_as_float(v << 16); }
DEV float bfhi(uint32_t v) { return __uint_as_float(v & 0xffff0000u); }
DEV uint32_t pk2(float a, float b) { f32x2 v = {a, b}; return __builtin_bit_cast(uint32_t, __builtin_convertvector(v, bf16x2)); }
DEV float rbf(float a) { return bflo(pk2(a, 0.f) & 0xffffu); }
DEV f32x4 mfma16(u32x4 a, u32x4 b, f32x4 c) {
  return __builtin_amdgcn_mfma_f32_16x16x32_bf16(__builtin_bit_cast(bf16x8, a), __builtin_bit_cast(bf16x8, b), c, 0, 0, 0);
}
DEV u32x2 tr_read(const char* p) {
  return __builtin_bit_cast(u32x2, __builtin_amdgcn_ds_read_tr16_b64_v4i16((lds_s16x4_ptr)p));
}
DEV u32x4 tr_read2(const char* p0, const char* p1) {
  u32x2 a = tr_read(p0), b = tr_read(p1);
  u32x4 r = {a.x, a.y, b.x, b.y};
  return r;
}
DEV uint32_t lds_addr(const void* p) { return (uint32_t)(uintptr_t)(__attribute__((address_space(3))) const char*)p; }
DEV void glds16(const void* sbase, uint32_t voff, uint32_t lds) {
  unsigned keep;
  asm volatile("s_mov_b32 %0, m0\n\ts_mov_b32 m0, %3\n\ts_nop 0\n\tglobal_load_lds_dwordx4 %1, %2\n\ts_mov_b32 m0, %0"
               : "=&s"(keep) : "v"(voff), "s"(sbase), "s"(lds) : "memory");
}
#define WAIT_V(n) asm volatile("s_waitcnt vmcnt(" #n ")" ::: "memory")
#define WAIT_L(n) asm volatile("s_waitcnt lgkmcnt(" #n ")" ::: "memory")
#define BAR() __builtin_amdgcn_s_barrier()

template <bool WT>
DEV void st16(void* base, size_t byte_off, u32x4 v) {
  if (WT) {
    __builtin_amdgcn_raw_buffer_store_b128(v, __builtin_amdgcn_make_buffer_rsrc(base, 0, 0x7fffffff, 0x00020000), (uint32_t)byte_off, 0, 16);
  } else {
    *(u32x4*)((char*)base + byte_off) = v;
  }
}
#define WT_AUX 1
#define WT_H 1

struct P0 {
  const u16 *u, *Are, *Aim, *Bre, *Bim, *Cre, *Cim, *logdt, *wg, *wu, *wd, *gw;
  u16 *h, *wdt, *uT, *E, *yT;
  float* g;
};

constexpr int STAGE_B = 49152;
constexpr int K0_LDS = 3 * STAGE_B;

DEV int wswz(int row) { return ((row & 3) << 2) | ((row >> 2) & 3); }

struct SsmPar { u32x4 are, aim, bre, bim, cre, cim; };

DEV void ssm_load(const P0& p, int ch0, int w, int lane, SsmPar& s) {
  const uint32_t off = (uint32_t)((ch0 + (w >> 2)) * NM + 32 * (w & 3) + 8 * (lane >> 4)) * 2u;
  asm volatile("global_load_dwordx4 %0, %1, %2" : "=&v"(s.are) : "v"(off), "s"(p.Are) : "memory");
  asm volatile("global_load_dwordx4 %0, %1, %2" : "=&v"(s.aim) : "v"(off), "s"(p.Aim) : "memory");
  asm volatile("global_load_dwordx4 %0, %1, %2" : "=&v"(s.bre) : "v"(off), "s"(p.Bre) : "memory");
  asm volatile("global_load_dwordx4 %0, %1, %2" : "=&v"(s.bim) : "v"(off), "s"(p.Bim) : "memory");
  asm volatile("global_load_dwordx4 %0, %1, %2" : "=&v"(s.cre) : "v"(off), "s"(p.Cre) : "memory");
  asm volatile("global_load_dwordx4 %0, %1, %2" : "=&v"(s.cim) : "v"(off), "s"(p.Cim) : "memory");
}
#define SSM_WAIT(n, s) asm volatile("s_waitcnt vmcnt(" #n ")" : "+v"(s.are), "+v"(s.aim), "+v"(s.bre), "+v"(s.bim), "+v"(s.cre), "+v"(s.cim)::"memory")

DEV void kgen(const P0& p, const SsmPar& sp, float dt, int ch0, char* scratch, int tid) {
  const int w = tid >> 6, lane = tid & 63, i = lane & 15;
  const float p0 = (float)((i & 7) + 128 * (i >> 3));
  const float p1 = (i < 8) ? (float)(256 + 2 * (i & 3) + 128 * (i >> 2)) : (float)(512 + 4 * (i & 1) + 128 * ((i - 8) >> 1));
  const float p2 = (i < 8) ? (float)(1024 + 128 * i) : 2048.f;
  const float q = (float)(8 * i);
  f32x4 acc[3] = {{0, 0, 0, 0}, {0, 0, 0, 0}, {0, 0, 0, 0}};
#pragma unroll
  for (int kk = 0; kk < 2; ++kk) {
    u32x4 ah[3], al[3], bh, bl;
#pragma unroll
    for (int j = 0; j < 4; ++j) {
      const int wi_ = 2 * kk + (j >> 1);
      const uint32_t wa = sp.are[wi_], wi = sp.aim[wi_], wbr = sp.bre[wi_], wbi = sp.bim[wi_], wcr = sp.cre[wi_], wci = sp.cim[wi_];
      const float ar = dt * ((j & 1) ? bfhi(wa) : bflo(wa)), ai = dt * ((j & 1) ? bfhi(wi) : bflo(wi));
      const float br = dt * ((j & 1) ? bfhi(wbr) : bflo(wbr)), bi = dt * ((j & 1) ? bfhi(wbi) : bflo(wbi));
      const float cr = (j & 1) ? bfhi(wcr) : bflo(wcr), ci = (j & 1) ? bfhi(wci) : bflo(wci);
      const float a2 = ar * 1.44269504088896340736f, wv = ai * 0.15915494309189533577f;
      const float xr = cr * br - ci * bi, xi = cr * bi + ci * br;
      {
        const float e = __builtin_amdgcn_exp2f(a2 * q), r = __builtin_amdgcn_fractf(wv * q);
        const float s = __builtin_amdgcn_sinf(r) * e, co = __builtin_amdgcn_cosf(r) * e;
        const float vr = xr * co - xi * s, vi = -(xr * s + xi * co);
        const uint32_t hi = pk2(vr, vi);
        bh[j] = hi;
        bl[j] = pk2(vr - bflo(hi), vi - bfhi(hi));
      }
#pragma unroll
      for (int T = 0; T < 3; ++T) {
        const float pp = T == 0 ? p0 : (T == 1 ? p1 : p2);
        const float e = __builtin_amdgcn_exp2f(a2 * pp), r = __builtin_amdgcn_fractf(wv * pp);
        const float wr = __builtin_amdgcn_cosf(r) * e, wim = __builtin_amdgcn_sinf(r) * e;
        const uint32_t hi = pk2(wr, wim);
        ah[T][j] = hi;
        al[T][j] = pk2(wr - bflo(hi), wim - bfhi(hi));
      }
    }
#pragma unroll
    for (int T = 0; T < 3; ++T) {
      acc[T] = mfma16(al[T], bh, acc[T]);
      acc[T] = mfma16(ah[T], bl, acc[T]);
      acc[T] = mfma16(ah[T], bh, acc[T]);
    }
  }
  float* sc = (float*)scratch;
  u16* kc = (u16*)(scratch + 24576);
#pragma unroll
  for (int T = 0; T < 3; ++T) *(f32x4*)(sc + ((w * 3 + T) * 64 + lane) * 4) = acc[T];
  WAIT_L(0);
  BAR();
  {
    const int cc = tid >> 8, pos = tid & 255, ln = pos >> 2, j = pos & 3;
    const int ii = 4 * (ln >> 4) + j, b = ln & 15;
#pragma unroll
    for (int T = 0; T < 3; ++T) {
      float s = 0.f;
#pragma unroll
      for (int ww = 0; ww < 4; ++ww) s += sc[((cc * 4 + ww) * 3 + T) * 256 + pos];
      int idx;
      if (T == 0) idx = (ii & 7) + 128 * (ii >> 3) + 8 * b;
      else if (T == 1) idx = ii < 8 ? 256 + (ii & 3) + 64 * (ii >> 2) + 4 * b : 384 + (ii & 1) + 32 * ((ii - 8) >> 1) + 2 * b;
      else idx = ii < 8 ? 512 + 16 * ii + b : ((ii == 8 && b == 0) ? 640 : 700);
      kc[cc * 768 + idx] = (u16)(pk2(s, 0.f) & 0xffffu);
    }
  }
  WAIT_L(0);
  BAR();
  {
    const int cc = tid >> 8, un = tid & 255, l0 = 8 * un;
    const u16* k = kc + cc * 768;
    u32x4 o;
#pragma unroll
    for (int e = 0; e < 4; ++e) {
      const int la = l0 + 2 * e, lb = la + 1;
      const uint32_t pb = pk2((float)la, (float)lb);
      const int ia = la < 128 ? la : (int)(pb & 0xffffu) - 17024, ib = lb < 128 ? lb : (int)(pb >> 16) - 17024;
      o[e] = (uint32_t)k[ia] | ((uint32_t)k[ib] << 16);
    }
    const size_t dst = (size_t)(ch0 + cc) * ES * 2;
    st16<WT_AUX>(p.E, dst + (16 + l0) * 2, o);
    if (un < 2) st16<WT_AUX>(p.E, dst + 16 * un, (u32x4){0, 0, 0, 0});
  }
}

DEV void k0_glds(const P0& p, uint32_t lb, const uint32_t (&voff)[3], int m0, int f0, int k0, int piece) {
  if (piece < 2) glds16(p.u + (size_t)m0 * D + k0, voff[0] + piece * (8 * D * 2), lb + piece * 1024);
  else if (piece < 4) glds16(p.wg + (size_t)k0 * F + f0, voff[piece - 1], lb + 16384 + (piece - 2) * 1024);
  else glds16(p.wu + (size_t)k0 * F + f0, voff[piece - 3], lb + 32768 + (piece - 4) * 1024);
}

__global__ __launch_bounds__(512, 2) void k0_kernel(P0 p) {
  extern __shared__ __attribute__((aligned(16))) char smem[];
  const int tid = threadIdx.x, lane = tid & 63, w = __builtin_amdgcn_readfirstlane(tid >> 6);
  const int b = blockIdx.x, xcd = b & 7, idx = b >> 3;
  TSDECL;
  TS(0);
  const uint32_t ld2 = *(const uint32_t*)(p.logdt + 2 * b);
  if (idx < 28) {
    SsmPar sp;
    ssm_load(p, 2 * b, w, lane, sp);
    const int mt = (xcd >> 1) * 4 + idx / 7, ft = (xcd & 1) * 7 + idx % 7;
    const int m0 = mt * 128, f0 = ft * 128;
#define K0ROT 2
    const int rot = K0ROT == 0 ? 2 * (idx / 7) + idx % 7 : K0ROT == 1 ? 0 : K0ROT == 2 ? (idx & 1) * 4 : K0ROT == 3 ? 2 * (idx / 7) : idx % 7;
    uint32_t voff[3];
    {
      const int row = 16 * w + (lane >> 3);
      voff[0] = (uint32_t)(row * D + 8 * ((lane & 7) ^ (row & 7))) * 2u;
      const int r0 = 8 * w + (lane >> 4), r1 = r0 + 4;
      voff[1] = (uint32_t)(r0 * F + 8 * ((lane & 15) ^ wswz(r0))) * 2u;
      voff[2] = (uint32_t)(r1 * F + 8 * ((lane & 15) ^ wswz(r1))) * 2u;
    }
    const uint32_t lb = lds_addr(smem) + 2048 * w;
#pragma unroll
    for (int s = 0; s < 2; ++s)
#pragma unroll
      for (int pc = 0; pc < 6; ++pc) k0_glds(p, lb + s * STAGE_B, voff, m0, f0, 64 * ((rot + s) & 7), pc);
    const float dt = rbf(__expf((w >> 2) ? bfhi(ld2) : bflo(ld2)));
    st16<WT_AUX>(p.yT, ((size_t)b * 4096 + tid * 8) * 2, (u32x4){~0u, ~0u, ~0u, ~0u});
    SSM_WAIT(13, sp);
    TS(1);
    kgen(p, sp, dt, 2 * b, smem + 2 * STAGE_B, tid);
    WAIT_L(0);
    BAR();
    TS(2);
#pragma unroll
    for (int pc = 0; pc < 6; ++pc) k0_glds(p, lb + 2 * STAGE_B, voff, m0, f0, 64 * ((rot + 2) & 7), pc);
    const int wm = w >> 2, wn = w & 3, g = lane >> 4, q = (lane & 15) >> 2, pp = lane & 3;
    f32x4 acc[2][2][4];
#pragma unroll
    for (int a = 0; a < 2; ++a)
#pragma unroll
      for (int c = 0; c < 2; ++c)
#pragma unroll
        for (int m = 0; m < 4; ++m) acc[a][c][m] = (f32x4){0, 0, 0, 0};
    u32x4 xf[2][4], wf[2][2][2];
    auto frags = [&](const char* buf, int ss) {
#pragma unroll
      for (int m = 0; m < 4; ++m) {
        const int row = 64 * wm + 16 * m + (lane & 15), c = 4 * ss + g;
        xf[ss][m] = *(const u32x4*)(buf + row * 128 + ((c ^ (row & 7)) << 4));
      }
#pragma unroll
      for (int a = 0; a < 2; ++a)
#pragma unroll
        for (int c = 0; c < 2; ++c) {
          const int ch = 4 * wn + 2 * c + (pp >> 1);
          const int r0 = 32 * ss + 8 * g + q, r1 = r0 + 4;
          const char* wb = buf + 16384 + a * 16384 + 8 * (pp & 1);
          wf[ss][a][c] = tr_read2(wb + 256 * r0 + 16 * (ch ^ wswz(r0)), wb + 256 * r1 + 16 * (ch ^ wswz(r1)));
        }
    };
    auto mm = [&](int ss, bool stage, int sb, int k0, int pc0) {
#pragma unroll
      for (int ac = 0; ac < 4; ++ac) {
#pragma unroll
        for (int m = 0; m < 4; ++m) acc[ac >> 1][ac & 1][m] = mfma16(wf[ss][ac >> 1][ac & 1], xf[ss][m], acc[ac >> 1][ac & 1][m]);
        if (stage && ac < 3) {
          __builtin_amdgcn_sched_barrier(0);
          k0_glds(p, lb + sb * STAGE_B, voff, m0, f0, k0, pc0 + ac);
          __builtin_amdgcn_sched_barrier(0);
        }
      }
    };
#pragma unroll
    for (int s = 0; s < 8; ++s) {
      if (s == 7) WAIT_V(0); else WAIT_V(6);
      WAIT_L(0);
      BAR();
      TS(3 + s);
      const char* buf = smem + (s % 3) * STAGE_B;
      const bool st = (s >= 1 && s + 2 < 8);
      const int k0 = 64 * ((rot + s + 2) & 7);
      frags(buf, 0);
      __builtin_amdgcn_sched_barrier(0);
      if (s > 0) mm(1, st, (s + 2) % 3, k0, 0);
      __builtin_amdgcn_sched_barrier(0);
      frags(buf, 1);
      __builtin_amdgcn_sched_barrier(0);
      mm(0, st, (s + 2) % 3, k0, 3);
      __builtin_amdgcn_sched_barrier(0);
    }
    mm(1, false, 0, 0, 0);
    TS(11);
#pragma unroll
    for (int m = 0; m < 4; ++m) {
      u32x4 o;
#pragma unroll
      for (int c = 0; c < 2; ++c)
#pragma unroll
        for (int jj = 0; jj < 2; ++jj) {
          const uint32_t g2 = pk2(acc[0][c][m][2 * jj], acc[0][c][m][2 * jj + 1]), u2 = pk2(acc[1][c][m][2 * jj], acc[1][c][m][2 * jj + 1]);
          const float g0 = bflo(g2), g1 = bfhi(g2);
          const uint32_t s2 = pk2(g0 * __builtin_amdgcn_rcpf(1.f + __expf(-g0)), g1 * __builtin_amdgcn_rcpf(1.f + __expf(-g1)));
          o[2 * c + jj] = pk2(bflo(s2) * bflo(u2), bfhi(s2) * bfhi(u2));
        }
      const int mt64 = (m0 >> 6) + wm, ks = 4 * ft + wn;
      st16<WT_H>(p.h, ((size_t)((mt64 * KS2 + ks) * 4 + m) * 64 + lane) * 16, o);
    }
    TS(12);
  } else {
    SsmPar sp;
    ssm_load(p, 2 * b, w, lane, sp);
    const int a = xcd * 4 + (idx - 28);
    constexpr int RS = 1056;
    const int t0 = 64 * a;
    u32x4 uv[8], wv[8];
#pragma unroll
    for (int r = 0; r < 8; ++r) uv[r] = *(const u32x4*)(p.u + (size_t)(t0 + 8 * w + r) * D + lane * 8);
    const int ks2 = a + 32 < KS2 ? a + 32 : a;
#pragma unroll
    for (int r = 0; r < 4; ++r) {
      wv[r] = *(const u32x4*)(p.wd + (size_t)(32 * a + 4 * w + r) * D + lane * 8);
      wv[4 + r] = *(const u32x4*)(p.wd + (size_t)(32 * ks2 + 4 * w + r) * D + lane * 8);
    }
    const u32x4 gwv = *(const u32x4*)(p.gw + lane * 8);
    const float dt = rbf(__expf((w >> 2) ? bfhi(ld2) : bflo(ld2)));
    st16<WT_AUX>(p.yT, ((size_t)b * 4096 + tid * 8) * 2, (u32x4){~0u, ~0u, ~0u, ~0u});
    SSM_WAIT(18, sp);
    TS(1);
    kgen(p, sp, dt, 2 * b, smem + 2 * STAGE_B, tid);
    TS(2);
    {
      float s[8];
#pragma unroll
      for (int r = 0; r < 8; ++r) {
        *(u32x4*)(smem + (8 * w + r) * RS + lane * 16) = uv[r];
        s[r] = 0.f;
#pragma unroll
        for (int e = 0; e < 4; ++e) {
          const uint32_t pr = pk2(bflo(uv[r][e]) * bflo(gwv[e]), bfhi(uv[r][e]) * bfhi(gwv[e]));
          s[r] += bflo(pr) + bfhi(pr);
        }
      }
#pragma unroll
      for (int r = 0; r < 4; ++r) { const bool hi = lane & 32; const float keep = hi ? s[r + 4] : s[r], send = hi ? s[r] : s[r + 4]; s[r] = keep + __shfl_xor(send, 32); }
#pragma unroll
      for (int r = 0; r < 2; ++r) { const bool hi = lane & 16; const float keep = hi ? s[r + 2] : s[r], send = hi ? s[r] : s[r + 2]; s[r] = keep + __shfl_xor(send, 16); }
      { const bool hi = lane & 8; const float keep = hi ? s[1] : s[0], send = hi ? s[0] : s[1]; s[0] = keep + __shfl_xor(send, 8); }
      s[0] += __shfl_xor(s[0], 4); s[0] += __shfl_xor(s[0], 2); s[0] += __shfl_xor(s[0], 1);
      if ((lane & 7) == 0) {
        const int r = ((lane >> 5) & 1) * 4 + ((lane >> 4) & 1) * 2 + ((lane >> 3) & 1);
        const float gs = rbf(s[0]);
        p.g[t0 + 8 * w + r] = rbf(1.f / (1.f + __expf(-gs)));
      }
    }
    WAIT_L(0);
    BAR();
    const int g = lane >> 4, q = (lane & 15) >> 2, pp = lane & 3;
#pragma unroll
    for (int c = 0; c < 4; ++c)
#pragma unroll
      for (int hh = 0; hh < 2; ++hh) {
        const int dt_ = 4 * w + c;
        const int r0 = 32 * hh + 8 * g + q;
        const char* base = smem + (16 * dt_ + 4 * pp) * 2;
        const u32x4 v = tr_read2(base + r0 * RS, base + (r0 + 4) * RS);
        st16<WT_AUX>(p.uT, ((size_t)(16 * dt_ + (lane & 15)) * L + t0 + 32 * hh + 8 * g) * 2, v);
      }
    TS(3);
    WAIT_L(0);
    BAR();
#pragma unroll
    for (int r = 0; r < 4; ++r) {
      *(u32x4*)(smem + (4 * w + r) * RS + lane * 16) = wv[r];
      *(u32x4*)(smem + (32 + 4 * w + r) * RS + lane * 16) = wv[4 + r];
    }
    WAIT_L(0);
    BAR();
#pragma unroll
    for (int hh = 0; hh < 2; ++hh) {
      if (hh == 0 || a + 32 < KS2) {
        const int ks = a + 32 * hh;
#pragma unroll
        for (int c = 0; c < 4; ++c) {
          const int nt = 4 * w + c;
          const char* base = smem + 32 * hh * RS + (16 * nt + 4 * pp) * 2;
          const u32x4 v = tr_read2(base + (4 * g + q) * RS, base + (16 + 4 * g + q) * RS);
          st16<WT_AUX>(p.wdt, ((size_t)(((nt >> 2) * KS2 + ks) * 4 + (nt & 3)) * 64 + lane) * 16, v);
        }
      }
    }
    TS(12);
  }
  TSDUMP(0);
}

#define GO_STEP 7
#define FPRIO 1
#define LPRIO 0
struct P1 {
  const u16 *u, *Dskip, *h, *wdt, *uT, *E;
  const float* g;
  u16 *yT, *out;
};

constexpr int CP_STRIDE = 4160;
constexpr int CW_U = 4 * CP_STRIDE, CW_SIZE = CW_U + 4608;
constexpr int K1_XCH = 2 * CW_SIZE;
constexpr int K1_YOFF = K1_XCH + 16384;
constexpr int K1_RED = K1_YOFF + 64 * 144;
constexpr int K1_FLAG = K1_RED + 49152;
constexpr int K1_LDS = K1_FLAG + 64;

DEV void conv_loop(const char* abase, const char* bbase, f32x4 (&y)[8]) {
  u32x4 af[2][8], bf[2][8];
  auto ld = [&](int buf, int it) {
#pragma unroll
    for (int m = 0; m < 8; ++m) bf[buf][m] = *(const u32x4*)(bbase + 512 * m - 64 * it);
#pragma unroll
    for (int m = 0; m < 8; ++m) {
      const char* ap = abase + 512 * m + 64 * it;
      af[buf][m] = tr_read2(ap, ap - 8);
    }
  };
  ld(0, 0);
#pragma unroll
  for (int it = 0; it < 4; ++it) {
    const int cur = it & 1;
    __builtin_amdgcn_sched_barrier(0);
    if (it < 3) ld(cur ^ 1, it + 1);
#pragma unroll
    for (int m = 0; m < 8; ++m)
#pragma unroll
      for (int T = m; T < 8; ++T) y[T] = mfma16(af[cur][m], bf[cur][T - m], y[T]);
    if (it < 3) {
#pragma unroll
      for (int i = 0; i < 12; ++i) {
        __builtin_amdgcn_sched_group_barrier(0x008, 3, 0);
        __builtin_amdgcn_sched_group_barrier(0x100, 2, 0);
      }
    }
  }
  __builtin_amdgcn_sched_barrier(0);
}
DEV void pair_sync(char* smem, int cw, int which) {
  const uint32_t mine = lds_addr(smem + K1_FLAG + cw * 8 + which * 4), other = mine ^ 8u;
  asm volatile("s_waitcnt lgkmcnt(0)\n\tds_write_b32 %0, %1" ::"v"(mine), "v"(1u) : "memory");
  for (unsigned spins = 0; spins < (1u << 24); ++spins) {
    uint32_t x;
    asm volatile("ds_read_b32 %0, %1\n\ts_waitcnt lgkmcnt(0)" : "=v"(x) : "v"(other) : "memory");
    if (__builtin_amdgcn_readfirstlane(x) == 1u) break;
  }
}

__global__ __launch_bounds__(512, 2) void k1_kernel(P1 p) {
  extern __shared__ __attribute__((aligned(16))) char smem[];
  const int tid = threadIdx.x, lane = tid & 63, w = __builtin_amdgcn_readfirstlane(tid >> 6);
  const int b = blockIdx.x, xcd = b & 7, idx = b >> 3;
  const int mt = (xcd >> 1) * 8 + (idx >> 2), nt = (xcd & 1) * 4 + (idx & 3);
  const int t0 = mt * 64, d0 = nt * 64;
  const int g = lane >> 4, n = lane & 15;
  TSDECL;
  TS(0);
  if (w >= 4) __builtin_amdgcn_s_setprio(3);
  if (w == 4) {
    asm volatile("ds_write_b32 %0, %1\n\ts_waitcnt lgkmcnt(0)" ::"v"(lds_addr(smem + K1_FLAG + (lane & 15) * 4)), "v"(0u) : "memory");
  }
  BAR();
  __builtin_amdgcn_sched_barrier(0);
  if (w < 4) {
    __builtin_amdgcn_s_setprio(FPRIO);
    f32x4 acc[4][4];
#pragma unroll
    for (int a = 0; a < 4; ++a)
#pragma unroll
      for (int c = 0; c < 4; ++c) acc[a][c] = (f32x4){0, 0, 0, 0};
    const u16* hp = p.h + ((size_t)mt * KS2 + 14 * w) * 2048 + lane * 8;
    const u16* wp = p.wdt + ((size_t)nt * KS2 + 14 * w) * 2048 + lane * 8;
#define PFD 4
    constexpr int PF = PFD;
    u32x4 hf[PF][4], wf[PF][4];
    const int hrot = w * 512;
#pragma unroll
    for (int s = 0; s < PF; ++s)
#pragma unroll
      for (int a = 0; a < 4; ++a) {
        hf[s][a] = *(const u32x4*)(hp + s * 2048 + ((a * 512 + hrot) & 2047));
        wf[s][a] = *(const u32x4*)(wp + s * 2048 + a * 512);
      }
    const int t = t0 + 16 * w + n;
    u32x2 uv[4], dv[4];
    float gt;
    __builtin_amdgcn_sched_barrier(0);
    TS(15);
    __builtin_amdgcn_sched_barrier(0);
#pragma unroll
    for (int s = 0; s < 14; ++s) {
#pragma unroll
      for (int a = 0; a < 4; ++a)
#pragma unroll
        for (int c = 0; c < 4; ++c) acc[a][c] = mfma16(wf[s % PF][a], hf[s % PF][c], acc[a][c]);
      __builtin_amdgcn_sched_barrier(0);
      if (s + PF < 14) {
#pragma unroll
        for (int a = 0; a < 4; ++a) {
          hf[s % PF][a] = *(const u32x4*)(hp + (s + PF) * 2048 + ((a * 512 + hrot) & 2047));
          wf[s % PF][a] = *(const u32x4*)(wp + (s + PF) * 2048 + a * 512);
        }
      }
      if (s == GO_STEP) asm volatile("ds_write_b32 %0, %1" ::"v"(lds_addr(smem + K1_FLAG + 32 + w * 4)), "v"(1u) : "memory");
      if (s + PF == 14) {
#pragma unroll
        for (int a = 0; a < 4; ++a) {
          uv[a] = *(const u32x2*)(p.u + (size_t)t * D + d0 + 16 * a + 4 * g);
          dv[a] = *(const u32x2*)(p.Dskip + d0 + 16 * a + 4 * g);
        }
        gt = p.g[t];
      }
      __builtin_amdgcn_sched_barrier(0);
    }
    TS(1);
#pragma unroll
    for (int c = 1; c < 4; ++c)
#pragma unroll
      for (int a = 0; a < 4; ++a) *(f32x4*)(smem + K1_RED + ((((w + c) & 3) * 3 + (c - 1)) * 4 + a) * 1024 + lane * 16) = acc[a][c];
    WAIT_L(0);
    BAR();
    TS(7);
    const float omg = rbf(1.f - gt);
    const int q = n >> 2, pp = lane & 3;
#pragma unroll
    for (int a = 0; a < 4; ++a) {
      f32x4 bs = acc[a][0];
#pragma unroll
      for (int s = 0; s < 3; ++s) bs += *(const f32x4*)(smem + K1_RED + ((w * 3 + s) * 4 + a) * 1024 + lane * 16);
      const u32x2 yv = tr_read(smem + K1_YOFF + (16 * a + 4 * g + q) * 144 + (16 * w + 4 * pp) * 2);
      u32x2 o;
#pragma unroll
      for (int e = 0; e < 2; ++e) {
        const uint32_t sk = pk2(bflo(uv[a][e]) * bflo(dv[a][e]), bfhi(uv[a][e]) * bfhi(dv[a][e]));
        const uint32_t av = pk2(bflo(yv[e]) + bflo(sk), bfhi(yv[e]) + bfhi(sk));
        const uint32_t ga = pk2(gt * bflo(av), gt * bfhi(av));
        const uint32_t bb = pk2(bs[2 * e], bs[2 * e + 1]);
        const uint32_t gb = pk2(omg * bflo(bb), omg * bfhi(bb));
        o[e] = pk2(bflo(ga) + bflo(gb), bfhi(ga) + bfhi(gb));
      }
      *(u32x2*)(p.out + (size_t)t * D + d0 + 16 * a + 4 * g) = o;
    }
    TS(9);
  } else {
    const int cw = w - 4, c = cw >> 1, half = cw & 1, d = 2 * b + c;
    char* base = smem + c * CW_SIZE;
    {
      const u16* eg = p.E + (size_t)d * ES;
      u32x4 cur[3];
      u32x2 prv[3];
#pragma unroll
      for (int it = 0; it < 3; ++it) {
        const int un = it < 2 ? (2 * half + it) * 64 + lane : 256 + (lane & 1);
        asm volatile("global_load_dwordx4 %0, %2, %3\n\tglobal_load_dwordx2 %1, %2, %3 offset:-8"
                     : "=&v"(cur[it]), "=&v"(prv[it]) : "v"(un * 16u), "s"(eg) : "memory");
      }
      const uint32_t lb = lds_addr(base + CW_U + 512) + half * 2048;
#pragma unroll
      for (int i = 0; i < 2; ++i) glds16(p.uT + (size_t)d * L + (2 * half + i) * 512, lane * 16u, lb + i * 1024);
      if (lane < 32 && half == 0) *(u32x4*)(base + CW_U + lane * 16) = (u32x4){0, 0, 0, 0};
      asm volatile("s_waitcnt vmcnt(2)" : "+v"(cur[0]), "+v"(cur[1]), "+v"(cur[2]), "+v"(prv[0]), "+v"(prv[1]), "+v"(prv[2])::"memory");
      TS(2);
#pragma unroll
      for (int it = 0; it < 3; ++it) {
        const int un = it < 2 ? (2 * half + it) * 64 + lane : 256 + (lane & 1);
        const u32x4 cu = cur[it];
        const u32x2 pv = prv[it];
        u32x4 o1, o2, o3;
        o1[0] = __builtin_amdgcn_alignbyte(cu[0], pv[1], 2); o1[1] = __builtin_amdgcn_alignbyte(cu[1], cu[0], 2);
        o1[2] = __builtin_amdgcn_alignbyte(cu[2], cu[1], 2); o1[3] = __builtin_amdgcn_alignbyte(cu[3], cu[2], 2);
        o2[0] = pv[1]; o2[1] = cu[0]; o2[2] = cu[1]; o2[3] = cu[2];
        o3[0] = __builtin_amdgcn_alignbyte(pv[1], pv[0], 2); o3[1] = o1[0]; o3[2] = o1[1]; o3[3] = o1[2];
        if (it < 2 || (lane < 2 && half == 1)) {
          *(u32x4*)(base + 0 * CP_STRIDE + un * 16) = cu;
          *(u32x4*)(base + 1 * CP_STRIDE + un * 16) = o1;
          *(u32x4*)(base + 2 * CP_STRIDE + un * 16) = o2;
          *(u32x4*)(base + 3 * CP_STRIDE + un * 16) = o3;
        }
      }
    }
    WAIT_V(0);
    pair_sync(smem, cw, 0);
    __builtin_amdgcn_s_setprio(LPRIO);
    TS(3);
    f32x4 y[8];
#pragma unroll
    for (int a = 0; a < 8; ++a) y[a] = (f32x4){0, 0, 0, 0};
    {
      const int q = n >> 2, pp = lane & 3;
      const char* abase = base + q * CP_STRIDE + (16 - 8 * g + 4 * pp) * 2 + 32 * (8 * half + 1);
      const char* bbase = base + CW_U + 512 + (16 * n + 8 * g) * 2 - 32 * (8 * half + 1);
      conv_loop(abase, bbase, y);
    }
    TS(4);
    __builtin_amdgcn_s_setprio(3);
#pragma unroll
    for (int a = 0; a < 4; ++a) *(f32x4*)(smem + K1_XCH + (cw * 4 + a) * 1024 + lane * 16) = half ? y[a] : y[4 + a];
    pair_sync(smem, cw, 1);
    TS(8);
#pragma unroll
    for (int a = 0; a < 4; ++a) {
      const f32x4 o = *(const f32x4*)(smem + K1_XCH + ((cw ^ 1) * 4 + a) * 1024 + lane * 16);
      const f32x4 v = half ? o + y[4 + a] : y[a] + o;
      const int ntile = 4 * half + a;
      uint32_t v0 = pk2(v[0], v[1]), v1 = pk2(v[2], v[3]);
      v0 = v0 == ~0u ? 0xfffffffeu : v0;
      v1 = v1 == ~0u ? 0xfffffffeu : v1;
      __hip_atomic_store((gu64*)(p.yT + (size_t)d * L + 256 * ntile + 16 * n + 4 * g), (unsigned long long)v0 | ((unsigned long long)v1 << 32), RLX_AGENT);
    }
    __builtin_amdgcn_s_setprio(0);
    TS(5);
    {
      const int dd = 16 * cw + (lane >> 2), ch = lane & 3;
      const uint32_t off = (uint32_t)((d0 + dd) * L + t0 + ch * 8) * 2u;
      u32x4 v0, v1;
      for (unsigned spins = 0; spins < (1u << 22); ++spins) {
        uint32_t x;
        asm volatile("ds_read_b32 %0, %1\n\ts_waitcnt lgkmcnt(0)" : "=v"(x) : "v"(lds_addr(smem + K1_FLAG + 32 + cw * 4)) : "memory");
        if (__builtin_amdgcn_readfirstlane(x) == 1u) break;
        __builtin_amdgcn_s_sleep(2);
      }
      for (unsigned spins = 0; spins < (1u << 20); ++spins) {
        asm volatile("global_load_dwordx4 %0, %2, %3 sc1\n\tglobal_load_dwordx4 %1, %2, %3 offset:64 sc1\n\ts_waitcnt vmcnt(0)"
                     : "=&v"(v0), "=&v"(v1) : "v"(off), "s"(p.yT) : "memory");
        const bool ok = v0[0] != ~0u && v0[1] != ~0u && v0[2] != ~0u && v0[3] != ~0u && v1[0] != ~0u && v1[1] != ~0u && v1[2] != ~0u && v1[3] != ~0u;
        if (__all(ok)) break;
        __builtin_amdgcn_s_sleep(4);
      }
      *(u32x4*)(smem + K1_YOFF + dd * 144 + ch * 16) = v0;
      *(u32x4*)(smem + K1_YOFF + dd * 144 + ch * 16 + 64) = v1;
    }
    TS(6);
    WAIT_L(0);
    BAR();
  }
  TSDUMP(1);
}

extern "C" void kernel_launch(void* const* d_in, const int* in_sizes, int n_in, void* d_out, int out_size, void* d_ws, size_t ws_size,
                              hipStream_t stream) {
  char* ws = (char*)d_ws;
  P0 p0;
  p0.u = (const u16*)d_in[0]; p0.Are = (const u16*)d_in[1]; p0.Aim = (const u16*)d_in[2]; p0.Bre = (const u16*)d_in[3];
  p0.Bim = (const u16*)d_in[4]; p0.Cre = (const u16*)d_in[5]; p0.Cim = (const u16*)d_in[6]; p0.logdt = (const u16*)d_in[7];
  p0.wg = (const u16*)d_in[10]; p0.wu = (const u16*)d_in[11]; p0.wd = (const u16*)d_in[12]; p0.gw = (const u16*)d_in[13];
  p0.h = (u16*)(ws + WS_H); p0.wdt = (u16*)(ws + WS_WDT); p0.uT = (u16*)(ws + WS_UT); p0.E = (u16*)(ws + WS_E);
  p0.yT = (u16*)(ws + WS_YT); p0.g = (float*)(ws + WS_G);
  P1 p1;
  p1.u = p0.u; p1.Dskip = (const u16*)d_in[8]; p1.h = p0.h; p1.wdt = p0.wdt; p1.uT = p0.uT; p1.E = p0.E; p1.g = p0.g;
  p1.yT = p0.yT; p1.out = (u16*)d_out;
  (void)hipFuncSetAttribute((const void*)k0_kernel, hipFuncAttributeMaxDynamicSharedMemorySize, K0_LDS);
  (void)hipFuncSetAttribute((const void*)k1_kernel, hipFuncAttributeMaxDynamicSharedMemorySize, K1_LDS);
  k0_kernel<<<256, 512, K0_LDS, stream>>>(p0);
  k1_kernel<<<256, 512, K1_LDS, stream>>>(p1);
}
```

```cpp
#include <hip/hip_runtime.h>
#include <hip/hip_bf16.h>
#include <stdint.h>

typedef unsigned short u16;
typedef __attribute__((ext_vector_type(4))) short s16x4;
typedef __attribute__((ext_vector_type(8))) short s16x8;
typedef __attribute__((ext_vector_type(8))) __bf16 bf16x8;
typedef __attribute__((ext_vector_type(2))) __bf16 bf16x2;
typedef __attribute__((ext_vector_type(2))) float f32x2;
typedef __attribute__((ext_vector_type(4))) float f32x4;
typedef __attribute__((ext_vector_type(4))) unsigned u32x4;
typedef __attribute__((ext_vector_type(2))) unsigned u32x2;
typedef __attribute__((address_space(3))) s16x4* lds_s16x4_ptr;
typedef __attribute__((address_space(1))) unsigned gu32;
typedef __attribute__((address_space(1))) unsigned long long gu64;
#define RLX_AGENT __ATOMIC_RELAXED, __HIP_MEMORY_SCOPE_AGENT
#define DEV __device__ __forceinline__

constexpr int L = 2048, D = 512, NM = 128, F = 1792;
constexpr int KS2 = F / 32;

constexpr size_t WS_H = 0;
constexpr size_t WS_WDT = WS_H + (size_t)L * F * 2;
constexpr size_t WS_UT = WS_WDT + (size_t)F * D * 2;
constexpr size_t WS_YT = WS_UT + (size_t)L * D * 2;
constexpr int ES = 2080;
constexpr size_t WS_E = WS_YT + (size_t)L * D * 2 + 64;
constexpr size_t WS_G = WS_E + (size_t)D * ES * 2;

#define TSDECL
#define TS(ph)
#define TSDUMP(K)
#define TS_ARG
#define TS_PASS
DEV float bf2f(u16 v) { return __uint_as_float(((uint32_t)v) << 16); }
DEV float bflo(uint32_t v) { return __uint_as_float(v << 16); }
DEV float bfhi(uint32_t v) { return __uint_as_float(v & 0xffff0000u); }
DEV uint32_t pk2(float a, float b) { f32x2 v = {a, b}; return __builtin_bit_cast(uint32_t, __builtin_convertvector(v, bf16x2)); }
DEV float rbf(float a) { return bflo(pk2(a, 0.f) & 0xffffu); }
DEV f32x4 mfma16(u32x4 a, u32x4 b, f32x4 c) {
  return __builtin_amdgcn_mfma_f32_16x16x32_bf16(__builtin_bit_cast(bf16x8, a), __builtin_bit_cast(bf16x8, b), c, 0, 0, 0);
}
DEV u32x2 tr_read(const char* p) {
  return __builtin_bit_cast(u32x2, __builtin_amdgcn_ds_read_tr16_b64_v4i16((lds_s16x4_ptr)p));
}
DEV u32x4 tr_read2(const char* p0, const char* p1) {
  u32x2 a = tr_read(p0), b = tr_read(p1);
  u32x4 r = {a.x, a.y, b.x, b.y};
  return r;
}
DEV uint32_t lds_addr(const void* p) { return (uint32_t)(uintptr_t)(__attribute__((address_space(3))) const char*)p; }
DEV void glds16(const void* sbase, uint32_t voff, uint32_t lds) {
  unsigned keep;
  asm volatile("s_mov_b32 %0, m0\n\ts_mov_b32 m0, %3\n\ts_nop 0\n\tglobal_load_lds_dwordx4 %1, %2\n\ts_mov_b32 m0, %0"
               : "=&s"(keep) : "v"(voff), "s"(sbase), "s"(lds) : "memory");
}
#define WAIT_V(n) asm volatile("s_waitcnt vmcnt(" #n ")" ::: "memory")
#define WAIT_L(n) asm volatile("s_waitcnt lgkmcnt(" #n ")" ::: "memory")
#define BAR() __builtin_amdgcn_s_barrier()

template <bool WT>
DEV void st16(void* base, size_t byte_off, u32x4 v) {
  if (WT) {
    __builtin_amdgcn_raw_buffer_store_b128(v, __builtin_amdgcn_make_buffer_rsrc(base, 0, 0x7fffffff, 0x00020000), (uint32_t)byte_off, 0, 16);
  } else {
    *(u32x4*)((char*)base + byte_off) = v;
  }
}
#define WT_AUX 1
#define WT_H 1

struct P0 {
  const u16 *u, *Are, *Aim, *Bre, *Bim, *Cre, *Cim, *logdt, *wg, *wu, *wd, *gw;
  u16 *h, *wdt, *uT, *E, *yT;
  float* g;
};

constexpr int STAGE_B = 49152;
constexpr int K0_LDS = 3 * STAGE_B + 15360;

DEV int wswz(int row) { return ((row & 3) << 2) | ((row >> 2) & 3); }

struct SsmPar { u32x4 are, aim, bre, bim, cre, cim; };

DEV void ssm_load(const P0& p, uint32_t off, SsmPar& s) {
  asm volatile("global_load_dwordx4 %0, %1, %2" : "=&v"(s.are) : "v"(off), "s"(p.Are) : "memory");
  asm volatile("global_load_dwordx4 %0, %1, %2" : "=&v"(s.aim) : "v"(off), "s"(p.Aim) : "memory");
  asm volatile("global_load_dwordx4 %0, %1, %2" : "=&v"(s.bre) : "v"(off), "s"(p.Bre) : "memory");
  asm volatile("global_load_dwordx4 %0, %1, %2" : "=&v"(s.bim) : "v"(off), "s"(p.Bim) : "memory");
  asm volatile("global_load_dwordx4 %0, %1, %2" : "=&v"(s.cre) : "v"(off), "s"(p.Cre) : "memory");
  asm volatile("global_load_dwordx4 %0, %1, %2" : "=&v"(s.cim) : "v"(off), "s"(p.Cim) : "memory");
}
#define SSM_WAIT(n, s) asm volatile("s_waitcnt vmcnt(" #n ")" : "+v"(s.are), "+v"(s.aim), "+v"(s.bre), "+v"(s.bim), "+v"(s.cre), "+v"(s.cim)::"memory")

DEV void kgen_step(const SsmPar& sp, int kk, float dt, float p0, float p1, float p2, float q, f32x4 (&acc)[3]) {
  u32x4 ah[3], al[3], bh, bl;
#pragma unroll
  for (int j = 0; j < 4; ++j) {
    const int wi_ = 2 * kk + (j >> 1);
    const uint32_t wa = sp.are[wi_], wi = sp.aim[wi_], wbr = sp.bre[wi_], wbi = sp.bim[wi_], wcr = sp.cre[wi_], wci = sp.cim[wi_];
    const float ar = dt * ((j & 1) ? bfhi(wa) : bflo(wa)), ai = dt * ((j & 1) ? bfhi(wi) : bflo(wi));
    const float br = dt * ((j & 1) ? bfhi(wbr) : bflo(wbr)), bi = dt * ((j & 1) ? bfhi(wbi) : bflo(wbi));
    const float cr = (j & 1) ? bfhi(wcr) : bflo(wcr), ci = (j & 1) ? bfhi(wci) : bflo(wci);
    const float a2 = ar * 1.44269504088896340736f, wv = ai * 0.15915494309189533577f;
    const float xr = cr * br - ci * bi, xi = cr * bi + ci * br;
    {
      const float e = __builtin_amdgcn_exp2f(a2 * q), r = __builtin_amdgcn_fractf(wv * q);
      const float s = __builtin_amdgcn_sinf(r) * e, co = __builtin_amdgcn_cosf(r) * e;
      const float vr = xr * co - xi * s, vi = -(xr * s + xi * co);
      const uint32_t hi = pk2(vr, vi);
      bh[j] = hi;
      bl[j] = pk2(vr - bflo(hi), vi - bfhi(hi));
    }
#pragma unroll
    for (int T = 0; T < 3; ++T) {
      const float pp = T == 0 ? p0 : (T == 1 ? p1 : p2);
      const float e = __builtin_amdgcn_exp2f(a2 * pp), r = __builtin_amdgcn_fractf(wv * pp);
      const float wr = __builtin_amdgcn_cosf(r) * e, wim = __builtin_amdgcn_sinf(r) * e;
      const uint32_t hi = pk2(wr, wim);
      ah[T][j] = hi;
      al[T][j] = pk2(wr - bflo(hi), wim - bfhi(hi));
    }
  }
#pragma unroll
  for (int T = 0; T < 3; ++T) {
    acc[T] = mfma16(al[T], bh, acc[T]);
    acc[T] = mfma16(ah[T], bl, acc[T]);
    acc[T] = mfma16(ah[T], bh, acc[T]);
  }
}

DEV void kgen_waves(const P0& p, char* scratch, int b, bool aux, int tid, uint32_t ld2 TS_ARG) {
  const int t = tid - 512, kw = __builtin_amdgcn_readfirstlane(t >> 6), lane = t & 63, i = lane & 15, ch0 = 2 * b;
  SsmPar sp0, sp1;
  const uint32_t off = (uint32_t)((ch0 + (kw >> 1)) * NM + 64 * (kw & 1) + 8 * (lane >> 4)) * 2u;
  ssm_load(p, off, sp0);
  ssm_load(p, off + 64u, sp1);
  __builtin_amdgcn_sched_barrier(0);
  BAR();
  __builtin_amdgcn_sched_barrier(0);
  st16<WT_AUX>(p.yT, ((size_t)b * 4096 + t * 8) * 2, (u32x4){~0u, ~0u, ~0u, ~0u});
  st16<WT_AUX>(p.yT, ((size_t)b * 4096 + 2048 + t * 8) * 2, (u32x4){~0u, ~0u, ~0u, ~0u});
  const float dt = rbf(__expf((kw >> 1) ? bfhi(ld2) : bflo(ld2)));
  const float p0 = (float)((i & 7) + 128 * (i >> 3));
  const float p1 = (i < 8) ? (float)(256 + 2 * (i & 3) + 128 * (i >> 2)) : (float)(512 + 4 * (i & 1) + 128 * ((i - 8) >> 1));
  const float p2 = (i < 8) ? (float)(1024 + 128 * i) : 2048.f;
  const float q = (float)(8 * i);
  f32x4 acc[3] = {{0, 0, 0, 0}, {0, 0, 0, 0}, {0, 0, 0, 0}};
  SSM_WAIT(8, sp0);
  TS(1);
  if (!aux) BAR();
  kgen_step(sp0, 0, dt, p0, p1, p2, q, acc);
  __builtin_amdgcn_sched_barrier(0);
  BAR();
  kgen_step(sp0, 1, dt, p0, p1, p2, q, acc);
  __builtin_amdgcn_sched_barrier(0);
  if (!aux) BAR();
  SSM_WAIT(2, sp1);
  kgen_step(sp1, 0, dt, p0, p1, p2, q, acc);
  __builtin_amdgcn_sched_barrier(0);
  if (!aux) BAR();
  kgen_step(sp1, 1, dt, p0, p1, p2, q, acc);
  float* sc = (float*)scratch;
  u16* kc = (u16*)(scratch + 12288);
#pragma unroll
  for (int T = 0; T < 3; ++T) *(f32x4*)(sc + ((kw * 3 + T) * 64 + lane) * 4) = acc[T];
  WAIT_L(0);
  TS(15);
  BAR();
  {
    const int ln = t >> 2, j = t & 3;
    const int ii = 4 * (ln >> 4) + j, bb = ln & 15;
#pragma unroll
    for (int cc = 0; cc < 2; ++cc)
#pragma unroll
      for (int T = 0; T < 3; ++T) {
        const float s = sc[((cc * 2) * 3 + T) * 256 + t] + sc[((cc * 2 + 1) * 3 + T) * 256 + t];
        int idx;
        if (T == 0) idx = (ii & 7) + 128 * (ii >> 3) + 8 * bb;
        else if (T == 1) idx = ii < 8 ? 256 + (ii & 3) + 64 * (ii >> 2) + 4 * bb : 384 + (ii & 1) + 32 * ((ii - 8) >> 1) + 2 * bb;
        else idx = ii < 8 ? 512 + 16 * ii + bb : ((ii == 8 && bb == 0) ? 640 : 700);
        kc[cc * 768 + idx] = (u16)(pk2(s, 0.f) & 0xffffu);
      }
  }
  WAIT_L(0);
  BAR();
  {
    const int l0 = 8 * t;
    int ix[8];
#pragma unroll
    for (int e = 0; e < 4; ++e) {
      const int la = l0 + 2 * e, lb = la + 1;
      const uint32_t pb = pk2((float)la, (float)lb);
      ix[2 * e] = la < 128 ? la : (int)(pb & 0xffffu) - 17024;
      ix[2 * e + 1] = lb < 128 ? lb : (int)(pb >> 16) - 17024;
    }
#pragma unroll
    for (int cc = 0; cc < 2; ++cc) {
      const u16* k = kc + cc * 768;
      u32x4 o;
#pragma unroll
      for (int e = 0; e < 4; ++e) o[e] = (uint32_t)k[ix[2 * e]] | ((uint32_t)k[ix[2 * e + 1]] << 16);
      st16<WT_AUX>(p.E, ((size_t)(ch0 + cc) * ES + 16 + l0) * 2, o);
    }
  }
  TS(14);
  if (!aux) { BAR(); BAR(); }
}

DEV void k0_glds(const P0& p, uint32_t lb, const uint32_t (&voff)[3], int m0, int f0, int k0, int piece) {
  if (piece < 2) glds16(p.u + (size_t)m0 * D + k0, voff[0] + piece * (8 * D * 2), lb + piece * 1024);
  else if (piece < 4) glds16(p.wg + (size_t)k0 * F + f0, voff[piece - 1], lb + 16384 + (piece - 2) * 1024);
  else glds16(p.wu + (size_t)k0 * F + f0, voff[piece - 3], lb + 32768 + (piece - 4) * 1024);
}

__global__ __launch_bounds__(768, 3) void k0_kernel(P0 p) {
  extern __shared__ __attribute__((aligned(16))) char smem[];
  const int tid = threadIdx.x, lane = tid & 63, w = __builtin_amdgcn_readfirstlane(tid >> 6);
  const int b = blockIdx.x, xcd = b & 7, idx = b >> 3;
  TSDECL;
  TS(0);
  if (w >= 8) {
    const uint32_t ld2 = *(const uint32_t*)(p.logdt + 2 * b);
    kgen_waves(p, smem + 3 * STAGE_B, b, idx >= 28, tid, ld2 TS_PASS);
  } else if (idx < 28) {
    const int mt = (xcd >> 1) * 4 + idx / 7, ft = (xcd & 1) * 7 + idx % 7;
    const int m0 = mt * 128, f0 = ft * 128;
#define K0ROT 2
    const int rot = K0ROT == 0 ? 2 * (idx / 7) + idx % 7 : K0ROT == 1 ? 0 : K0ROT == 2 ? (idx & 1) * 4 : K0ROT == 3 ? 2 * (idx / 7) : idx % 7;
    uint32_t voff[3];
    {
      const int row = 16 * w + (lane >> 3);
      voff[0] = (uint32_t)(row * D + 8 * ((lane & 7) ^ (row & 7))) * 2u;
      const int r0 = 8 * w + (lane >> 4), r1 = r0 + 4;
      voff[1] = (uint32_t)(r0 * F + 8 * ((lane & 15) ^ wswz(r0))) * 2u;
      voff[2] = (uint32_t)(r1 * F + 8 * ((lane & 15) ^ wswz(r1))) * 2u;
    }
    const uint32_t lb = lds_addr(smem) + 2048 * w;
#pragma unroll
    for (int s = 0; s < 3; ++s) {
      if (s == 1) BAR();
#pragma unroll
      for (int pc = 0; pc < 6; ++pc) k0_glds(p, lb + s * STAGE_B, voff, m0, f0, 64 * ((rot + s) & 7), pc);
    }
    const int wm = w >> 2, wn = w & 3, g = lane >> 4, q = (lane & 15) >> 2, pp = lane & 3;
    f32x4 acc[2][2][4];
#pragma unroll
    for (int a = 0; a < 2; ++a)
#pragma unroll
      for (int c = 0; c < 2; ++c)
#pragma unroll
        for (int m = 0; m < 4; ++m) acc[a][c][m] = (f32x4){0, 0, 0, 0};
    u32x4 xf[2][4], wf[2][2][2];
    auto frags = [&](const char* buf, int ss) {
#pragma unroll
      for (int m = 0; m < 4; ++m) {
        const int row = 64 * wm + 16 * m + (lane & 15), c = 4 * ss + g;
        xf[ss][m] = *(const u32x4*)(buf + row * 128 + ((c ^ (row & 7)) << 4));
      }
#pragma unroll
      for (int a = 0; a < 2; ++a)
#pragma unroll
        for (int c = 0; c < 2; ++c) {
          const int ch = 4 * wn + 2 * c + (pp >> 1);
          const int r0 = 32 * ss + 8 * g + q, r1 = r0 + 4;
          const char* wb = buf + 16384 + a * 16384 + 8 * (pp & 1);
          wf[ss][a][c] = tr_read2(wb + 256 * r0 + 16 * (ch ^ wswz(r0)), wb + 256 * r1 + 16 * (ch ^ wswz(r1)));
        }
    };
    auto mm = [&](int ss, bool stage, int sb, int k0, int pc0) {
#pragma unroll
      for (int ac = 0; ac < 4; ++ac) {
#pragma unroll
        for (int m = 0; m < 4; ++m) acc[ac >> 1][ac & 1][m] = mfma16(wf[ss][ac >> 1][ac & 1], xf[ss][m], acc[ac >> 1][ac & 1][m]);
        if (stage && ac < 3) {
          __builtin_amdgcn_sched_barrier(0);
          k0_glds(p, lb + sb * STAGE_B, voff, m0, f0, k0, pc0 + ac);
          __builtin_amdgcn_sched_barrier(0);
        }
      }
    };
#pragma unroll
    for (int s = 0; s < 8; ++s) {
      if (s == 7) WAIT_V(0); else if (s == 0) WAIT_V(12); else WAIT_V(6);
      WAIT_L(0);
      BAR();
      TS(3 + s);
      int bo = (s % 3) * STAGE_B;
      asm volatile("" : "+s"(bo));
      const char* buf = smem + bo;
      const bool st = (s >= 1 && s + 2 < 8);
      const int k0 = 64 * ((rot + s + 2) & 7);
      frags(buf, 0);
      __builtin_amdgcn_sched_barrier(0);
      if (s > 0) mm(1, st, (s + 2) % 3, k0, 0);
      __builtin_amdgcn_sched_barrier(0);
      frags(buf, 1);
      __builtin_amdgcn_sched_barrier(0);
      mm(0, st, (s + 2) % 3, k0, 3);
      __builtin_amdgcn_sched_barrier(0);
    }
    mm(1, false, 0, 0, 0);
    TS(11);
#pragma unroll
    for (int m = 0; m < 4; ++m) {
      u32x4 o;
#pragma unroll
      for (int c = 0; c < 2; ++c)
#pragma unroll
        for (int jj = 0; jj < 2; ++jj) {
          const uint32_t g2 = pk2(acc[0][c][m][2 * jj], acc[0][c][m][2 * jj + 1]), u2 = pk2(acc[1][c][m][2 * jj], acc[1][c][m][2 * jj + 1]);
          const float g0 = bflo(g2), g1 = bfhi(g2);
          const uint32_t s2 = pk2(g0 * __builtin_amdgcn_rcpf(1.f + __expf(-g0)), g1 * __builtin_amdgcn_rcpf(1.f + __expf(-g1)));
          o[2 * c + jj] = pk2(bflo(s2) * bflo(u2), bfhi(s2) * bfhi(u2));
        }
      const int mt64 = (m0 >> 6) + wm, ks = 4 * ft + wn;
      st16<WT_H>(p.h, ((size_t)((mt64 * KS2 + ks) * 4 + m) * 64 + lane) * 16, o);
    }
    TS(12);
  } else {
    BAR();
    const int a = xcd * 4 + (idx - 28);
    constexpr int RS = 1056;
    const int t0 = 64 * a;
    u32x4 uv[8], wv[8];
#pragma unroll
    for (int r = 0; r < 8; ++r) uv[r] = *(const u32x4*)(p.u + (size_t)(t0 + 8 * w + r) * D + lane * 8);
    const int ks2 = a + 32 < KS2 ? a + 32 : a;
#pragma unroll
    for (int r = 0; r < 4; ++r) {
      wv[r] = *(const u32x4*)(p.wd + (size_t)(32 * a + 4 * w + r) * D + lane * 8);
      wv[4 + r] = *(const u32x4*)(p.wd + (size_t)(32 * ks2 + 4 * w + r) * D + lane * 8);
    }
    const u32x4 gwv = *(const u32x4*)(p.gw + lane * 8);
    {
      float s[8];
#pragma unroll
      for (int r = 0; r < 8; ++r) {
        *(u32x4*)(smem + (8 * w + r) * RS + lane * 16) = uv[r];
        s[r] = 0.f;
#pragma unroll
        for (int e = 0; e < 4; ++e) {
          const uint32_t pr = pk2(bflo(uv[r][e]) * bflo(gwv[e]), bfhi(uv[r][e]) * bfhi(gwv[e]));
          s[r] += bflo(pr) + bfhi(pr);
        }
      }
#pragma unroll
      for (int r = 0; r < 4; ++r) { const bool hi = lane & 32; const float keep = hi ? s[r + 4] : s[r], send = hi ? s[r] : s[r + 4]; s[r] = keep + __shfl_xor(send, 32); }
#pragma unroll
      for (int r = 0; r < 2; ++r) { const bool hi = lane & 16; const float keep = hi ? s[r + 2] : s[r], send = hi ? s[r] : s[r + 2]; s[r] = keep + __shfl_xor(send, 16); }
      { const bool hi = lane & 8; const float keep = hi ? s[1] : s[0], send = hi ? s[0] : s[1]; s[0] = keep + __shfl_xor(send, 8); }
      s[0] += __shfl_xor(s[0], 4); s[0] += __shfl_xor(s[0], 2); s[0] += __shfl_xor(s[0], 1);
      if ((lane & 7) == 0) {
        const int r = ((lane >> 5) & 1) * 4 + ((lane >> 4) & 1) * 2 + ((lane >> 3) & 1);
        const float gs = rbf(s[0]);
        p.g[t0 + 8 * w + r] = rbf(1.f / (1.f + __expf(-gs)));
      }
    }
    WAIT_L(0);
    BAR();
    const int g = lane >> 4, q = (lane & 15) >> 2, pp = lane & 3;
#pragma unroll
    for (int c = 0; c < 4; ++c)
#pragma unroll
      for (int hh = 0; hh < 2; ++hh) {
        const int dt_ = 4 * w + c;
        const int r0 = 32 * hh + 8 * g + q;
        const char* base = smem + (16 * dt_ + 4 * pp) * 2;
        const u32x4 v = tr_read2(base + r0 * RS, base + (r0 + 4) * RS);
        st16<WT_AUX>(p.uT, ((size_t)(16 * dt_ + (lane & 15)) * L + t0 + 32 * hh + 8 * g) * 2, v);
      }
    TS(3);
    WAIT_L(0);
    BAR();
#pragma unroll
    for (int r = 0; r < 4; ++r) {
      *(u32x4*)(smem + (4 * w + r) * RS + lane * 16) = wv[r];
      *(u32x4*)(smem + (32 + 4 * w + r) * RS + lane * 16) = wv[4 + r];
    }
    WAIT_L(0);
    BAR();
#pragma unroll
    for (int hh = 0; hh < 2; ++hh) {
      if (hh == 0 || a + 32 < KS2) {
        const int ks = a + 32 * hh;
#pragma unroll
        for (int c = 0; c < 4; ++c) {
          const int nt = 4 * w + c;
          const char* base = smem + 32 * hh * RS + (16 * nt + 4 * pp) * 2;
          const u32x4 v = tr_read2(base + (4 * g + q) * RS, base + (16 + 4 * g + q) * RS);
          st16<WT_AUX>(p.wdt, ((size_t)(((nt >> 2) * KS2 + ks) * 4 + (nt & 3)) * 64 + lane) * 16, v);
        }
      }
    }
    TS(12);
  }
  TSDUMP(0);
}

#define GO_STEP 7
#define FPRIO 1
#define LPRIO 0
struct P1 {
  const u16 *u, *Dskip, *h, *wdt, *uT, *E;
  const float* g;
  u16 *yT, *out;
};

constexpr int CP_STRIDE = 4160;
constexpr int CW_U = 4 * CP_STRIDE, CW_SIZE = CW_U + 4608;
constexpr int K1_XCH = 2 * CW_SIZE;
constexpr int K1_YOFF = K1_XCH + 16384;
constexpr int K1_RED = K1_YOFF + 64 * 144;
constexpr int K1_FLAG = K1_RED + 49152;
constexpr int K1_LDS = K1_FLAG + 64;

DEV void conv_loop(const char* abase, const char* bbase, f32x4 (&y)[8]) {
  u32x4 af[2][8], bf[2][8];
  auto ld = [&](int buf, int it) {
#pragma unroll
    for (int m = 0; m < 8; ++m) bf[buf][m] = *(const u32x4*)(bbase + 512 * m - 64 * it);
#pragma unroll
    for (int m = 0; m < 8; ++m) {
      const char* ap = abase + 512 * m + 64 * it;
      af[buf][m] = tr_read2(ap, ap - 8);
    }
  };
  ld(0, 0);
#pragma unroll
  for (int it = 0; it < 4; ++it) {
    const int cur = it & 1;
    __builtin_amdgcn_sched_barrier(0);
    if (it < 3) ld(cur ^ 1, it + 1);
#pragma unroll
    for (int m = 0; m < 8; ++m)
#pragma unroll
      for (int T = m; T < 8; ++T) y[T] = mfma16(af[cur][m], bf[cur][T - m], y[T]);
    if (it < 3) {
#pragma unroll
      for (int i = 0; i < 12; ++i) {
        __builtin_amdgcn_sched_group_barrier(0x008, 3, 0);
        __builtin_amdgcn_sched_group_barrier(0x100, 2, 0);
      }
    }
  }
  __builtin_amdgcn_sched_barrier(0);
}
DEV void pair_sync(char* smem, int cw, int which) {
  const uint32_t mine = lds_addr(smem + K1_FLAG + cw * 8 + which * 4), other = mine ^ 8u;
  asm volatile("s_waitcnt lgkmcnt(0)\n\tds_write_b32 %0, %1" ::"v"(mine), "v"(1u) : "memory");
  for (unsigned spins = 0; spins < (1u << 24); ++spins) {
    uint32_t x;
    asm volatile("ds_read_b32 %0, %1\n\ts_waitcnt lgkmcnt(0)" : "=v"(x) : "v"(other) : "memory");
    if (__builtin_amdgcn_readfirstlane(x) == 1u) break;
  }
}

__global__ __launch_bounds__(512, 2) void k1_kernel(P1 p) {
  extern __shared__ __attribute__((aligned(16))) char smem[];
  const int tid = threadIdx.x, lane = tid & 63, w = __builtin_amdgcn_readfirstlane(tid >> 6);
  const int b = blockIdx.x, xcd = b & 7, idx = b >> 3;
  const int mt = (xcd >> 1) * 8 + (idx >> 2), nt = (xcd & 1) * 4 + (idx & 3);
  const int t0 = mt * 64, d0 = nt * 64;
  const int g = lane >> 4, n = lane & 15;
  TSDECL;
  TS(0);
  if (w >= 4) __builtin_amdgcn_s_setprio(3);
  if (w == 4) {
    asm volatile("ds_write_b32 %0, %1\n\ts_waitcnt lgkmcnt(0)" ::"v"(lds_addr(smem + K1_FLAG + (lane & 15) * 4)), "v"(0u) : "memory");
  }
  BAR();
  __builtin_amdgcn_sched_barrier(0);
  if (w < 4) {
    __builtin_amdgcn_s_setprio(FPRIO);
    f32x4 acc[4][4];
#pragma unroll
    for (int a = 0; a < 4; ++a)
#pragma unroll
      for (int c = 0; c < 4; ++c) acc[a][c] = (f32x4){0, 0, 0, 0};
    const u16* hp = p.h + ((size_t)mt * KS2 + 14 * w) * 2048 + lane * 8;
    const u16* wp = p.wdt + ((size_t)nt * KS2 + 14 * w) * 2048 + lane * 8;
#define PFD 4
    constexpr int PF = PFD;
    u32x4 hf[PF][4], wf[PF][4];
    const int hrot = w * 512;
#pragma unroll
    for (int s = 0; s < PF; ++s)
#pragma unroll
      for (int a = 0; a < 4; ++a) {
        hf[s][a] = *(const u32x4*)(hp + s * 2048 + ((a * 512 + hrot) & 2047));
        wf[s][a] = *(const u32x4*)(wp + s * 2048 + a * 512);
      }
    const int t = t0 + 16 * w + n;
    u32x2 uv[4], dv[4];
    float gt;
    __builtin_amdgcn_sched_barrier(0);
    TS(15);
    __builtin_amdgcn_sched_barrier(0);
#pragma unroll
    for (int s = 0; s < 14; ++s) {
#pragma unroll
      for (int a = 0; a < 4; ++a)
#pragma unroll
        for (int c = 0; c < 4; ++c) acc[a][c] = mfma16(wf[s % PF][a], hf[s % PF][c], acc[a][c]);
      __builtin_amdgcn_sched_barrier(0);
      if (s + PF < 14) {
#pragma unroll
        for (int a = 0; a < 4; ++a) {
          hf[s % PF][a] = *(const u32x4*)(hp + (s + PF) * 2048 + ((a * 512 + hrot) & 2047));
          wf[s % PF][a] = *(const u32x4*)(wp + (s + PF) * 2048 + a * 512);
        }
      }
      if (s == GO_STEP) asm volatile("ds_write_b32 %0, %1" ::"v"(lds_addr(smem + K1_FLAG + 32 + w * 4)), "v"(1u) : "memory");
      if (s + PF == 14) {
#pragma unroll
        for (int a = 0; a < 4; ++a) {
          uv[a] = *(const u32x2*)(p.u + (size_t)t * D + d0 + 16 * a + 4 * g);
          dv[a] = *(const u32x2*)(p.Dskip + d0 + 16 * a + 4 * g);
        }
        gt = p.g[t];
      }
      __builtin_amdgcn_sched_barrier(0);
    }
    TS(1);
#pragma unroll
    for (int c = 1; c < 4; ++c)
#pragma unroll
      for (int a = 0; a < 4; ++a) *(f32x4*)(smem + K1_RED + ((((w + c) & 3) * 3 + (c - 1)) * 4 + a) * 1024 + lane * 16) = acc[a][c];
    WAIT_L(0);
    BAR();
    TS(7);
    const float omg = rbf(1.f - gt);
    const int q = n >> 2, pp = lane & 3;
#pragma unroll
    for (int a = 0; a < 4; ++a) {
      f32x4 bs = acc[a][0];
#pragma unroll
      for (int s = 0; s < 3; ++s) bs += *(const f32x4*)(smem + K1_RED + ((w * 3 + s) * 4 + a) * 1024 + lane * 16);
      const u32x2 yv = tr_read(smem + K1_YOFF + (16 * a + 4 * g + q) * 144 + (16 * w + 4 * pp) * 2);
      u32x2 o;
#pragma unroll
      for (int e = 0; e < 2; ++e) {
        const uint32_t sk = pk2(bflo(uv[a][e]) * bflo(dv[a][e]), bfhi(uv[a][e]) * bfhi(dv[a][e]));
        const uint32_t av = pk2(bflo(yv[e]) + bflo(sk), bfhi(yv[e]) + bfhi(sk));
        const uint32_t ga = pk2(gt * bflo(av), gt * bfhi(av));
        const uint32_t bb = pk2(bs[2 * e], bs[2 * e + 1]);
        const uint32_t gb = pk2(omg * bflo(bb), omg * bfhi(bb));
        o[e] = pk2(bflo(ga) + bflo(gb), bfhi(ga) + bfhi(gb));
      }
      *(u32x2*)(p.out + (size_t)t * D + d0 + 16 * a + 4 * g) = o;
    }
    TS(9);
  } else {
    const int cw = w - 4, c = cw >> 1, half = cw & 1, d = 2 * b + c;
    char* base = smem + c * CW_SIZE;
    {
      const u16* eg = p.E + (size_t)d * ES;
      u32x4 cur[3];
      u32x2 prv[3];
#pragma unroll
      for (int it = 0; it < 3; ++it) {
        const int un = it < 2 ? (2 * half + it) * 64 + lane : 256 + (lane & 1);
        asm volatile("global_load_dwordx4 %0, %2, %3\n\tglobal_load_dwordx2 %1, %2, %3 offset:-8"
                     : "=&v"(cur[it]), "=&v"(prv[it]) : "v"(un * 16u), "s"(eg) : "memory");
      }
      const uint32_t lb = lds_addr(base + CW_U + 512) + half * 2048;
#pragma unroll
      for (int i = 0; i < 2; ++i) glds16(p.uT + (size_t)d * L + (2 * half + i) * 512, lane * 16u, lb + i * 1024);
      if (lane < 32 && half == 0) *(u32x4*)(base + CW_U + lane * 16) = (u32x4){0, 0, 0, 0};
      asm volatile("s_waitcnt vmcnt(2)" : "+v"(cur[0]), "+v"(cur[1]), "+v"(cur[2]), "+v"(prv[0]), "+v"(prv[1]), "+v"(prv[2])::"memory");
      TS(2);
      if (half == 0) {
        if (lane < 2) cur[0] = (u32x4){0, 0, 0, 0};
        if (lane < 3) prv[0] = (u32x2){0, 0};
      }
#pragma unroll
      for (int it = 0; it < 3; ++it) {
        const int un = it < 2 ? (2 * half + it) * 64 + lane : 256 + (lane & 1);
        const u32x4 cu = cur[it];
        const u32x2 pv = prv[it];
        u32x4 o1, o2, o3;
        o1[0] = __builtin_amdgcn_alignbyte(cu[0], pv[1], 2); o1[1] = __builtin_amdgcn_alignbyte(cu[1], cu[0], 2);
        o1[2] = __builtin_amdgcn_alignbyte(cu[2], cu[1], 2); o1[3] = __builtin_amdgcn_alignbyte(cu[3], cu[2], 2);
        o2[0] = pv[1]; o2[1] = cu[0]; o2[2] = cu[1]; o2[3] = cu[2];
        o3[0] = __builtin_amdgcn_alignbyte(pv[1], pv[0], 2); o3[1] = o1[0]; o3[2] = o1[1]; o3[3] = o1[2];
        if (it < 2 || (lane < 2 && half == 1)) {
          *(u32x4*)(base + 0 * CP_STRIDE + un * 16) = cu;
          *(u32x4*)(base + 1 * CP_STRIDE + un * 16) = o1;
          *(u32x4*)(base + 2 * CP_STRIDE + un * 16) = o2;
          *(u32x4*)(base + 3 * CP_STRIDE + un * 16) = o3;
        }
      }
    }
    WAIT_V(0);
    pair_sync(smem, cw, 0);
    __builtin_amdgcn_s_setprio(LPRIO);
    TS(3);
    f32x4 y[8];
#pragma unroll
    for (int a = 0; a < 8; ++a) y[a] = (f32x4){0, 0, 0, 0};
    {
      const int q = n >> 2, pp = lane & 3;
      const char* abase = base + q * CP_STRIDE + (16 - 8 * g + 4 * pp) * 2 + 32 * (8 * half + 1);
      const char* bbase = base + CW_U + 512 + (16 * n + 8 * g) * 2 - 32 * (8 * half + 1);
      conv_loop(abase, bbase, y);
    }
    TS(4);
    __builtin_amdgcn_s_setprio(3);
#pragma unroll
    for (int a = 0; a < 4; ++a) *(f32x4*)(smem + K1_XCH + (cw * 4 + a) * 1024 + lane * 16) = half ? y[a] : y[4 + a];
    pair_sync(smem, cw, 1);
    TS(8);
#pragma unroll
    for (int a = 0; a < 4; ++a) {
      const f32x4 o = *(const f32x4*)(smem + K1_XCH + ((cw ^ 1) * 4 + a) * 1024 + lane * 16);
      const f32x4 v = half ? o + y[4 + a] : y[a] + o;
      const int ntile = 4 * half + a;
      uint32_t v0 = pk2(v[0], v[1]), v1 = pk2(v[2], v[3]);
      v0 = v0 == ~0u ? 0xfffffffeu : v0;
      v1 = v1 == ~0u ? 0xfffffffeu : v1;
      __hip_atomic_store((gu64*)(p.yT + (size_t)d * L + 256 * ntile + 16 * n + 4 * g), (unsigned long long)v0 | ((unsigned long long)v1 << 32), RLX_AGENT);
    }
    __builtin_amdgcn_s_setprio(0);
    TS(5);
    {
      const int dd = 16 * cw + (lane >> 2), ch = lane & 3;
      const uint32_t off = (uint32_t)((d0 + dd) * L + t0 + ch * 8) * 2u;
      u32x4 v0, v1;
      for (unsigned spins = 0; spins < (1u << 22); ++spins) {
        uint32_t x;
        asm volatile("ds_read_b32 %0, %1\n\ts_waitcnt lgkmcnt(0)" : "=v"(x) : "v"(lds_addr(smem + K1_FLAG + 32 + cw * 4)) : "memory");
        if (__builtin_amdgcn_readfirstlane(x) == 1u) break;
        __builtin_amdgcn_s_sleep(2);
      }
      for (unsigned spins = 0; spins < (1u << 20); ++spins) {
        asm volatile("global_load_dwordx4 %0, %2, %3 sc1\n\tglobal_load_dwordx4 %1, %2, %3 offset:64 sc1\n\ts_waitcnt vmcnt(0)"
                     : "=&v"(v0), "=&v"(v1) : "v"(off), "s"(p.yT) : "memory");
        const bool ok = v0[0] != ~0u && v0[1] != ~0u && v0[2] != ~0u && v0[3] != ~0u && v1[0] != ~0u && v1[1] != ~0u && v1[2] != ~0u && v1[3] != ~0u;
        if (__all(ok)) break;
        __builtin_amdgcn_s_sleep(4);
      }
      *(u32x4*)(smem + K1_YOFF + dd * 144 + ch * 16) = v0;
      *(u32x4*)(smem + K1_YOFF + dd * 144 + ch * 16 + 64) = v1;
    }
    TS(6);
    WAIT_L(0);
    BAR();
  }
  TSDUMP(1);
}

extern "C" void kernel_launch(void* const* d_in, const int* in_sizes, int n_in, void* d_out, int out_size, void* d_ws, size_t ws_size,
                              hipStream_t stream) {
  char* ws = (char*)d_ws;
  P0 p0;
  p0.u = (const u16*)d_in[0]; p0.Are = (const u16*)d_in[1]; p0.Aim = (const u16*)d_in[2]; p0.Bre = (const u16*)d_in[3];
  p0.Bim = (const u16*)d_in[4]; p0.Cre = (const u16*)d_in[5]; p0.Cim = (const u16*)d_in[6]; p0.logdt = (const u16*)d_in[7];
  p0.wg = (const u16*)d_in[10]; p0.wu = (const u16*)d_in[11]; p0.wd = (const u16*)d_in[12]; p0.gw = (const u16*)d_in[13];
  p0.h = (u16*)(ws + WS_H); p0.wdt = (u16*)(ws + WS_WDT); p0.uT = (u16*)(ws + WS_UT); p0.E = (u16*)(ws + WS_E);
  p0.yT = (u16*)(ws + WS_YT); p0.g = (float*)(ws + WS_G);
  P1 p1;
  p1.u = p0.u; p1.Dskip = (const u16*)d_in[8]; p1.h = p0.h; p1.wdt = p0.wdt; p1.uT = p0.uT; p1.E = p0.E; p1.g = p0.g;
  p1.yT = p0.yT; p1.out = (u16*)d_out;
  (void)hipFuncSetAttribute((const void*)k0_kernel, hipFuncAttributeMaxDynamicSharedMemorySize, K0_LDS);
  (void)hipFuncSetAttribute((const void*)k1_kernel, hipFuncAttributeMaxDynamicSharedMemorySize, K1_LDS);
  k0_kernel<<<256, 768, K0_LDS, stream>>>(p0);
  k1_kernel<<<256, 512, K1_LDS, stream>>>(p1);
}
```

```cpp
#include <hip/hip_runtime.h>
#include <hip/hip_bf16.h>
#include <stdint.h>

typedef unsigned short u16;
typedef __attribute__((ext_vector_type(4))) short s16x4;
typedef __attribute__((ext_vector_type(8))) short s16x8;
typedef __attribute__((ext_vector_type(8))) __bf16 bf16x8;
typedef __attribute__((ext_vector_type(2))) __bf16 bf16x2;
typedef __attribute__((ext_vector_type(2))) float f32x2;
typedef __attribute__((ext_vector_type(4))) float f32x4;
typedef __attribute__((ext_vector_type(4))) unsigned u32x4;
typedef __attribute__((ext_vector_type(2))) unsigned u32x2;
typedef __attribute__((address_space(3))) s16x4* lds_s16x4_ptr;
typedef __attribute__((address_space(1))) unsigned gu32;
typedef __attribute__((address_space(1))) unsigned long long gu64;
#define RLX_AGENT __ATOMIC_RELAXED, __HIP_MEMORY_SCOPE_AGENT
#define DEV __device__ __forceinline__

constexpr int L = 2048, D = 512, NM = 128, F = 1792;
constexpr int KS2 = F / 32;

constexpr size_t WS_H = 0;
constexpr size_t WS_WDT = WS_H + (size_t)L * F * 2;
constexpr size_t WS_UT = WS_WDT + (size_t)F * D * 2;
constexpr size_t WS_YT = WS_UT + (size_t)L * D * 2;
constexpr int ES = 2080;
constexpr size_t WS_E = WS_YT + (size_t)L * D * 2 + 64;
constexpr size_t WS_G = WS_E + (size_t)D * ES * 2;

#define TSDECL
#define TS(ph)
#define TSDUMP(K)
#define TS_ARG
#define TS_PASS
DEV float bf2f(u16 v) { return __uint_as_float(((uint32_t)v) << 16); }
DEV float bflo(uint32_t v) { return __uint_as_float(v << 16); }
DEV float bfhi(uint32_t v) { return __uint_as_float(v & 0xffff0000u); }
DEV uint32_t pk2(float a, float b) { f32x2 v = {a, b}; return __builtin_bit_cast(uint32_t, __builtin_convertvector(v, bf16x2)); }
DEV float rbf(float a) { return bflo(pk2(a, 0.f) & 0xffffu); }
DEV f32x4 mfma16(u32x4 a, u32x4 b, f32x4 c) {
  return __builtin_amdgcn_mfma_f32_16x16x32_bf16(__builtin_bit_cast(bf16x8, a), __builtin_bit_cast(bf16x8, b), c, 0, 0, 0);
}
DEV u32x2 tr_read(const char* p) {
  return __builtin_bit_cast(u32x2, __builtin_amdgcn_ds_read_tr16_b64_v4i16((lds_s16x4_ptr)p));
}
DEV u32x4 tr_read2(const char* p0, const char* p1) {
  u32x2 a = tr_read(p0), b = tr_read(p1);
  u32x4 r = {a.x, a.y, b.x, b.y};
  return r;
}
DEV uint32_t lds_addr(const void* p) { return (uint32_t)(uintptr_t)(__attribute__((address_space(3))) const char*)p; }
DEV void glds16(const void* sbase, uint32_t voff, uint32_t lds) {
  unsigned keep;
  asm volatile("s_mov_b32 %0, m0\n\ts_mov_b32 m0, %3\n\ts_nop 0\n\tglobal_load_lds_dwordx4 %1, %2\n\ts_mov_b32 m0, %0"
               : "=&s"(keep) : "v"(voff), "s"(sbase), "s"(lds) : "memory");
}
#define WAIT_V(n) asm volatile("s_waitcnt vmcnt(" #n ")" ::: "memory")
#define WAIT_L(n) asm volatile("s_waitcnt lgkmcnt(" #n ")" ::: "memory")
#define BAR() __builtin_amdgcn_s_barrier()

template <bool WT>
DEV void st16(void* base, size_t byte_off, u32x4 v) {
  if (WT) {
    __builtin_amdgcn_raw_buffer_store_b128(v, __builtin_amdgcn_make_buffer_rsrc(base, 0, 0x7fffffff, 0x00020000), (uint32_t)byte_off, 0, 16);
  } else {
    *(u32x4*)((char*)base + byte_off) = v;
  }
}
#define WT_AUX 1
#define WT_H 1

struct P0 {
  const u16 *u, *Are, *Aim, *Bre, *Bim, *Cre, *Cim, *logdt, *wg, *wu, *wd, *gw;
  u16 *h, *wdt, *uT, *E, *yT;
  float* g;
};

constexpr int STAGE_B = 49152;
constexpr int K0_LDS = 3 * STAGE_B + 15360;

DEV int wswz(int row) { return ((row & 3) << 2) | ((row >> 2) & 3); }

struct SsmPar { u32x4 are, aim, bre, bim, cre, cim; };

DEV void ssm_load(const P0& p, uint32_t off, SsmPar& s) {
  asm volatile("global_load_dwordx4 %0, %1, %2" : "=&v"(s.are) : "v"(off), "s"(p.Are) : "memory");
  asm volatile("global_load_dwordx4 %0, %1, %2" : "=&v"(s.aim) : "v"(off), "s"(p.Aim) : "memory");
  asm volatile("global_load_dwordx4 %0, %1, %2" : "=&v"(s.bre) : "v"(off), "s"(p.Bre) : "memory");
  asm volatile("global_load_dwordx4 %0, %1, %2" : "=&v"(s.bim) : "v"(off), "s"(p.Bim) : "memory");
  asm volatile("global_load_dwordx4 %0, %1, %2" : "=&v"(s.cre) : "v"(off), "s"(p.Cre) : "memory");
  asm volatile("global_load_dwordx4 %0, %1, %2" : "=&v"(s.cim) : "v"(off), "s"(p.Cim) : "memory");
}
#define SSM_WAIT(n, s) asm volatile("s_waitcnt vmcnt(" #n ")" : "+v"(s.are), "+v"(s.aim), "+v"(s.bre), "+v"(s.bim), "+v"(s.cre), "+v"(s.cim)::"memory")

DEV void pair_sync_at(uint32_t mine) {
  const uint32_t other = mine ^ 8u;
  asm volatile("s_waitcnt lgkmcnt(0)\n\tds_write_b32 %0, %1" ::"v"(mine), "v"(1u) : "memory");
  for (unsigned spins = 0; spins < (1u << 24); ++spins) {
    uint32_t x;
    asm volatile("ds_read_b32 %0, %1\n\ts_waitcnt lgkmcnt(0)" : "=v"(x) : "v"(other) : "memory");
    if (__builtin_amdgcn_readfirstlane(x) == 1u) break;
  }
}

struct KgFrag { u32x4 ah[3], al[3], bh, bl; };
template <int H>
DEV void kgen_half(const SsmPar& sp, float dt, float p0, float p1, float p2, float q, KgFrag& f, f32x4 (&acc)[3]) {
  constexpr int W = H & 3, JH = H & 1;
  uint32_t wa = sp.are[W], wi = sp.aim[W], wbr = sp.bre[W], wbi = sp.bim[W], wcr = sp.cre[W], wci = sp.cim[W];
  asm volatile("" : "+v"(wa), "+v"(wi), "+v"(wbr), "+v"(wbi), "+v"(wcr), "+v"(wci));
#pragma unroll
  for (int jj = 0; jj < 2; ++jj) {
    const int j = 2 * JH + jj;
    const float ar = dt * (jj ? bfhi(wa) : bflo(wa)), ai = dt * (jj ? bfhi(wi) : bflo(wi));
    const float br = dt * (jj ? bfhi(wbr) : bflo(wbr)), bi = dt * (jj ? bfhi(wbi) : bflo(wbi));
    const float cr = jj ? bfhi(wcr) : bflo(wcr), ci = jj ? bfhi(wci) : bflo(wci);
    const float a2 = ar * 1.44269504088896340736f, wv = ai * 0.15915494309189533577f;
    const float xr = cr * br - ci * bi, xi = cr * bi + ci * br;
    {
      const float e = __builtin_amdgcn_exp2f(a2 * q), r = __builtin_amdgcn_fractf(wv * q);
      const float s = __builtin_amdgcn_sinf(r) * e, co = __builtin_amdgcn_cosf(r) * e;
      const float vr = xr * co - xi * s, vi = -(xr * s + xi * co);
      const uint32_t hi = pk2(vr, vi);
      f.bh[j] = hi;
      f.bl[j] = pk2(vr - bflo(hi), vi - bfhi(hi));
    }
#pragma unroll
    for (int T = 0; T < 3; ++T) {
      const float pp = T == 0 ? p0 : (T == 1 ? p1 : p2);
      const float e = __builtin_amdgcn_exp2f(a2 * pp), r = __builtin_amdgcn_fractf(wv * pp);
      const float wr = __builtin_amdgcn_cosf(r) * e, wim = __builtin_amdgcn_sinf(r) * e;
      const uint32_t hi = pk2(wr, wim);
      f.ah[T][j] = hi;
      f.al[T][j] = pk2(wr - bflo(hi), wim - bfhi(hi));
    }
  }
  if (JH == 1) {
#pragma unroll
    for (int T = 0; T < 3; ++T) {
      acc[T] = mfma16(f.al[T], f.bh, acc[T]);
      acc[T] = mfma16(f.ah[T], f.bl, acc[T]);
      acc[T] = mfma16(f.ah[T], f.bh, acc[T]);
    }
    asm volatile("" : "+v"(acc[0]), "+v"(acc[1]), "+v"(acc[2]));
  } else {
    asm volatile("" : "+v"(f.ah[0]), "+v"(f.ah[1]), "+v"(f.ah[2]), "+v"(f.al[0]), "+v"(f.al[1]), "+v"(f.al[2]), "+v"(f.bh), "+v"(f.bl));
  }
  __builtin_amdgcn_sched_barrier(0);
}

#define KTS(i)
DEV void kgen_waves(const P0& p, char* scratch, int b, bool aux, int tid, uint32_t ld2 TS_ARG) {
  const int t = tid - 512, kw = __builtin_amdgcn_readfirstlane(t >> 6), lane = t & 63, i = lane & 15, ch0 = 2 * b, cc = kw >> 1;
#define KPRIO 3
#define KPRIO2 KPRIO
#define NPRE 4
  __builtin_amdgcn_s_setprio(KPRIO);
  SsmPar sp0, sp1;
  const uint32_t off = (uint32_t)((ch0 + cc) * NM + 64 * (kw & 1) + 8 * (lane >> 4)) * 2u;
  ssm_load(p, off, sp0);
  ssm_load(p, off + 64u, sp1);
  __builtin_amdgcn_sched_barrier(0);
  BAR();
  __builtin_amdgcn_sched_barrier(0);
  st16<WT_AUX>(p.yT, ((size_t)b * 4096 + t * 8) * 2, (u32x4){~0u, ~0u, ~0u, ~0u});
  st16<WT_AUX>(p.yT, ((size_t)b * 4096 + 2048 + t * 8) * 2, (u32x4){~0u, ~0u, ~0u, ~0u});
  const float dt = rbf(__expf(cc ? bfhi(ld2) : bflo(ld2)));
  const float p0 = (float)((i & 7) + 128 * (i >> 3));
  const float p1 = (i < 8) ? (float)(256 + 2 * (i & 3) + 128 * (i >> 2)) : (float)(512 + 4 * (i & 1) + 128 * ((i - 8) >> 1));
  const float p2 = (i < 8) ? (float)(1024 + 128 * i) : 2048.f;
  const float q = (float)(8 * i);
  f32x4 acc[3] = {{0, 0, 0, 0}, {0, 0, 0, 0}, {0, 0, 0, 0}};
  KgFrag f;
#pragma unroll
  for (int T = 0; T < 3; ++T) f.ah[T] = f.al[T] = (u32x4){0, 0, 0, 0};
  f.bh = f.bl = (u32x4){0, 0, 0, 0};
  SSM_WAIT(8, sp0);
  TS(1);
  if (0 == NPRE || (0 > NPRE && !aux)) BAR();
  kgen_half<0>(sp0, dt, p0, p1, p2, q, f, acc);
  KTS(3);
  if (1 == NPRE || (1 > NPRE && !aux)) BAR();
  kgen_half<1>(sp0, dt, p0, p1, p2, q, f, acc);
  KTS(4);
  if (2 == NPRE || (2 > NPRE && !aux)) BAR();
  kgen_half<2>(sp0, dt, p0, p1, p2, q, f, acc);
  KTS(5);
  if (3 == NPRE || (3 > NPRE && !aux)) BAR();
  kgen_half<3>(sp0, dt, p0, p1, p2, q, f, acc);
  KTS(6);
  if (4 == NPRE || (4 > NPRE && !aux)) BAR();
  SSM_WAIT(2, sp1);
  kgen_half<4>(sp1, dt, p0, p1, p2, q, f, acc);
  KTS(7);
  if (5 == NPRE || (5 > NPRE && !aux)) BAR();
  kgen_half<5>(sp1, dt, p0, p1, p2, q, f, acc);
  KTS(8);
  if (6 == NPRE || (6 > NPRE && !aux)) BAR();
  kgen_half<6>(sp1, dt, p0, p1, p2, q, f, acc);
  KTS(9);
  if (7 == NPRE || (7 > NPRE && !aux)) BAR();
  kgen_half<7>(sp1, dt, p0, p1, p2, q, f, acc);
  float* sc = (float*)scratch;
  u16* kc = (u16*)(scratch + 12288) + cc * 768;
#pragma unroll
  for (int T = 0; T < 3; ++T) *(f32x4*)(sc + ((kw * 3 + T) * 64 + lane) * 4) = acc[T];
  WAIT_L(0);
  TS(15);
  BAR();
  const int tt = t & 127;
#pragma unroll
  for (int T = 0; T < 3; ++T) {
    if (T == 2 && !aux) BAR();
#pragma unroll
    for (int hf = 0; hf < 2; ++hf) {
      const int e = hf * 128 + tt, ln = e >> 2, j = e & 3;
      const int ii = 4 * (ln >> 4) + j, bb = ln & 15;
      const float s = sc[((cc * 2) * 3 + T) * 256 + e] + sc[((cc * 2 + 1) * 3 + T) * 256 + e];
      int idx;
      if (T == 0) idx = (ii & 7) + 128 * (ii >> 3) + 8 * bb;
      else if (T == 1) idx = ii < 8 ? 256 + (ii & 3) + 64 * (ii >> 2) + 4 * bb : 384 + (ii & 1) + 32 * ((ii - 8) >> 1) + 2 * bb;
      else idx = ii < 8 ? 512 + 16 * ii + bb : ((ii == 8 && bb == 0) ? 640 : 700);
      kc[idx] = (u16)(pk2(s, 0.f) & 0xffffu);
    }
    __builtin_amdgcn_sched_barrier(0);
  }
  WAIT_L(0);
  BAR();
#pragma unroll
  for (int hf = 0; hf < 2; ++hf) {
    if (hf == 1 && !aux) BAR();
    int l0 = 1024 * hf + 8 * tt;
    asm volatile("" : "+v"(l0));
    u32x4 o;
#pragma unroll
    for (int e = 0; e < 4; ++e) {
      const int la = l0 + 2 * e, lb = la + 1;
      const uint32_t pb = pk2((float)la, (float)lb);
      const int ia = la < 128 ? la : (int)(pb & 0xffffu) - 17024, ib = lb < 128 ? lb : (int)(pb >> 16) - 17024;
      o[e] = (uint32_t)kc[ia] | ((uint32_t)kc[ib] << 16);
    }
    st16<WT_AUX>(p.E, ((size_t)(ch0 + cc) * ES + 16 + l0) * 2, o);
    __builtin_amdgcn_sched_barrier(0);
  }
  TS(14);
  if (!aux)
    for (int i_ = 0; i_ < NPRE - 4; ++i_) BAR();
}

DEV void k0_glds(const P0& p, uint32_t lb, const uint32_t (&voff)[3], int m0, int f0, int k0, int piece) {
  if (piece < 2) glds16(p.u + (size_t)m0 * D + k0, voff[0] + piece * (8 * D * 2), lb + piece * 1024);
  else if (piece < 4) glds16(p.wg + (size_t)k0 * F + f0, voff[piece - 1], lb + 16384 + (piece - 2) * 1024);
  else glds16(p.wu + (size_t)k0 * F + f0, voff[piece - 3], lb + 32768 + (piece - 4) * 1024);
}

__global__ __launch_bounds__(768, 3) void k0_kernel(P0 p) {
  extern __shared__ __attribute__((aligned(16))) char smem[];
  const int tid = threadIdx.x, lane = tid & 63, w = __builtin_amdgcn_readfirstlane(tid >> 6);
  const int b = blockIdx.x, xcd = b & 7, idx = b >> 3;
  TSDECL;
  TS(0);
  if (w >= 8) {
    const uint32_t ld2 = *(const uint32_t*)(p.logdt + 2 * b);
    kgen_waves(p, smem + 3 * STAGE_B, b, idx >= 28, tid, ld2 TS_PASS);
  } else if (idx < 28) {
    const int mt = (xcd >> 1) * 4 + idx / 7, ft = (xcd & 1) * 7 + idx % 7;
    const int m0 = mt * 128, f0 = ft * 128;
#define K0ROT 2
    const int rot = K0ROT == 0 ? 2 * (idx / 7) + idx % 7 : K0ROT == 1 ? 0 : K0ROT == 2 ? (idx & 1) * 4 : K0ROT == 3 ? 2 * (idx / 7) : idx % 7;
    uint32_t voff[3];
    {
      const int row = 16 * w + (lane >> 3);
      voff[0] = (uint32_t)(row * D + 8 * ((lane & 7) ^ (row & 7))) * 2u;
      const int r0 = 8 * w + (lane >> 4), r1 = r0 + 4;
      voff[1] = (uint32_t)(r0 * F + 8 * ((lane & 15) ^ wswz(r0))) * 2u;
      voff[2] = (uint32_t)(r1 * F + 8 * ((lane & 15) ^ wswz(r1))) * 2u;
    }
    const uint32_t lb = lds_addr(smem) + 2048 * w;
#pragma unroll
    for (int s = 0; s < 3; ++s) {
      if (s == 1) BAR();
#pragma unroll
      for (int pc = 0; pc < 6; ++pc) k0_glds(p, lb + s * STAGE_B, voff, m0, f0, 64 * ((rot + s) & 7), pc);
    }
    const int wm = w >> 2, wn = w & 3, g = lane >> 4, q = (lane & 15) >> 2, pp = lane & 3;
    f32x4 acc[2][2][4];
#pragma unroll
    for (int a = 0; a < 2; ++a)
#pragma unroll
      for (int c = 0; c < 2; ++c)
#pragma unroll
        for (int m = 0; m < 4; ++m) acc[a][c][m] = (f32x4){0, 0, 0, 0};
    u32x4 xf[2][4], wf[2][2][2];
    auto frags = [&](const char* buf, int ss) {
#pragma unroll
      for (int m = 0; m < 4; ++m) {
        const int row = 64 * wm + 16 * m + (lane & 15), c = 4 * ss + g;
        xf[ss][m] = *(const u32x4*)(buf + row * 128 + ((c ^ (row & 7)) << 4));
      }
#pragma unroll
      for (int a = 0; a < 2; ++a)
#pragma unroll
        for (int c = 0; c < 2; ++c) {
          const int ch = 4 * wn + 2 * c + (pp >> 1);
          const int r0 = 32 * ss + 8 * g + q, r1 = r0 + 4;
          const char* wb = buf + 16384 + a * 16384 + 8 * (pp & 1);
          wf[ss][a][c] = tr_read2(wb + 256 * r0 + 16 * (ch ^ wswz(r0)), wb + 256 * r1 + 16 * (ch ^ wswz(r1)));
        }
    };
    auto mm = [&](int ss, bool stage, int sb, int k0, int pc0) {
#pragma unroll
      for (int ac = 0; ac < 4; ++ac) {
#pragma unroll
        for (int m = 0; m < 4; ++m) acc[ac >> 1][ac & 1][m] = mfma16(wf[ss][ac >> 1][ac & 1], xf[ss][m], acc[ac >> 1][ac & 1][m]);
        if (stage && ac < 3) {
          __builtin_amdgcn_sched_barrier(0);
          k0_glds(p, lb + sb * STAGE_B, voff, m0, f0, k0, pc0 + ac);
          __builtin_amdgcn_sched_barrier(0);
        }
      }
    };
#pragma unroll
    for (int s = 0; s < 8; ++s) {
      if (s == 7) WAIT_V(0); else if (s == 0) WAIT_V(12); else WAIT_V(6);
      WAIT_L(0);
      BAR();
      TS(3 + s);
      int bo = (s % 3) * STAGE_B;
      asm volatile("" : "+s"(bo));
      const char* buf = smem + bo;
      const bool st = (s >= 1 && s + 2 < 8);
      const int k0 = 64 * ((rot + s + 2) & 7);
      frags(buf, 0);
      __builtin_amdgcn_sched_barrier(0);
      if (s > 0) mm(1, st, (s + 2) % 3, k0, 0);
      __builtin_amdgcn_sched_barrier(0);
      frags(buf, 1);
      __builtin_amdgcn_sched_barrier(0);
      mm(0, st, (s + 2) % 3, k0, 3);
      __builtin_amdgcn_sched_barrier(0);
    }
    mm(1, false, 0, 0, 0);
    TS(11);
#pragma unroll
    for (int m = 0; m < 4; ++m) {
      u32x4 o;
#pragma unroll
      for (int c = 0; c < 2; ++c)
#pragma unroll
        for (int jj = 0; jj < 2; ++jj) {
          const uint32_t g2 = pk2(acc[0][c][m][2 * jj], acc[0][c][m][2 * jj + 1]), u2 = pk2(acc[1][c][m][2 * jj], acc[1][c][m][2 * jj + 1]);
          const float g0 = bflo(g2), g1 = bfhi(g2);
          const uint32_t s2 = pk2(g0 * __builtin_amdgcn_rcpf(1.f + __expf(-g0)), g1 * __builtin_amdgcn_rcpf(1.f + __expf(-g1)));
          o[2 * c + jj] = pk2(bflo(s2) * bflo(u2), bfhi(s2) * bfhi(u2));
        }
      const int mt64 = (m0 >> 6) + wm, ks = 4 * ft + wn;
      st16<WT_H>(p.h, ((size_t)((mt64 * KS2 + ks) * 4 + m) * 64 + lane) * 16, o);
    }
    TS(12);
  } else {
    BAR();
    const int a = xcd * 4 + (idx - 28);
    constexpr int RS = 1056;
    const int t0 = 64 * a;
    u32x4 uv[8], wv[8];
#pragma unroll
    for (int r = 0; r < 8; ++r) uv[r] = *(const u32x4*)(p.u + (size_t)(t0 + 8 * w + r) * D + lane * 8);
    const int ks2 = a + 32 < KS2 ? a + 32 : a;
#pragma unroll
    for (int r = 0; r < 4; ++r) {
      wv[r] = *(const u32x4*)(p.wd + (size_t)(32 * a + 4 * w + r) * D + lane * 8);
      wv[4 + r] = *(const u32x4*)(p.wd + (size_t)(32 * ks2 + 4 * w + r) * D + lane * 8);
    }
    const u32x4 gwv = *(const u32x4*)(p.gw + lane * 8);
    {
      float s[8];
#pragma unroll
      for (int r = 0; r < 8; ++r) {
        *(u32x4*)(smem + (8 * w + r) * RS + lane * 16) = uv[r];
        s[r] = 0.f;
#pragma unroll
        for (int e = 0; e < 4; ++e) {
          const uint32_t pr = pk2(bflo(uv[r][e]) * bflo(gwv[e]), bfhi(uv[r][e]) * bfhi(gwv[e]));
          s[r] += bflo(pr) + bfhi(pr);
        }
      }
#pragma unroll
      for (int r = 0; r < 4; ++r) { const bool hi = lane & 32; const float keep = hi ? s[r + 4] : s[r], send = hi ? s[r] : s[r + 4]; s[r] = keep + __shfl_xor(send, 32); }
#pragma unroll
      for (int r = 0; r < 2; ++r) { const bool hi = lane & 16; const float keep = hi ? s[r + 2] : s[r], send = hi ? s[r] : s[r + 2]; s[r] = keep + __shfl_xor(send, 16); }
      { const bool hi = lane & 8; const float keep = hi ? s[1] : s[0], send = hi ? s[0] : s[1]; s[0] = keep + __shfl_xor(send, 8); }
      s[0] += __shfl_xor(s[0], 4); s[0] += __shfl_xor(s[0], 2); s[0] += __shfl_xor(s[0], 1);
      if ((lane & 7) == 0) {
        const int r = ((lane >> 5) & 1) * 4 + ((lane >> 4) & 1) * 2 + ((lane >> 3) & 1);
        const float gs = rbf(s[0]);
        p.g[t0 + 8 * w + r] = rbf(1.f / (1.f + __expf(-gs)));
      }
    }
    WAIT_L(0);
    BAR();
    const int g = lane >> 4, q = (lane & 15) >> 2, pp = lane & 3;
#pragma unroll
    for (int c = 0; c < 4; ++c)
#pragma unroll
      for (int hh = 0; hh < 2; ++hh) {
        const int dt_ = 4 * w + c;
        const int r0 = 32 * hh + 8 * g + q;
        const char* base = smem + (16 * dt_ + 4 * pp) * 2;
        const u32x4 v = tr_read2(base + r0 * RS, base + (r0 + 4) * RS);
        st16<WT_AUX>(p.uT, ((size_t)(16 * dt_ + (lane & 15)) * L + t0 + 32 * hh + 8 * g) * 2, v);
      }
    TS(3);
    WAIT_L(0);
    BAR();
#pragma unroll
    for (int r = 0; r < 4; ++r) {
      *(u32x4*)(smem + (4 * w + r) * RS + lane * 16) = wv[r];
      *(u32x4*)(smem + (32 + 4 * w + r) * RS + lane * 16) = wv[4 + r];
    }
    WAIT_L(0);
    BAR();
#pragma unroll
    for (int hh = 0; hh < 2; ++hh) {
      if (hh == 0 || a + 32 < KS2) {
        const int ks = a + 32 * hh;
#pragma unroll
        for (int c = 0; c < 4; ++c) {
          const int nt = 4 * w + c;
          const char* base = smem + 32 * hh * RS + (16 * nt + 4 * pp) * 2;
          const u32x4 v = tr_read2(base + (4 * g + q) * RS, base + (16 + 4 * g + q) * RS);
          st16<WT_AUX>(p.wdt, ((size_t)(((nt >> 2) * KS2 + ks) * 4 + (nt & 3)) * 64 + lane) * 16, v);
        }
      }
    }
    TS(12);
  }
  TSDUMP(0);
}

#define GO_STEP 7
#define FPRIO 1
#define LPRIO 0
struct P1 {
  const u16 *u, *Dskip, *h, *wdt, *uT, *E;
  const float* g;
  u16 *yT, *out;
};

constexpr int CP_STRIDE = 4160;
constexpr int CW_U = 4 * CP_STRIDE, CW_SIZE = CW_U + 4608;
constexpr int K1_XCH = 2 * CW_SIZE;
constexpr int K1_YOFF = K1_XCH + 16384;
constexpr int K1_RED = K1_YOFF + 64 * 144;
constexpr int K1_FLAG = K1_RED + 49152;
constexpr int K1_LDS = K1_FLAG + 64;

DEV void conv_loop(const char* abase, const char* bbase, f32x4 (&y)[8]) {
  u32x4 af[2][8], bf[2][8];
  auto ld = [&](int buf, int it) {
#pragma unroll
    for (int m = 0; m < 8; ++m) bf[buf][m] = *(const u32x4*)(bbase + 512 * m - 64 * it);
#pragma unroll
    for (int m = 0; m < 8; ++m) {
      const char* ap = abase + 512 * m + 64 * it;
      af[buf][m] = tr_read2(ap, ap - 8);
    }
  };
  ld(0, 0);
#pragma unroll
  for (int it = 0; it < 4; ++it) {
    const int cur = it & 1;
    __builtin_amdgcn_sched_barrier(0);
    if (it < 3) ld(cur ^ 1, it + 1);
#pragma unroll
    for (int m = 0; m < 8; ++m)
#pragma unroll
      for (int T = m; T < 8; ++T) y[T] = mfma16(af[cur][m], bf[cur][T - m], y[T]);
    if (it < 3) {
#pragma unroll
      for (int i = 0; i < 12; ++i) {
        __builtin_amdgcn_sched_group_barrier(0x008, 3, 0);
        __builtin_amdgcn_sched_group_barrier(0x100, 2, 0);
      }
    }
  }
  __builtin_amdgcn_sched_barrier(0);
}
DEV void pair_sync(char* smem, int cw, int which) { pair_sync_at(lds_addr(smem + K1_FLAG + cw * 8 + which * 4)); }

__global__ __launch_bounds__(512, 2) void k1_kernel(P1 p) {
  extern __shared__ __attribute__((aligned(16))) char smem[];
  const int tid = threadIdx.x, lane = tid & 63, w = __builtin_amdgcn_readfirstlane(tid >> 6);
  const int b = blockIdx.x, xcd = b & 7, idx = b >> 3;
  const int mt = (xcd >> 1) * 8 + (idx >> 2), nt = (xcd & 1) * 4 + (idx & 3);
  const int t0 = mt * 64, d0 = nt * 64;
  const int g = lane >> 4, n = lane & 15;
  TSDECL;
  TS(0);
  if (w >= 4) __builtin_amdgcn_s_setprio(3);
  if (w == 4) {
    asm volatile("ds_write_b32 %0, %1\n\ts_waitcnt lgkmcnt(0)" ::"v"(lds_addr(smem + K1_FLAG + (lane & 15) * 4)), "v"(0u) : "memory");
  }
  BAR();
  __builtin_amdgcn_sched_barrier(0);
  if (w < 4) {
    __builtin_amdgcn_s_setprio(FPRIO);
    f32x4 acc[4][4];
#pragma unroll
    for (int a = 0; a < 4; ++a)
#pragma unroll
      for (int c = 0; c < 4; ++c) acc[a][c] = (f32x4){0, 0, 0, 0};
    const u16* hp = p.h + ((size_t)mt * KS2 + 14 * w) * 2048 + lane * 8;
    const u16* wp = p.wdt + ((size_t)nt * KS2 + 14 * w) * 2048 + lane * 8;
#define PFD 4
    constexpr int PF = PFD;
    u32x4 hf[PF][4], wf[PF][4];
    const int hrot = w * 512;
#pragma unroll
    for (int s = 0; s < PF; ++s)
#pragma unroll
      for (int a = 0; a < 4; ++a) {
        hf[s][a] = *(const u32x4*)(hp + s * 2048 + ((a * 512 + hrot) & 2047));
        wf[s][a] = *(const u32x4*)(wp + s * 2048 + a * 512);
      }
    const int t = t0 + 16 * w + n;
    u32x2 uv[4], dv[4];
    float gt;
    __builtin_amdgcn_sched_barrier(0);
    TS(15);
    __builtin_amdgcn_sched_barrier(0);
#pragma unroll
    for (int s = 0; s < 14; ++s) {
#pragma unroll
      for (int a = 0; a < 4; ++a)
#pragma unroll
        for (int c = 0; c < 4; ++c) acc[a][c] = mfma16(wf[s % PF][a], hf[s % PF][c], acc[a][c]);
      __builtin_amdgcn_sched_barrier(0);
      if (s + PF < 14) {
#pragma unroll
        for (int a = 0; a < 4; ++a) {
          hf[s % PF][a] = *(const u32x4*)(hp + (s + PF) * 2048 + ((a * 512 + hrot) & 2047));
          wf[s % PF][a] = *(const u32x4*)(wp + (s + PF) * 2048 + a * 512);
        }
      }
      if (s == GO_STEP) asm volatile("ds_write_b32 %0, %1" ::"v"(lds_addr(smem + K1_FLAG + 32 + w * 4)), "v"(1u) : "memory");
      if (s + PF == 14) {
#pragma unroll
        for (int a = 0; a < 4; ++a) {
          uv[a] = *(const u32x2*)(p.u + (size_t)t * D + d0 + 16 * a + 4 * g);
          dv[a] = *(const u32x2*)(p.Dskip + d0 + 16 * a + 4 * g);
        }
        gt = p.g[t];
      }
      __builtin_amdgcn_sched_barrier(0);
    }
    TS(1);
#pragma unroll
    for (int c = 1; c < 4; ++c)
#pragma unroll
      for (int a = 0; a < 4; ++a) *(f32x4*)(smem + K1_RED + ((((w + c) & 3) * 3 + (c - 1)) * 4 + a) * 1024 + lane * 16) = acc[a][c];
    WAIT_L(0);
    BAR();
    TS(7);
    const float omg = rbf(1.f - gt);
    const int q = n >> 2, pp = lane & 3;
#pragma unroll
    for (int a = 0; a < 4; ++a) {
      f32x4 bs = acc[a][0];
#pragma unroll
      for (int s = 0; s < 3; ++s) bs += *(const f32x4*)(smem + K1_RED + ((w * 3 + s) * 4 + a) * 1024 + lane * 16);
      const u32x2 yv = tr_read(smem + K1_YOFF + (16 * a + 4 * g + q) * 144 + (16 * w + 4 * pp) * 2);
      u32x2 o;
#pragma unroll
      for (int e = 0; e < 2; ++e) {
        const uint32_t sk = pk2(bflo(uv[a][e]) * bflo(dv[a][e]), bfhi(uv[a][e]) * bfhi(dv[a][e]));
        const uint32_t av = pk2(bflo(yv[e]) + bflo(sk), bfhi(yv[e]) + bfhi(sk));
        const uint32_t ga = pk2(gt * bflo(av), gt * bfhi(av));
        const uint32_t bb = pk2(bs[2 * e], bs[2 * e + 1]);
        const uint32_t gb = pk2(omg * bflo(bb), omg * bfhi(bb));
        o[e] = pk2(bflo(ga) + bflo(gb), bfhi(ga) + bfhi(gb));
      }
      *(u32x2*)(p.out + (size_t)t * D + d0 + 16 * a + 4 * g) = o;
    }
    TS(9);
  } else {
    const int cw = w - 4, c = cw >> 1, half = cw & 1, d = 2 * b + c;
    char* base = smem + c * CW_SIZE;
    {
      const u16* eg = p.E + (size_t)d * ES;
      u32x4 cur[3];
      u32x2 prv[3];
#pragma unroll
      for (int it = 0; it < 3; ++it) {
        const int un = it < 2 ? (2 * half + it) * 64 + lane : 256 + (lane & 1);
        asm volatile("global_load_dwordx4 %0, %2, %3\n\tglobal_load_dwordx2 %1, %2, %3 offset:-8"
                     : "=&v"(cur[it]), "=&v"(prv[it]) : "v"(un * 16u), "s"(eg) : "memory");
      }
      const uint32_t lb = lds_addr(base + CW_U + 512) + half * 2048;
#pragma unroll
      for (int i = 0; i < 2; ++i) glds16(p.uT + (size_t)d * L + (2 * half + i) * 512, lane * 16u, lb + i * 1024);
      if (lane < 32 && half == 0) *(u32x4*)(base + CW_U + lane * 16) = (u32x4){0, 0, 0, 0};
      asm volatile("s_waitcnt vmcnt(2)" : "+v"(cur[0]), "+v"(cur[1]), "+v"(cur[2]), "+v"(prv[0]), "+v"(prv[1]), "+v"(prv[2])::"memory");
      TS(2);
      if (half == 0) {
        if (lane < 2) cur[0] = (u32x4){0, 0, 0, 0};
        if (lane < 3) prv[0] = (u32x2){0, 0};
      }
#pragma unroll
      for (int it = 0; it < 3; ++it) {
        const int un = it < 2 ? (2 * half + it) * 64 + lane : 256 + (lane & 1);
        const u32x4 cu = cur[it];
        const u32x2 pv = prv[it];
        u32x4 o1, o2, o3;
        o1[0] = __builtin_amdgcn_alignbyte(cu[0], pv[1], 2); o1[1] = __builtin_amdgcn_alignbyte(cu[1], cu[0], 2);
        o1[2] = __builtin_amdgcn_alignbyte(cu[2], cu[1], 2); o1[3] = __builtin_amdgcn_alignbyte(cu[3], cu[2], 2);
        o2[0] = pv[1]; o2[1] = cu[0]; o2[2] = cu[1]; o2[3] = cu[2];
        o3[0] = __builtin_amdgcn_alignbyte(pv[1], pv[0], 2); o3[1] = o1[0]; o3[2] = o1[1]; o3[3] = o1[2];
        if (it < 2 || (lane < 2 && half == 1)) {
          *(u32x4*)(base + 0 * CP_STRIDE + un * 16) = cu;
          *(u32x4*)(base + 1 * CP_STRIDE + un * 16) = o1;
          *(u32x4*)(base + 2 * CP_STRIDE + un * 16) = o2;
          *(u32x4*)(base + 3 * CP_STRIDE + un * 16) = o3;
        }
      }
    }
    WAIT_V(0);
    pair_sync(smem, cw, 0);
    __builtin_amdgcn_s_setprio(LPRIO);
    TS(3);
    f32x4 y[8];
#pragma unroll
    for (int a = 0; a < 8; ++a) y[a] = (f32x4){0, 0, 0, 0};
    {
      const int q = n >> 2, pp = lane & 3;
      const char* abase = base + q * CP_STRIDE + (16 - 8 * g + 4 * pp) * 2 + 32 * (8 * half + 1);
      const char* bbase = base + CW_U + 512 + (16 * n + 8 * g) * 2 - 32 * (8 * half + 1);
      conv_loop(abase, bbase, y);
    }
    TS(4);
    __builtin_amdgcn_s_setprio(3);
#pragma unroll
    for (int a = 0; a < 4; ++a) *(f32x4*)(smem + K1_XCH + (cw * 4 + a) * 1024 + lane * 16) = half ? y[a] : y[4 + a];
    pair_sync(smem, cw, 1);
    TS(8);
#pragma unroll
    for (int a = 0; a < 4; ++a) {
      const f32x4 o = *(const f32x4*)(smem + K1_XCH + ((cw ^ 1) * 4 + a) * 1024 + lane * 16);
      const f32x4 v = half ? o + y[4 + a] : y[a] + o;
      const int ntile = 4 * half + a;
      uint32_t v0 = pk2(v[0], v[1]), v1 = pk2(v[2], v[3]);
      v0 = v0 == ~0u ? 0xfffffffeu : v0;
      v1 = v1 == ~0u ? 0xfffffffeu : v1;
      __hip_atomic_store((gu64*)(p.yT + (size_t)d * L + 256 * ntile + 16 * n + 4 * g), (unsigned long long)v0 | ((unsigned long long)v1 << 32), RLX_AGENT);
    }
    __builtin_amdgcn_s_setprio(0);
    TS(5);
    {
      const int dd = 16 * cw + (lane >> 2), ch = lane & 3;
      const uint32_t off = (uint32_t)((d0 + dd) * L + t0 + ch * 8) * 2u;
      u32x4 v0, v1;
      for (unsigned spins = 0; spins < (1u << 22); ++spins) {
        uint32_t x;
        asm volatile("ds_read_b32 %0, %1\n\ts_waitcnt lgkmcnt(0)" : "=v"(x) : "v"(lds_addr(smem + K1_FLAG + 32 + cw * 4)) : "memory");
        if (__builtin_amdgcn_readfirstlane(x) == 1u) break;
        __builtin_amdgcn_s_sleep(2);
      }
      for (unsigned spins = 0; spins < (1u << 20); ++spins) {
        asm volatile("global_load_dwordx4 %0, %2, %3 sc1\n\tglobal_load_dwordx4 %1, %2, %3 offset:64 sc1\n\ts_waitcnt vmcnt(0)"
                     : "=&v"(v0), "=&v"(v1) : "v"(off), "s"(p.yT) : "memory");
        const bool ok = v0[0] != ~0u && v0[1] != ~0u && v0[2] != ~0u && v0[3] != ~0u && v1[0] != ~0u && v1[1] != ~0u && v1[2] != ~0u && v1[3] != ~0u;
        if (__all(ok)) break;
        __builtin_amdgcn_s_sleep(4);
      }
      *(u32x4*)(smem + K1_YOFF + dd * 144 + ch * 16) = v0;
      *(u32x4*)(smem + K1_YOFF + dd * 144 + ch * 16 + 64) = v1;
    }
    TS(6);
    WAIT_L(0);
    BAR();
  }
  TSDUMP(1);
}

extern "C" void kernel_launch(void* const* d_in, const int* in_sizes, int n_in, void* d_out, int out_size, void* d_ws, size_t ws_size,
                              hipStream_t stream) {
  char* ws = (char*)d_ws;
  P0 p0;
  p0.u = (const u16*)d_in[0]; p0.Are = (const u16*)d_in[1]; p0.Aim = (const u16*)d_in[2]; p0.Bre = (const u16*)d_in[3];
  p0.Bim = (const u16*)d_in[4]; p0.Cre = (const u16*)d_in[5]; p0.Cim = (const u16*)d_in[6]; p0.logdt = (const u16*)d_in[7];
  p0.wg = (const u16*)d_in[10]; p0.wu = (const u16*)d_in[11]; p0.wd = (const u16*)d_in[12]; p0.gw = (const u16*)d_in[13];
  p0.h = (u16*)(ws + WS_H); p0.wdt = (u16*)(ws + WS_WDT); p0.uT = (u16*)(ws + WS_UT); p0.E = (u16*)(ws + WS_E);
  p0.yT = (u16*)(ws + WS_YT); p0.g = (float*)(ws + WS_G);
  P1 p1;
  p1.u = p0.u; p1.Dskip = (const u16*)d_in[8]; p1.h = p0.h; p1.wdt = p0.wdt; p1.uT = p0.uT; p1.E = p0.E; p1.g = p0.g;
  p1.yT = p0.yT; p1.out = (u16*)d_out;
  (void)hipFuncSetAttribute((const void*)k0_kernel, hipFuncAttributeMaxDynamicSharedMemorySize, K0_LDS);
  (void)hipFuncSetAttribute((const void*)k1_kernel, hipFuncAttributeMaxDynamicSharedMemorySize, K1_LDS);
  k0_kernel<<<256, 768, K0_LDS, stream>>>(p0);
  k1_kernel<<<256, 512, K1_LDS, stream>>>(p1);
}
```

```cpp
#include <hip/hip_runtime.h>
#include <hip/hip_bf16.h>
#include <stdint.h>

typedef unsigned short u16;
typedef __attribute__((ext_vector_type(4))) short s16x4;
typedef __attribute__((ext_vector_type(8))) short s16x8;
typedef __attribute__((ext_vector_type(8))) __bf16 bf16x8;
typedef __attribute__((ext_vector_type(2))) __bf16 bf16x2;
typedef __attribute__((ext_vector_type(2))) float f32x2;
typedef __attribute__((ext_vector_type(4))) float f32x4;
typedef __attribute__((ext_vector_type(4))) unsigned u32x4;
typedef __attribute__((ext_vector_type(2))) unsigned u32x2;
typedef __attribute__((address_space(3))) s16x4* lds_s16x4_ptr;
typedef __attribute__((address_space(1))) unsigned gu32;
typedef __attribute__((address_space(1))) unsigned long long gu64;
#define RLX_AGENT __ATOMIC_RELAXED, __HIP_MEMORY_SCOPE_AGENT
#define DEV __device__ __forceinline__

constexpr int L = 2048, D = 512, NM = 128, F = 1792;
constexpr int KS2 = F / 32;

constexpr size_t WS_H = 0;
constexpr size_t WS_WDT = WS_H + (size_t)L * F * 2;
constexpr size_t WS_UT = WS_WDT + (size_t)F * D * 2;
constexpr size_t WS_YT = WS_UT + (size_t)L * D * 2;
constexpr int ES = 2080;
constexpr size_t WS_E = WS_YT + (size_t)L * D * 2 + 64;
constexpr size_t WS_G = WS_E + (size_t)D * ES * 2;

#define TSDECL
#define TS(ph)
#define TSDUMP(K)
#define TS_ARG
#define TS_PASS
DEV float bf2f(u16 v) { return __uint_as_float(((uint32_t)v) << 16); }
DEV float bflo(uint32_t v) { return __uint_as_float(v << 16); }
DEV float bfhi(uint32_t v) { return __uint_as_float(v & 0xffff0000u); }
DEV uint32_t pk2(float a, float b) { f32x2 v = {a, b}; return __builtin_bit_cast(uint32_t, __builtin_convertvector(v, bf16x2)); }
DEV float rbf(float a) { return bflo(pk2(a, 0.f) & 0xffffu); }
DEV f32x4 mfma16(u32x4 a, u32x4 b, f32x4 c) {
  return __builtin_amdgcn_mfma_f32_16x16x32_bf16(__builtin_bit_cast(bf16x8, a), __builtin_bit_cast(bf16x8, b), c, 0, 0, 0);
}
DEV u32x2 tr_read(const char* p) {
  return __builtin_bit_cast(u32x2, __builtin_amdgcn_ds_read_tr16_b64_v4i16((lds_s16x4_ptr)p));
}
DEV u32x4 tr_read2(const char* p0, const char* p1) {
  u32x2 a = tr_read(p0), b = tr_read(p1);
  u32x4 r = {a.x, a.y, b.x, b.y};
  return r;
}
DEV uint32_t lds_addr(const void* p) { return (uint32_t)(uintptr_t)(__attribute__((address_space(3))) const char*)p; }
DEV void glds16(const void* sbase, uint32_t voff, uint32_t lds) {
  unsigned keep;
  asm volatile("s_mov_b32 %0, m0\n\ts_mov_b32 m0, %3\n\ts_nop 0\n\tglobal_load_lds_dwordx4 %1, %2\n\ts_mov_b32 m0, %0"
               : "=&s"(keep) : "v"(voff), "s"(sbase), "s"(lds) : "memory");
}
#define WAIT_V(n) asm volatile("s_waitcnt vmcnt(" #n ")" ::: "memory")
#define WAIT_L(n) asm volatile("s_waitcnt lgkmcnt(" #n ")" ::: "memory")
#define BAR() __builtin_amdgcn_s_barrier()

template <bool WT>
DEV void st16(void* base, size_t byte_off, u32x4 v) {
  if (WT) {
    __builtin_amdgcn_raw_buffer_store_b128(v, __builtin_amdgcn_make_buffer_rsrc(base, 0, 0x7fffffff, 0x00020000), (uint32_t)byte_off, 0, 16);
  } else {
    *(u32x4*)((char*)base + byte_off) = v;
  }
}
#define WT_AUX 1
#define WT_H 1

struct P0 {
  const u16 *u, *Are, *Aim, *Bre, *Bim, *Cre, *Cim, *logdt, *wg, *wu, *wd, *gw;
  u16 *h, *wdt, *uT, *E, *yT;
  float* g;
};

constexpr int STAGE_B = 49152;
constexpr int K0_LDS = 3 * STAGE_B + 15360;

DEV int wswz(int row) { return ((row & 3) << 2) | ((row >> 2) & 3); }

struct SsmPar { u32x4 are, aim, bre, bim, cre, cim; };

DEV void ssm_load(const P0& p, uint32_t off, SsmPar& s) {
  asm volatile("global_load_dwordx4 %0, %1, %2" : "=&v"(s.are) : "v"(off), "s"(p.Are) : "memory");
  asm volatile("global_load_dwordx4 %0, %1, %2" : "=&v"(s.aim) : "v"(off), "s"(p.Aim) : "memory");
  asm volatile("global_load_dwordx4 %0, %1, %2" : "=&v"(s.bre) : "v"(off), "s"(p.Bre) : "memory");
  asm volatile("global_load_dwordx4 %0, %1, %2" : "=&v"(s.bim) : "v"(off), "s"(p.Bim) : "memory");
  asm volatile("global_load_dwordx4 %0, %1, %2" : "=&v"(s.cre) : "v"(off), "s"(p.Cre) : "memory");
  asm volatile("global_load_dwordx4 %0, %1, %2" : "=&v"(s.cim) : "v"(off), "s"(p.Cim) : "memory");
}
#define SSM_WAIT(n, s) asm volatile("s_waitcnt vmcnt(" #n ")" : "+v"(s.are), "+v"(s.aim), "+v"(s.bre), "+v"(s.bim), "+v"(s.cre), "+v"(s.cim)::"memory")

DEV void pair_sync_at(uint32_t mine) {
  const uint32_t other = mine ^ 8u;
  asm volatile("s_waitcnt lgkmcnt(0)\n\tds_write_b32 %0, %1" ::"v"(mine), "v"(1u) : "memory");
  for (unsigned spins = 0; spins < (1u << 24); ++spins) {
    uint32_t x;
    asm volatile("ds_read_b32 %0, %1\n\ts_waitcnt lgkmcnt(0)" : "=v"(x) : "v"(other) : "memory");
    if (__builtin_amdgcn_readfirstlane(x) == 1u) break;
  }
}

struct KgFrag { u32x4 ah[3], al[3], bh, bl; };
template <int H>
DEV void kgen_half(const SsmPar& sp, float dt, float p0, float p1, float p2, float q, KgFrag& f, f32x4 (&acc)[3]) {
  constexpr int W = H & 3, JH = H & 1;
  uint32_t wa = sp.are[W], wi = sp.aim[W], wbr = sp.bre[W], wbi = sp.bim[W], wcr = sp.cre[W], wci = sp.cim[W];
  asm volatile("" : "+v"(wa), "+v"(wi), "+v"(wbr), "+v"(wbi), "+v"(wcr), "+v"(wci));
#pragma unroll
  for (int jj = 0; jj < 2; ++jj) {
    const int j = 2 * JH + jj;
    const float ar = dt * (jj ? bfhi(wa) : bflo(wa)), ai = dt * (jj ? bfhi(wi) : bflo(wi));
    const float br = dt * (jj ? bfhi(wbr) : bflo(wbr)), bi = dt * (jj ? bfhi(wbi) : bflo(wbi));
    const float cr = jj ? bfhi(wcr) : bflo(wcr), ci = jj ? bfhi(wci) : bflo(wci);
    const float a2 = ar * 1.44269504088896340736f, wv = ai * 0.15915494309189533577f;
    const float xr = cr * br - ci * bi, xi = cr * bi + ci * br;
    {
      const float e = __builtin_amdgcn_exp2f(a2 * q), r = __builtin_amdgcn_fractf(wv * q);
      const float s = __builtin_amdgcn_sinf(r) * e, co = __builtin_amdgcn_cosf(r) * e;
      const float vr = xr * co - xi * s, vi = -(xr * s + xi * co);
      const uint32_t hi = pk2(vr, vi);
      f.bh[j] = hi;
      f.bl[j] = pk2(vr - bflo(hi), vi - bfhi(hi));
    }
#pragma unroll
    for (int T = 0; T < 3; ++T) {
      const float pp = T == 0 ? p0 : (T == 1 ? p1 : p2);
      const float e = __builtin_amdgcn_exp2f(a2 * pp), r = __builtin_amdgcn_fractf(wv * pp);
      const float wr = __builtin_amdgcn_cosf(r) * e, wim = __builtin_amdgcn_sinf(r) * e;
      const uint32_t hi = pk2(wr, wim);
      f.ah[T][j] = hi;
      f.al[T][j] = pk2(wr - bflo(hi), wim - bfhi(hi));
    }
  }
  if (JH == 1) {
#pragma unroll
    for (int T = 0; T < 3; ++T) {
      acc[T] = mfma16(f.al[T], f.bh, acc[T]);
      acc[T] = mfma16(f.ah[T], f.bl, acc[T]);
      acc[T] = mfma16(f.ah[T], f.bh, acc[T]);
    }
    asm volatile("" : "+v"(acc[0]), "+v"(acc[1]), "+v"(acc[2]));
  } else {
    asm volatile("" : "+v"(f.ah[0]), "+v"(f.ah[1]), "+v"(f.ah[2]), "+v"(f.al[0]), "+v"(f.al[1]), "+v"(f.al[2]), "+v"(f.bh), "+v"(f.bl));
  }
  __builtin_amdgcn_sched_barrier(0);
}

#define KTS(i)
DEV void kgen_waves(const P0& p, char* scratch, int b, bool aux, int tid, uint32_t ld2 TS_ARG) {
  const int t = tid - 512, kw = __builtin_amdgcn_readfirstlane(t >> 6), lane = t & 63, i = lane & 15, ch0 = 2 * b, cc = kw >> 1;
#define KPRIO 3
#define KPRIO2 0
#define NPRE 5
  __builtin_amdgcn_s_setprio(KPRIO);
  SsmPar sp0, sp1;
  const uint32_t off = (uint32_t)((ch0 + cc) * NM + 64 * (kw & 1) + 8 * (lane >> 4)) * 2u;
  ssm_load(p, off, sp0);
  ssm_load(p, off + 64u, sp1);
  __builtin_amdgcn_sched_barrier(0);
  BAR();
  __builtin_amdgcn_sched_barrier(0);
  st16<WT_AUX>(p.yT, ((size_t)b * 4096 + t * 8) * 2, (u32x4){~0u, ~0u, ~0u, ~0u});
  st16<WT_AUX>(p.yT, ((size_t)b * 4096 + 2048 + t * 8) * 2, (u32x4){~0u, ~0u, ~0u, ~0u});
  const float dt = rbf(__expf(cc ? bfhi(ld2) : bflo(ld2)));
  const float p0 = (float)((i & 7) + 128 * (i >> 3));
  const float p1 = (i < 8) ? (float)(256 + 2 * (i & 3) + 128 * (i >> 2)) : (float)(512 + 4 * (i & 1) + 128 * ((i - 8) >> 1));
  const float p2 = (i < 8) ? (float)(1024 + 128 * i) : 2048.f;
  const float q = (float)(8 * i);
  f32x4 acc[3] = {{0, 0, 0, 0}, {0, 0, 0, 0}, {0, 0, 0, 0}};
  KgFrag f;
#pragma unroll
  for (int T = 0; T < 3; ++T) f.ah[T] = f.al[T] = (u32x4){0, 0, 0, 0};
  f.bh = f.bl = (u32x4){0, 0, 0, 0};
  SSM_WAIT(8, sp0);
  TS(1);
  if (0 == NPRE || (0 > NPRE && !aux)) BAR();
  kgen_half<0>(sp0, dt, p0, p1, p2, q, f, acc);
  KTS(3);
  if (1 == NPRE || (1 > NPRE && !aux)) BAR();
  kgen_half<1>(sp0, dt, p0, p1, p2, q, f, acc);
  KTS(4);
  if (2 == NPRE || (2 > NPRE && !aux)) BAR();
  kgen_half<2>(sp0, dt, p0, p1, p2, q, f, acc);
  KTS(5);
  if (3 == NPRE || (3 > NPRE && !aux)) BAR();
  kgen_half<3>(sp0, dt, p0, p1, p2, q, f, acc);
  KTS(6);
  if (!aux) __builtin_amdgcn_s_setprio(KPRIO2);
  if (4 == NPRE || (4 > NPRE && !aux)) BAR();
  SSM_WAIT(2, sp1);
  kgen_half<4>(sp1, dt, p0, p1, p2, q, f, acc);
  KTS(7);
  if (5 == NPRE || (5 > NPRE && !aux)) BAR();
  kgen_half<5>(sp1, dt, p0, p1, p2, q, f, acc);
  KTS(8);
  if (6 == NPRE || (6 > NPRE && !aux)) BAR();
  kgen_half<6>(sp1, dt, p0, p1, p2, q, f, acc);
  KTS(9);
  if (7 == NPRE || (7 > NPRE && !aux)) BAR();
  kgen_half<7>(sp1, dt, p0, p1, p2, q, f, acc);
  float* sc = (float*)scratch;
  u16* kc = (u16*)(scratch + 12288) + cc * 768;
#pragma unroll
  for (int T = 0; T < 3; ++T) *(f32x4*)(sc + ((kw * 3 + T) * 64 + lane) * 4) = acc[T];
  WAIT_L(0);
  TS(15);
  BAR();
  const int tt = t & 127;
#pragma unroll
  for (int T = 0; T < 3; ++T) {
    if (T == 2 && !aux) BAR();
#pragma unroll
    for (int hf = 0; hf < 2; ++hf) {
      const int e = hf * 128 + tt, ln = e >> 2, j = e & 3;
      const int ii = 4 * (ln >> 4) + j, bb = ln & 15;
      const float s = sc[((cc * 2) * 3 + T) * 256 + e] + sc[((cc * 2 + 1) * 3 + T) * 256 + e];
      int idx;
      if (T == 0) idx = (ii & 7) + 128 * (ii >> 3) + 8 * bb;
      else if (T == 1) idx = ii < 8 ? 256 + (ii & 3) + 64 * (ii >> 2) + 4 * bb : 384 + (ii & 1) + 32 * ((ii - 8) >> 1) + 2 * bb;
      else idx = ii < 8 ? 512 + 16 * ii + bb : ((ii == 8 && bb == 0) ? 640 : 700);
      kc[idx] = (u16)(pk2(s, 0.f) & 0xffffu);
    }
    __builtin_amdgcn_sched_barrier(0);
  }
  WAIT_L(0);
  BAR();
#pragma unroll
  for (int hf = 0; hf < 2; ++hf) {
    if (hf == 1 && !aux) BAR();
    int l0 = 1024 * hf + 8 * tt;
    asm volatile("" : "+v"(l0));
    u32x4 o;
#pragma unroll
    for (int e = 0; e < 4; ++e) {
      const int la = l0 + 2 * e, lb = la + 1;
      const uint32_t pb = pk2((float)la, (float)lb);
      const int ia = la < 128 ? la : (int)(pb & 0xffffu) - 17024, ib = lb < 128 ? lb : (int)(pb >> 16) - 17024;
      o[e] = (uint32_t)kc[ia] | ((uint32_t)kc[ib] << 16);
    }
    st16<WT_AUX>(p.E, ((size_t)(ch0 + cc) * ES + 16 + l0) * 2, o);
    __builtin_amdgcn_sched_barrier(0);
  }
  TS(14);
  if (!aux)
    for (int i_ = 0; i_ < NPRE - 4; ++i_) BAR();
}

DEV void k0_glds(const P0& p, uint32_t lb, const uint32_t (&voff)[3], int m0, int f0, int k0, int piece) {
  if (piece < 2) glds16(p.u + (size_t)m0 * D + k0, voff[0] + piece * (8 * D * 2), lb + piece * 1024);
  else if (piece < 4) glds16(p.wg + (size_t)k0 * F + f0, voff[piece - 1], lb + 16384 + (piece - 2) * 1024);
  else glds16(p.wu + (size_t)k0 * F + f0, voff[piece - 3], lb + 32768 + (piece - 4) * 1024);
}

__global__ __launch_bounds__(768, 3) void k0_kernel(P0 p) {
  extern __shared__ __attribute__((aligned(16))) char smem[];
  const int tid = threadIdx.x, lane = tid & 63, w = __builtin_amdgcn_readfirstlane(tid >> 6);
  const int b = blockIdx.x, xcd = b & 7, idx = b >> 3;
  TSDECL;
  TS(0);
  if (w >= 8) {
    const uint32_t ld2 = *(const uint32_t*)(p.logdt + 2 * b);
    kgen_waves(p, smem + 3 * STAGE_B, b, idx >= 28, tid, ld2 TS_PASS);
  } else if (idx < 28) {
    const int mt = (xcd >> 1) * 4 + idx / 7, ft = (xcd & 1) * 7 + idx % 7;
    const int m0 = mt * 128, f0 = ft * 128;
#define K0ROT 2
    const int rot = K0ROT == 0 ? 2 * (idx / 7) + idx % 7 : K0ROT == 1 ? 0 : K0ROT == 2 ? (idx & 1) * 4 : K0ROT == 3 ? 2 * (idx / 7) : idx % 7;
    uint32_t voff[3];
    {
      const int row = 16 * w + (lane >> 3);
      voff[0] = (uint32_t)(row * D + 8 * ((lane & 7) ^ (row & 7))) * 2u;
      const int r0 = 8 * w + (lane >> 4), r1 = r0 + 4;
      voff[1] = (uint32_t)(r0 * F + 8 * ((lane & 15) ^ wswz(r0))) * 2u;
      voff[2] = (uint32_t)(r1 * F + 8 * ((lane & 15) ^ wswz(r1))) * 2u;
    }
    const uint32_t lb = lds_addr(smem) + 2048 * w;
#pragma unroll
    for (int s = 0; s < 3; ++s) {
      if (s == 1) BAR();
#pragma unroll
      for (int pc = 0; pc < 6; ++pc) k0_glds(p, lb + s * STAGE_B, voff, m0, f0, 64 * ((rot + s) & 7), pc);
    }
    const int wm = w >> 2, wn = w & 3, g = lane >> 4, q = (lane & 15) >> 2, pp = lane & 3;
    f32x4 acc[2][2][4];
#pragma unroll
    for (int a = 0; a < 2; ++a)
#pragma unroll
      for (int c = 0; c < 2; ++c)
#pragma unroll
        for (int m = 0; m < 4; ++m) acc[a][c][m] = (f32x4){0, 0, 0, 0};
    u32x4 xf[2][4], wf[2][2][2];
    auto frags = [&](const char* buf, int ss) {
#pragma unroll
      for (int m = 0; m < 4; ++m) {
        const int row = 64 * wm + 16 * m + (lane & 15), c = 4 * ss + g;
        xf[ss][m] = *(const u32x4*)(buf + row * 128 + ((c ^ (row & 7)) << 4));
      }
#pragma unroll
      for (int a = 0; a < 2; ++a)
#pragma unroll
        for (int c = 0; c < 2; ++c) {
          const int ch = 4 * wn + 2 * c + (pp >> 1);
          const int r0 = 32 * ss + 8 * g + q, r1 = r0 + 4;
          const char* wb = buf + 16384 + a * 16384 + 8 * (pp & 1);
          wf[ss][a][c] = tr_read2(wb + 256 * r0 + 16 * (ch ^ wswz(r0)), wb + 256 * r1 + 16 * (ch ^ wswz(r1)));
        }
    };
    auto mm = [&](int ss, bool stage, int sb, int k0, int pc0) {
#pragma unroll
      for (int ac = 0; ac < 4; ++ac) {
#pragma unroll
        for (int m = 0; m < 4; ++m) acc[ac >> 1][ac & 1][m] = mfma16(wf[ss][ac >> 1][ac & 1], xf[ss][m], acc[ac >> 1][ac & 1][m]);
        if (stage && ac < 3) {
          __builtin_amdgcn_sched_barrier(0);
          k0_glds(p, lb + sb * STAGE_B, voff, m0, f0, k0, pc0 + ac);
          __builtin_amdgcn_sched_barrier(0);
        }
      }
    };
#pragma unroll
    for (int s = 0; s < 8; ++s) {
      if (s == 7) WAIT_V(0); else if (s == 0) WAIT_V(12); else WAIT_V(6);
      WAIT_L(0);
      BAR();
      TS(3 + s);
      int bo = (s % 3) * STAGE_B;
      asm volatile("" : "+s"(bo));
      const char* buf = smem + bo;
      const bool st = (s >= 1 && s + 2 < 8);
      const int k0 = 64 * ((rot + s + 2) & 7);
      frags(buf, 0);
      __builtin_amdgcn_sched_barrier(0);
      if (s > 0) mm(1, st, (s + 2) % 3, k0, 0);
      __builtin_amdgcn_sched_barrier(0);
      frags(buf, 1);
      __builtin_amdgcn_sched_barrier(0);
      mm(0, st, (s + 2) % 3, k0, 3);
      __builtin_amdgcn_sched_barrier(0);
    }
    mm(1, false, 0, 0, 0);
    TS(11);
#pragma unroll
    for (int m = 0; m < 4; ++m) {
      u32x4 o;
#pragma unroll
      for (int c = 0; c < 2; ++c)
#pragma unroll
        for (int jj = 0; jj < 2; ++jj) {
          const uint32_t g2 = pk2(acc[0][c][m][2 * jj], acc[0][c][m][2 * jj + 1]), u2 = pk2(acc[1][c][m][2 * jj], acc[1][c][m][2 * jj + 1]);
          const f32x2 gg = {bflo(g2), bfhi(g2)};
          const f32x2 tt = gg * -1.44269504088896340736f;
          const f32x2 dn = (f32x2){__builtin_amdgcn_exp2f(tt[0]), __builtin_amdgcn_exp2f(tt[1])} + 1.f;
          const f32x2 sv = gg * (f32x2){__builtin_amdgcn_rcpf(dn[0]), __builtin_amdgcn_rcpf(dn[1])};
          const uint32_t s2 = pk2(sv[0], sv[1]);
          const f32x2 hv = (f32x2){bflo(s2), bfhi(s2)} * (f32x2){bflo(u2), bfhi(u2)};
          o[2 * c + jj] = pk2(hv[0], hv[1]);
        }
      const int mt64 = (m0 >> 6) + wm, ks = 4 * ft + wn;
      st16<WT_H>(p.h, ((size_t)((mt64 * KS2 + ks) * 4 + m) * 64 + lane) * 16, o);
    }
    TS(12);
  } else {
    BAR();
    const int a = xcd * 4 + (idx - 28);
    constexpr int RS = 1056;
    const int t0 = 64 * a;
    u32x4 uv[8], wv[8];
#pragma unroll
    for (int r = 0; r < 8; ++r) uv[r] = *(const u32x4*)(p.u + (size_t)(t0 + 8 * w + r) * D + lane * 8);
    const int ks2 = a + 32 < KS2 ? a + 32 : a;
#pragma unroll
    for (int r = 0; r < 4; ++r) {
      wv[r] = *(const u32x4*)(p.wd + (size_t)(32 * a + 4 * w + r) * D + lane * 8);
      wv[4 + r] = *(const u32x4*)(p.wd + (size_t)(32 * ks2 + 4 * w + r) * D + lane * 8);
    }
    const u32x4 gwv = *(const u32x4*)(p.gw + lane * 8);
    {
      float s[8];
#pragma unroll
      for (int r = 0; r < 8; ++r) {
        *(u32x4*)(smem + (8 * w + r) * RS + lane * 16) = uv[r];
        s[r] = 0.f;
#pragma unroll
        for (int e = 0; e < 4; ++e) {
          const uint32_t pr = pk2(bflo(uv[r][e]) * bflo(gwv[e]), bfhi(uv[r][e]) * bfhi(gwv[e]));
          s[r] += bflo(pr) + bfhi(pr);
        }
      }
#pragma unroll
      for (int r = 0; r < 4; ++r) { const bool hi = lane & 32; const float keep = hi ? s[r + 4] : s[r], send = hi ? s[r] : s[r + 4]; s[r] = keep + __shfl_xor(send, 32); }
#pragma unroll
      for (int r = 0; r < 2; ++r) { const bool hi = lane & 16; const float keep = hi ? s[r + 2] : s[r], send = hi ? s[r] : s[r + 2]; s[r] = keep + __shfl_xor(send, 16); }
      { const bool hi = lane & 8; const float keep = hi ? s[1] : s[0], send = hi ? s[0] : s[1]; s[0] = keep + __shfl_xor(send, 8); }
      s[0] += __shfl_xor(s[0], 4); s[0] += __shfl_xor(s[0], 2); s[0] += __shfl_xor(s[0], 1);
      if ((lane & 7) == 0) {
        const int r = ((lane >> 5) & 1) * 4 + ((lane >> 4) & 1) * 2 + ((lane >> 3) & 1);
        const float gs = rbf(s[0]);
        p.g[t0 + 8 * w + r] = rbf(1.f / (1.f + __expf(-gs)));
      }
    }
    WAIT_L(0);
    BAR();
    const int g = lane >> 4, q = (lane & 15) >> 2, pp = lane & 3;
#pragma unroll
    for (int c = 0; c < 4; ++c)
#pragma unroll
      for (int hh = 0; hh < 2; ++hh) {
        const int dt_ = 4 * w + c;
        const int r0 = 32 * hh + 8 * g + q;
        const char* base = smem + (16 * dt_ + 4 * pp) * 2;
        const u32x4 v = tr_read2(base + r0 * RS, base + (r0 + 4) * RS);
        st16<WT_AUX>(p.uT, ((size_t)(16 * dt_ + (lane & 15)) * L + t0 + 32 * hh + 8 * g) * 2, v);
      }
    TS(3);
    WAIT_L(0);
    BAR();
#pragma unroll
    for (int r = 0; r < 4; ++r) {
      *(u32x4*)(smem + (4 * w + r) * RS + lane * 16) = wv[r];
      *(u32x4*)(smem + (32 + 4 * w + r) * RS + lane * 16) = wv[4 + r];
    }
    WAIT_L(0);
    BAR();
#pragma unroll
    for (int hh = 0; hh < 2; ++hh) {
      if (hh == 0 || a + 32 < KS2) {
        const int ks = a + 32 * hh;
#pragma unroll
        for (int c = 0; c < 4; ++c) {
          const int nt = 4 * w + c;
          const char* base = smem + 32 * hh * RS + (16 * nt + 4 * pp) * 2;
          const u32x4 v = tr_read2(base + (4 * g + q) * RS, base + (16 + 4 * g + q) * RS);
          st16<WT_AUX>(p.wdt, ((size_t)(((nt >> 2) * KS2 + ks) * 4 + (nt & 3)) * 64 + lane) * 16, v);
        }
      }
    }
    TS(12);
  }
  TSDUMP(0);
}

#define GO_STEP 7
#define FPRIO 1
#define LPRIO 0
struct P1 {
  const u16 *u, *Dskip, *h, *wdt, *uT, *E;
  const float* g;
  u16 *yT, *out;
};

constexpr int CP_STRIDE = 4160;
constexpr int CW_U = 4 * CP_STRIDE, CW_SIZE = CW_U + 4608;
constexpr int K1_XCH = 2 * CW_SIZE;
constexpr int K1_YOFF = K1_XCH + 16384;
constexpr int K1_RED = K1_YOFF + 64 * 144;
constexpr int K1_FLAG = K1_RED + 49152;
constexpr int K1_LDS = K1_FLAG + 64;

DEV void conv_loop(const char* abase, const char* bbase, f32x4 (&y)[8]) {
  u32x4 af[2][8], bf[2][8];
  auto ld = [&](int buf, int it) {
#pragma unroll
    for (int m = 0; m < 8; ++m) bf[buf][m] = *(const u32x4*)(bbase + 512 * m - 64 * it);
#pragma unroll
    for (int m = 0; m < 8; ++m) {
      const char* ap = abase + 512 * m + 64 * it;
      af[buf][m] = tr_read2(ap, ap - 8);
    }
  };
  ld(0, 0);
#pragma unroll
  for (int it = 0; it < 4; ++it) {
    const int cur = it & 1;
    __builtin_amdgcn_sched_barrier(0);
    if (it < 3) ld(cur ^ 1, it + 1);
#pragma unroll
    for (int m = 0; m < 8; ++m)
#pragma unroll
      for (int T = m; T < 8; ++T) y[T] = mfma16(af[cur][m], bf[cur][T - m], y[T]);
    if (it < 3) {
#pragma unroll
      for (int i = 0; i < 12; ++i) {
        __builtin_amdgcn_sched_group_barrier(0x008, 3, 0);
        __builtin_amdgcn_sched_group_barrier(0x100, 2, 0);
      }
    }
  }
  __builtin_amdgcn_sched_barrier(0);
}
DEV void pair_sync(char* smem, int cw, int which) { pair_sync_at(lds_addr(smem + K1_FLAG + cw * 8 + which * 4)); }

__global__ __launch_bounds__(512, 2) void k1_kernel(P1 p) {
  extern __shared__ __attribute__((aligned(16))) char smem[];
  const int tid = threadIdx.x, lane = tid & 63, w = __builtin_amdgcn_readfirstlane(tid >> 6);
  const int b = blockIdx.x, xcd = b & 7, idx = b >> 3;
  const int mt = (xcd >> 1) * 8 + (idx >> 2), nt = (xcd & 1) * 4 + (idx & 3);
  const int t0 = mt * 64, d0 = nt * 64;
  const int g = lane >> 4, n = lane & 15;
  TSDECL;
  TS(0);
  if (w >= 4) __builtin_amdgcn_s_setprio(3);
  if (w == 4) {
    asm volatile("ds_write_b32 %0, %1\n\ts_waitcnt lgkmcnt(0)" ::"v"(lds_addr(smem + K1_FLAG + (lane & 15) * 4)), "v"(0u) : "memory");
  }
  BAR();
  __builtin_amdgcn_sched_barrier(0);
  if (w < 4) {
    __builtin_amdgcn_s_setprio(FPRIO);
    f32x4 acc[4][4];
#pragma unroll
    for (int a = 0; a < 4; ++a)
#pragma unroll
      for (int c = 0; c < 4; ++c) acc[a][c] = (f32x4){0, 0, 0, 0};
    const u16* hp = p.h + ((size_t)mt * KS2 + 14 * w) * 2048 + lane * 8;
    const u16* wp = p.wdt + ((size_t)nt * KS2 + 14 * w) * 2048 + lane * 8;
#define PFD 4
    constexpr int PF = PFD;
    u32x4 hf[PF][4], wf[PF][4];
    const int hrot = w * 512;
#pragma unroll
    for (int s = 0; s < PF; ++s)
#pragma unroll
      for (int a = 0; a < 4; ++a) {
        hf[s][a] = *(const u32x4*)(hp + s * 2048 + ((a * 512 + hrot) & 2047));
        wf[s][a] = *(const u32x4*)(wp + s * 2048 + a * 512);
      }
    const int t = t0 + 16 * w + n;
    float gt;
    __builtin_amdgcn_sched_barrier(0);
    TS(15);
    __builtin_amdgcn_sched_barrier(0);
#pragma unroll
    for (int s = 0; s < 14; ++s) {
#pragma unroll
      for (int a = 0; a < 4; ++a)
#pragma unroll
        for (int c = 0; c < 4; ++c) acc[a][c] = mfma16(wf[s % PF][a], hf[s % PF][c], acc[a][c]);
      __builtin_amdgcn_sched_barrier(0);
      if (s + PF < 14) {
#pragma unroll
        for (int a = 0; a < 4; ++a) {
          hf[s % PF][a] = *(const u32x4*)(hp + (s + PF) * 2048 + ((a * 512 + hrot) & 2047));
          wf[s % PF][a] = *(const u32x4*)(wp + (s + PF) * 2048 + a * 512);
        }
      }
      if (s == GO_STEP) asm volatile("ds_write_b32 %0, %1" ::"v"(lds_addr(smem + K1_FLAG + 32 + w * 4)), "v"(1u) : "memory");
      if (s + PF == 14) gt = p.g[t];
      __builtin_amdgcn_sched_barrier(0);
    }
    TS(1);
#pragma unroll
    for (int c = 1; c < 4; ++c)
#pragma unroll
      for (int a = 0; a < 4; ++a) *(f32x4*)(smem + K1_RED + ((((w + c) & 3) * 3 + (c - 1)) * 4 + a) * 1024 + lane * 16) = acc[a][c];
    WAIT_L(0);
    BAR();
    TS(7);
    const float omg = rbf(1.f - gt);
    const int q = n >> 2, pp = lane & 3;
#pragma unroll
    for (int a = 0; a < 4; ++a) {
      f32x4 bs = acc[a][0];
#pragma unroll
      for (int s = 0; s < 3; ++s) bs += *(const f32x4*)(smem + K1_RED + ((w * 3 + s) * 4 + a) * 1024 + lane * 16);
      const u32x2 yv = tr_read(smem + K1_YOFF + (16 * a + 4 * g + q) * 144 + (16 * w + 4 * pp) * 2);
      u32x2 o;
#pragma unroll
      for (int e = 0; e < 2; ++e) {
        const uint32_t ga = pk2(gt * bflo(yv[e]), gt * bfhi(yv[e]));
        const uint32_t bb = pk2(bs[2 * e], bs[2 * e + 1]);
        const uint32_t gb = pk2(omg * bflo(bb), omg * bfhi(bb));
        o[e] = pk2(bflo(ga) + bflo(gb), bfhi(ga) + bfhi(gb));
      }
      *(u32x2*)(p.out + (size_t)t * D + d0 + 16 * a + 4 * g) = o;
    }
    TS(9);
  } else {
    const int cw = w - 4, c = cw >> 1, half = cw & 1, d = 2 * b + c;
    char* base = smem + c * CW_SIZE;
    const uint32_t dsk2 = *(const uint32_t*)(p.Dskip + 2 * b);
    const float dsk = c ? bfhi(dsk2) : bflo(dsk2);
    {
      const u16* eg = p.E + (size_t)d * ES;
      u32x4 cur[3];
      u32x2 prv[3];
#pragma unroll
      for (int it = 0; it < 3; ++it) {
        const int un = it < 2 ? (2 * half + it) * 64 + lane : 256 + (lane & 1);
        asm volatile("global_load_dwordx4 %0, %2, %3\n\tglobal_load_dwordx2 %1, %2, %3 offset:-8"
                     : "=&v"(cur[it]), "=&v"(prv[it]) : "v"(un * 16u), "s"(eg) : "memory");
      }
      const uint32_t lb = lds_addr(base + CW_U + 512) + half * 2048;
#pragma unroll
      for (int i = 0; i < 2; ++i) glds16(p.uT + (size_t)d * L + (2 * half + i) * 512, lane * 16u, lb + i * 1024);
      if (lane < 32 && half == 0) *(u32x4*)(base + CW_U + lane * 16) = (u32x4){0, 0, 0, 0};
      asm volatile("s_waitcnt vmcnt(2)" : "+v"(cur[0]), "+v"(cur[1]), "+v"(cur[2]), "+v"(prv[0]), "+v"(prv[1]), "+v"(prv[2])::"memory");
      TS(2);
      if (half == 0) {
        if (lane < 2) cur[0] = (u32x4){0, 0, 0, 0};
        if (lane < 3) prv[0] = (u32x2){0, 0};
      }
#pragma unroll
      for (int it = 0; it < 3; ++it) {
        const int un = it < 2 ? (2 * half + it) * 64 + lane : 256 + (lane & 1);
        const u32x4 cu = cur[it];
        const u32x2 pv = prv[it];
        u32x4 o1, o2, o3;
        o1[0] = __builtin_amdgcn_alignbyte(cu[0], pv[1], 2); o1[1] = __builtin_amdgcn_alignbyte(cu[1], cu[0], 2);
        o1[2] = __builtin_amdgcn_alignbyte(cu[2], cu[1], 2); o1[3] = __builtin_amdgcn_alignbyte(cu[3], cu[2], 2);
        o2[0] = pv[1]; o2[1] = cu[0]; o2[2] = cu[1]; o2[3] = cu[2];
        o3[0] = __builtin_amdgcn_alignbyte(pv[1], pv[0], 2); o3[1] = o1[0]; o3[2] = o1[1]; o3[3] = o1[2];
        if (it < 2 || (lane < 2 && half == 1)) {
          *(u32x4*)(base + 0 * CP_STRIDE + un * 16) = cu;
          *(u32x4*)(base + 1 * CP_STRIDE + un * 16) = o1;
          *(u32x4*)(base + 2 * CP_STRIDE + un * 16) = o2;
          *(u32x4*)(base + 3 * CP_STRIDE + un * 16) = o3;
        }
      }
    }
    WAIT_V(0);
    pair_sync(smem, cw, 0);
    __builtin_amdgcn_s_setprio(LPRIO);
    TS(3);
    f32x4 y[8];
#pragma unroll
    for (int a = 0; a < 8; ++a) y[a] = (f32x4){0, 0, 0, 0};
    {
      const int q = n >> 2, pp = lane & 3;
      const char* abase = base + q * CP_STRIDE + (16 - 8 * g + 4 * pp) * 2 + 32 * (8 * half + 1);
      const char* bbase = base + CW_U + 512 + (16 * n + 8 * g) * 2 - 32 * (8 * half + 1);
      conv_loop(abase, bbase, y);
    }
    TS(4);
    __builtin_amdgcn_s_setprio(3);
#pragma unroll
    for (int a = 0; a < 4; ++a) *(f32x4*)(smem + K1_XCH + (cw * 4 + a) * 1024 + lane * 16) = half ? y[a] : y[4 + a];
    pair_sync(smem, cw, 1);
    TS(8);
#pragma unroll
    for (int a = 0; a < 4; ++a) {
      const f32x4 o = *(const f32x4*)(smem + K1_XCH + ((cw ^ 1) * 4 + a) * 1024 + lane * 16);
      const f32x4 v = half ? o + y[4 + a] : y[a] + o;
      const int ntile = 4 * half + a;
      uint32_t v0 = pk2(v[0], v[1]), v1 = pk2(v[2], v[3]);
      {
        const u32x2 uu = *(const u32x2*)(base + CW_U + 512 + (256 * ntile + 16 * n + 4 * g) * 2);
        const uint32_t s0 = pk2(bflo(uu[0]) * dsk, bfhi(uu[0]) * dsk), s1 = pk2(bflo(uu[1]) * dsk, bfhi(uu[1]) * dsk);
        v0 = pk2(bflo(v0) + bflo(s0), bfhi(v0) + bfhi(s0));
        v1 = pk2(bflo(v1) + bflo(s1), bfhi(v1) + bfhi(s1));
      }
      v0 = v0 == ~0u ? 0xfffffffeu : v0;
      v1 = v1 == ~0u ? 0xfffffffeu : v1;
      __hip_atomic_store((gu64*)(p.yT + (size_t)d * L + 256 * ntile + 16 * n + 4 * g), (unsigned long long)v0 | ((unsigned long long)v1 << 32), RLX_AGENT);
    }
    __builtin_amdgcn_s_setprio(0);
    TS(5);
    {
      const int dd = 16 * cw + (lane >> 2), ch = lane & 3;
      const uint32_t off = (uint32_t)((d0 + dd) * L + t0 + ch * 8) * 2u;
      u32x4 v0, v1;
      for (unsigned spins = 0; spins < (1u << 22); ++spins) {
        uint32_t x;
        asm volatile("ds_read_b32 %0, %1\n\ts_waitcnt lgkmcnt(0)" : "=v"(x) : "v"(lds_addr(smem + K1_FLAG + 32 + cw * 4)) : "memory");
        if (__builtin_amdgcn_readfirstlane(x) == 1u) break;
        __builtin_amdgcn_s_sleep(2);
      }
      for (unsigned spins = 0; spins < (1u << 20); ++spins) {
        asm volatile("global_load_dwordx4 %0, %2, %3 sc1\n\tglobal_load_dwordx4 %1, %2, %3 offset:64 sc1\n\ts_waitcnt vmcnt(0)"
                     : "=&v"(v0), "=&v"(v1) : "v"(off), "s"(p.yT) : "memory");
        const bool ok = v0[0] != ~0u && v0[1] != ~0u && v0[2] != ~0u && v0[3] != ~0u && v1[0] != ~0u && v1[1] != ~0u && v1[2] != ~0u && v1[3] != ~0u;
        if (__all(ok)) break;
        __builtin_amdgcn_s_sleep(4);
      }
      *(u32x4*)(smem + K1_YOFF + dd * 144 + ch * 16) = v0;
      *(u32x4*)(smem + K1_YOFF + dd * 144 + ch * 16 + 64) = v1;
    }
    TS(6);
    WAIT_L(0);
    BAR();
  }
  TSDUMP(1);
}

extern "C" void kernel_launch(void* const* d_in, const int* in_sizes, int n_in, void* d_out, int out_size, void* d_ws, size_t ws_size,
                              hipStream_t stream) {
  char* ws = (char*)d_ws;
  P0 p0;
  p0.u = (const u16*)d_in[0]; p0.Are = (const u16*)d_in[1]; p0.Aim = (const u16*)d_in[2]; p0.Bre = (const u16*)d_in[3];
  p0.Bim = (const u16*)d_in[4]; p0.Cre = (const u16*)d_in[5]; p0.Cim = (const u16*)d_in[6]; p0.logdt = (const u16*)d_in[7];
  p0.wg = (const u16*)d_in[10]; p0.wu = (const u16*)d_in[11]; p0.wd = (const u16*)d_in[12]; p0.gw = (const u16*)d_in[13];
  p0.h = (u16*)(ws + WS_H); p0.wdt = (u16*)(ws + WS_WDT); p0.uT = (u16*)(ws + WS_UT); p0.E = (u16*)(ws + WS_E);
  p0.yT = (u16*)(ws + WS_YT); p0.g = (float*)(ws + WS_G);
  P1 p1;
  p1.u = p0.u; p1.Dskip = (const u16*)d_in[8]; p1.h = p0.h; p1.wdt = p0.wdt; p1.uT = p0.uT; p1.E = p0.E; p1.g = p0.g;
  p1.yT = p0.yT; p1.out = (u16*)d_out;
  (void)hipFuncSetAttribute((const void*)k0_kernel, hipFuncAttributeMaxDynamicSharedMemorySize, K0_LDS);
  (void)hipFuncSetAttribute((const void*)k1_kernel, hipFuncAttributeMaxDynamicSharedMemorySize, K1_LDS);
  k0_kernel<<<256, 768, K0_LDS, stream>>>(p0);
  k1_kernel<<<256, 512, K1_LDS, stream>>>(p1);
}
```

```cpp
#include <hip/hip_runtime.h>
#include <hip/hip_bf16.h>
#include <stdint.h>

typedef unsigned short u16;
typedef __attribute__((ext_vector_type(4))) short s16x4;
typedef __attribute__((ext_vector_type(8))) short s16x8;
typedef __attribute__((ext_vector_type(8))) __bf16 bf16x8;
typedef __attribute__((ext_vector_type(2))) __bf16 bf16x2;
typedef __attribute__((ext_vector_type(2))) float f32x2;
typedef __attribute__((ext_vector_type(4))) float f32x4;
typedef __attribute__((ext_vector_type(4))) unsigned u32x4;
typedef __attribute__((ext_vector_type(2))) unsigned u32x2;
typedef __attribute__((address_space(3))) s16x4* lds_s16x4_ptr;
typedef __attribute__((address_space(1))) unsigned gu32;
typedef __attribute__((address_space(1))) unsigned long long gu64;
#define RLX_AGENT __ATOMIC_RELAXED, __HIP_MEMORY_SCOPE_AGENT
#define DEV __device__ __forceinline__

constexpr int L = 2048, D = 512, NM = 128, F = 1792;
constexpr int KS2 = F / 32;

constexpr size_t WS_H = 0;
constexpr size_t WS_WDT = WS_H + (size_t)L * F * 2;
constexpr size_t WS_UT = WS_WDT + (size_t)F * D * 2;
constexpr size_t WS_YT = WS_UT + (size_t)L * D * 2;
constexpr int ES = 2080;
constexpr size_t WS_E = WS_YT + (size_t)L * D * 2 + 64;
constexpr size_t WS_G = WS_E + (size_t)D * ES * 2;

#define TSDECL
#define TS(ph)
#define TSDUMP(K)
#define TS_ARG
#define TS_PASS
DEV float bf2f(u16 v) { return __uint_as_float(((uint32_t)v) << 16); }
DEV float bflo(uint32_t v) { return __uint_as_float(v << 16); }
DEV float bfhi(uint32_t v) { return __uint_as_float(v & 0xffff0000u); }
DEV uint32_t pk2(float a, float b) { f32x2 v = {a, b}; return __builtin_bit_cast(uint32_t, __builtin_convertvector(v, bf16x2)); }
DEV float rbf(float a) { return bflo(pk2(a, 0.f) & 0xffffu); }
DEV f32x4 mfma16(u32x4 a, u32x4 b, f32x4 c) {
  return __builtin_amdgcn_mfma_f32_16x16x32_bf16(__builtin_bit_cast(bf16x8, a), __builtin_bit_cast(bf16x8, b), c, 0, 0, 0);
}
DEV u32x2 tr_read(const char* p) {
  return __builtin_bit_cast(u32x2, __builtin_amdgcn_ds_read_tr16_b64_v4i16((lds_s16x4_ptr)p));
}
DEV u32x4 tr_read2(const char* p0, const char* p1) {
  u32x2 a = tr_read(p0), b = tr_read(p1);
  u32x4 r = {a.x, a.y, b.x, b.y};
  return r;
}
DEV uint32_t lds_addr(const void* p) { return (uint32_t)(uintptr_t)(__attribute__((address_space(3))) const char*)p; }
DEV void glds16(const void* sbase, uint32_t voff, uint32_t lds) {
  unsigned keep;
  asm volatile("s_mov_b32 %0, m0\n\ts_mov_b32 m0, %3\n\ts_nop 0\n\tglobal_load_lds_dwordx4 %1, %2\n\ts_mov_b32 m0, %0"
               : "=&s"(keep) : "v"(voff), "s"(sbase), "s"(lds) : "memory");
}
#define WAIT_V(n) asm volatile("s_waitcnt vmcnt(" #n ")" ::: "memory")
#define WAIT_L(n) asm volatile("s_waitcnt lgkmcnt(" #n ")" ::: "memory")
#define BAR() __builtin_amdgcn_s_barrier()

template <bool WT>
DEV void st16(void* base, size_t byte_off, u32x4 v) {
  if (WT) {
    __builtin_amdgcn_raw_buffer_store_b128(v, __builtin_amdgcn_make_buffer_rsrc(base, 0, 0x7fffffff, 0x00020000), (uint32_t)byte_off, 0, 16);
  } else {
    *(u32x4*)((char*)base + byte_off) = v;
  }
}
#define WT_AUX 1
#define WT_H 1

struct P0 {
  const u16 *u, *Are, *Aim, *Bre, *Bim, *Cre, *Cim, *logdt, *wg, *wu, *wd, *gw;
  u16 *h, *wdt, *uT, *E, *yT;
  float* g;
};

constexpr int STAGE_B = 49152;
constexpr int K0_LDS = 3 * STAGE_B + 15360;

DEV int wswz(int row) { return ((row & 3) << 2) | ((row >> 2) & 3); }

struct SsmPar { u32x4 are, aim, bre, bim, cre, cim; };

DEV void ssm_load(const P0& p, uint32_t off, SsmPar& s) {
  asm volatile("global_load_dwordx4 %0, %1, %2" : "=&v"(s.are) : "v"(off), "s"(p.Are) : "memory");
  asm volatile("global_load_dwordx4 %0, %1, %2" : "=&v"(s.aim) : "v"(off), "s"(p.Aim) : "memory");
  asm volatile("global_load_dwordx4 %0, %1, %2" : "=&v"(s.bre) : "v"(off), "s"(p.Bre) : "memory");
  asm volatile("global_load_dwordx4 %0, %1, %2" : "=&v"(s.bim) : "v"(off), "s"(p.Bim) : "memory");
  asm volatile("global_load_dwordx4 %0, %1, %2" : "=&v"(s.cre) : "v"(off), "s"(p.Cre) : "memory");
  asm volatile("global_load_dwordx4 %0, %1, %2" : "=&v"(s.cim) : "v"(off), "s"(p.Cim) : "memory");
}
#define SSM_WAIT(n, s) asm volatile("s_waitcnt vmcnt(" #n ")" : "+v"(s.are), "+v"(s.aim), "+v"(s.bre), "+v"(s.bim), "+v"(s.cre), "+v"(s.cim)::"memory")

DEV void pair_sync_at(uint32_t mine) {
  const uint32_t other = mine ^ 8u;
  asm volatile("s_waitcnt lgkmcnt(0)\n\tds_write_b32 %0, %1" ::"v"(mine), "v"(1u) : "memory");
  for (unsigned spins = 0; spins < (1u << 24); ++spins) {
    uint32_t x;
    asm volatile("ds_read_b32 %0, %1\n\ts_waitcnt lgkmcnt(0)" : "=v"(x) : "v"(other) : "memory");
    if (__builtin_amdgcn_readfirstlane(x) == 1u) break;
  }
}

struct KgFrag { u32x4 ah[3], al[3], bh, bl; };
template <int H>
DEV void kgen_half(const SsmPar& sp, float dt, float p0, float p1, float p2, float q, KgFrag& f, f32x4 (&acc)[3]) {
  constexpr int W = H & 3, JH = H & 1;
  uint32_t wa = sp.are[W], wi = sp.aim[W], wbr = sp.bre[W], wbi = sp.bim[W], wcr = sp.cre[W], wci = sp.cim[W];
  asm volatile("" : "+v"(wa), "+v"(wi), "+v"(wbr), "+v"(wbi), "+v"(wcr), "+v"(wci));
#pragma unroll
  for (int jj = 0; jj < 2; ++jj) {
    const int j = 2 * JH + jj;
    const float ar = dt * (jj ? bfhi(wa) : bflo(wa)), ai = dt * (jj ? bfhi(wi) : bflo(wi));
    const float br = dt * (jj ? bfhi(wbr) : bflo(wbr)), bi = dt * (jj ? bfhi(wbi) : bflo(wbi));
    const float cr = jj ? bfhi(wcr) : bflo(wcr), ci = jj ? bfhi(wci) : bflo(wci);
    const float a2 = ar * 1.44269504088896340736f, wv = ai * 0.15915494309189533577f;
    const float xr = cr * br - ci * bi, xi = cr * bi + ci * br;
    {
      const float e = __builtin_amdgcn_exp2f(a2 * q), r = __builtin_amdgcn_fractf(wv * q);
      const float s = __builtin_amdgcn_sinf(r) * e, co = __builtin_amdgcn_cosf(r) * e;
      const float vr = xr * co - xi * s, vi = -(xr * s + xi * co);
      const uint32_t hi = pk2(vr, vi);
      f.bh[j] = hi;
      f.bl[j] = pk2(vr - bflo(hi), vi - bfhi(hi));
    }
#pragma unroll
    for (int T = 0; T < 3; ++T) {
      const float pp = T == 0 ? p0 : (T == 1 ? p1 : p2);
      const float e = __builtin_amdgcn_exp2f(a2 * pp), r = __builtin_amdgcn_fractf(wv * pp);
      const float wr = __builtin_amdgcn_cosf(r) * e, wim = __builtin_amdgcn_sinf(r) * e;
      const uint32_t hi = pk2(wr, wim);
      f.ah[T][j] = hi;
      f.al[T][j] = pk2(wr - bflo(hi), wim - bfhi(hi));
    }
  }
  if (JH == 1) {
#pragma unroll
    for (int T = 0; T < 3; ++T) {
      acc[T] = mfma16(f.al[T], f.bh, acc[T]);
      acc[T] = mfma16(f.ah[T], f.bl, acc[T]);
      acc[T] = mfma16(f.ah[T], f.bh, acc[T]);
    }
    asm volatile("" : "+v"(acc[0]), "+v"(acc[1]), "+v"(acc[2]));
  } else {
    asm volatile("" : "+v"(f.ah[0]), "+v"(f.ah[1]), "+v"(f.ah[2]), "+v"(f.al[0]), "+v"(f.al[1]), "+v"(f.al[2]), "+v"(f.bh), "+v"(f.bl));
  }
  __builtin_amdgcn_sched_barrier(0);
}

#define KTS(i)
DEV void kgen_waves(const P0& p, char* scratch, int b, bool aux, int tid, uint32_t ld2 TS_ARG) {
  const int t = tid - 512, kw = __builtin_amdgcn_readfirstlane(t >> 6), lane = t & 63, i = lane & 15, ch0 = 2 * b, cc = kw >> 1;
#define KPRIO 3
#define KPRIO2 KPRIO
#define NPRE 4
  __builtin_amdgcn_s_setprio(KPRIO);
  SsmPar sp0, sp1;
  const uint32_t off = (uint32_t)((ch0 + cc) * NM + 64 * (kw & 1) + 8 * (lane >> 4)) * 2u;
  ssm_load(p, off, sp0);
  ssm_load(p, off + 64u, sp1);
  __builtin_amdgcn_sched_barrier(0);
  BAR();
  __builtin_amdgcn_sched_barrier(0);
  const float dt = rbf(__expf(cc ? bfhi(ld2) : bflo(ld2)));
  const float p0 = (float)((i & 7) + 128 * (i >> 3));
  const float p1 = (i < 8) ? (float)(256 + 2 * (i & 3) + 128 * (i >> 2)) : (float)(512 + 4 * (i & 1) + 128 * ((i - 8) >> 1));
  const float p2 = (i < 8) ? (float)(1024 + 128 * i) : 2048.f;
  const float q = (float)(8 * i);
  f32x4 acc[3] = {{0, 0, 0, 0}, {0, 0, 0, 0}, {0, 0, 0, 0}};
  KgFrag f;
#pragma unroll
  for (int T = 0; T < 3; ++T) f.ah[T] = f.al[T] = (u32x4){0, 0, 0, 0};
  f.bh = f.bl = (u32x4){0, 0, 0, 0};
  SSM_WAIT(6, sp0);
  TS(1);
  if (0 == NPRE || (0 > NPRE && !aux)) BAR();
  kgen_half<0>(sp0, dt, p0, p1, p2, q, f, acc);
  KTS(3);
  if (1 == NPRE || (1 > NPRE && !aux)) BAR();
  kgen_half<1>(sp0, dt, p0, p1, p2, q, f, acc);
  KTS(4);
  if (2 == NPRE || (2 > NPRE && !aux)) BAR();
  kgen_half<2>(sp0, dt, p0, p1, p2, q, f, acc);
  KTS(5);
  if (3 == NPRE || (3 > NPRE && !aux)) BAR();
  kgen_half<3>(sp0, dt, p0, p1, p2, q, f, acc);
  KTS(6);
  if (4 == NPRE || (4 > NPRE && !aux)) BAR();
  SSM_WAIT(0, sp1);
  kgen_half<4>(sp1, dt, p0, p1, p2, q, f, acc);
  KTS(7);
  if (5 == NPRE || (5 > NPRE && !aux)) BAR();
  kgen_half<5>(sp1, dt, p0, p1, p2, q, f, acc);
  KTS(8);
  if (6 == NPRE || (6 > NPRE && !aux)) BAR();
  kgen_half<6>(sp1, dt, p0, p1, p2, q, f, acc);
  KTS(9);
  if (7 == NPRE || (7 > NPRE && !aux)) BAR();
  kgen_half<7>(sp1, dt, p0, p1, p2, q, f, acc);
  float* sc = (float*)scratch;
  u16* kc = (u16*)(scratch + 12288) + cc * 768;
#pragma unroll
  for (int T = 0; T < 3; ++T) *(f32x4*)(sc + ((kw * 3 + T) * 64 + lane) * 4) = acc[T];
  WAIT_L(0);
  TS(15);
  BAR();
  const int tt = t & 127;
#pragma unroll
  for (int T = 0; T < 3; ++T) {
    if (T == 2 && !aux) BAR();
#pragma unroll
    for (int hf = 0; hf < 2; ++hf) {
      const int e = hf * 128 + tt, ln = e >> 2, j = e & 3;
      const int ii = 4 * (ln >> 4) + j, bb = ln & 15;
      const float s = sc[((cc * 2) * 3 + T) * 256 + e] + sc[((cc * 2 + 1) * 3 + T) * 256 + e];
      int idx;
      if (T == 0) idx = (ii & 7) + 128 * (ii >> 3) + 8 * bb;
      else if (T == 1) idx = ii < 8 ? 256 + (ii & 3) + 64 * (ii >> 2) + 4 * bb : 384 + (ii & 1) + 32 * ((ii - 8) >> 1) + 2 * bb;
      else idx = ii < 8 ? 512 + 16 * ii + bb : ((ii == 8 && bb == 0) ? 640 : 700);
      kc[idx] = (u16)(pk2(s, 0.f) & 0xffffu);
    }
    __builtin_amdgcn_sched_barrier(0);
  }
  WAIT_L(0);
  BAR();
#pragma unroll
  for (int hf = 0; hf < 2; ++hf) {
    if (hf == 1 && !aux) BAR();
    int l0 = 1024 * hf + 8 * tt;
    asm volatile("" : "+v"(l0));
    u32x4 o;
#pragma unroll
    for (int e = 0; e < 4; ++e) {
      const int la = l0 + 2 * e, lb = la + 1;
      const uint32_t pb = pk2((float)la, (float)lb);
      const int ia = la < 128 ? la : (int)(pb & 0xffffu) - 17024, ib = lb < 128 ? lb : (int)(pb >> 16) - 17024;
      o[e] = (uint32_t)kc[ia] | ((uint32_t)kc[ib] << 16);
    }
    st16<WT_AUX>(p.E, ((size_t)(ch0 + cc) * ES + 16 + l0) * 2, o);
    __builtin_amdgcn_sched_barrier(0);
  }
  st16<WT_AUX>(p.yT, ((size_t)b * 4096 + t * 8) * 2, (u32x4){~0u, ~0u, ~0u, ~0u});
  st16<WT_AUX>(p.yT, ((size_t)b * 4096 + 2048 + t * 8) * 2, (u32x4){~0u, ~0u, ~0u, ~0u});
  TS(14);
  if (!aux)
    for (int i_ = 0; i_ < NPRE - 4; ++i_) BAR();
}

DEV void k0_glds(const P0& p, uint32_t lb, const uint32_t (&voff)[3], int m0, int f0, int k0, int piece) {
  if (piece < 2) glds16(p.u + (size_t)m0 * D + k0, voff[0] + piece * (8 * D * 2), lb + piece * 1024);
  else if (piece < 4) glds16(p.wg + (size_t)k0 * F + f0, voff[piece - 1], lb + 16384 + (piece - 2) * 1024);
  else glds16(p.wu + (size_t)k0 * F + f0, voff[piece - 3], lb + 32768 + (piece - 4) * 1024);
}

__global__ __launch_bounds__(768, 3) void k0_kernel(P0 p) {
  extern __shared__ __attribute__((aligned(16))) char smem[];
  const int tid = threadIdx.x, lane = tid & 63, w = __builtin_amdgcn_readfirstlane(tid >> 6);
  const int b = blockIdx.x, xcd = b & 7, idx = b >> 3;
  TSDECL;
  TS(0);
  if (w >= 8) {
    const uint32_t ld2 = *(const uint32_t*)(p.logdt + 2 * b);
    kgen_waves(p, smem + 3 * STAGE_B, b, idx >= 28, tid, ld2 TS_PASS);
  } else if (idx < 28) {
    const int mt = (xcd >> 1) * 4 + idx / 7, ft = (xcd & 1) * 7 + idx % 7;
    const int m0 = mt * 128, f0 = ft * 128;
#define K0ROT 2
    const int rot = K0ROT == 0 ? 2 * (idx / 7) + idx % 7 : K0ROT == 1 ? 0 : K0ROT == 2 ? (idx & 1) * 4 : K0ROT == 3 ? 2 * (idx / 7) : idx % 7;
    uint32_t voff[3];
    {
      const int row = 16 * w + (lane >> 3);
      voff[0] = (uint32_t)(row * D + 8 * ((lane & 7) ^ (row & 7))) * 2u;
      const int r0 = 8 * w + (lane >> 4), r1 = r0 + 4;
      voff[1] = (uint32_t)(r0 * F + 8 * ((lane & 15) ^ wswz(r0))) * 2u;
      voff[2] = (uint32_t)(r1 * F + 8 * ((lane & 15) ^ wswz(r1))) * 2u;
    }
    const uint32_t lb = lds_addr(smem) + 2048 * w;
#pragma unroll
    for (int s = 0; s < 3; ++s) {
      if (s == 1) BAR();
#pragma unroll
      for (int pc = 0; pc < 6; ++pc) k0_glds(p, lb + s * STAGE_B, voff, m0, f0, 64 * ((rot + s) & 7), pc);
    }
    const int wm = w >> 2, wn = w & 3, g = lane >> 4, q = (lane & 15) >> 2, pp = lane & 3;
    f32x4 acc[2][2][4];
#pragma unroll
    for (int a = 0; a < 2; ++a)
#pragma unroll
      for (int c = 0; c < 2; ++c)
#pragma unroll
        for (int m = 0; m < 4; ++m) acc[a][c][m] = (f32x4){0, 0, 0, 0};
    u32x4 xf[2][4], wf[2][2][2];
    auto frags = [&](const char* buf, int ss) {
#pragma unroll
      for (int m = 0; m < 4; ++m) {
        const int row = 64 * wm + 16 * m + (lane & 15), c = 4 * ss + g;
        xf[ss][m] = *(const u32x4*)(buf + row * 128 + ((c ^ (row & 7)) << 4));
      }
#pragma unroll
      for (int a = 0; a < 2; ++a)
#pragma unroll
        for (int c = 0; c < 2; ++c) {
          const int ch = 4 * wn + 2 * c + (pp >> 1);
          const int r0 = 32 * ss + 8 * g + q, r1 = r0 + 4;
          const char* wb = buf + 16384 + a * 16384 + 8 * (pp & 1);
          wf[ss][a][c] = tr_read2(wb + 256 * r0 + 16 * (ch ^ wswz(r0)), wb + 256 * r1 + 16 * (ch ^ wswz(r1)));
        }
    };
    auto mm = [&](int ss, bool stage, int sb, int k0, int pc0) {
#pragma unroll
      for (int ac = 0; ac < 4; ++ac) {
#pragma unroll
        for (int m = 0; m < 4; ++m) acc[ac >> 1][ac & 1][m] = mfma16(wf[ss][ac >> 1][ac & 1], xf[ss][m], acc[ac >> 1][ac & 1][m]);
        if (stage && ac < 3) {
          __builtin_amdgcn_sched_barrier(0);
          k0_glds(p, lb + sb * STAGE_B, voff, m0, f0, k0, pc0 + ac);
          __builtin_amdgcn_sched_barrier(0);
        }
      }
    };
#pragma unroll
    for (int s = 0; s < 8; ++s) {
      if (s == 7) WAIT_V(0); else if (s == 0) WAIT_V(12); else WAIT_V(6);
      WAIT_L(0);
      BAR();
      TS(3 + s);
      int bo = (s % 3) * STAGE_B;
      asm volatile("" : "+s"(bo));
      const char* buf = smem + bo;
      const bool st = (s >= 1 && s + 2 < 8);
      const int k0 = 64 * ((rot + s + 2) & 7);
      frags(buf, 0);
      __builtin_amdgcn_sched_barrier(0);
      if (s > 0) mm(1, st, (s + 2) % 3, k0, 0);
      __builtin_amdgcn_sched_barrier(0);
      frags(buf, 1);
      __builtin_amdgcn_sched_barrier(0);
      mm(0, st, (s + 2) % 3, k0, 3);
      __builtin_amdgcn_sched_barrier(0);
    }
    mm(1, false, 0, 0, 0);
    TS(11);
#pragma unroll
    for (int m = 0; m < 4; ++m) {
      u32x4 o;
#pragma unroll
      for (int c = 0; c < 2; ++c)
#pragma unroll
        for (int jj = 0; jj < 2; ++jj) {
          const uint32_t g2 = pk2(acc[0][c][m][2 * jj], acc[0][c][m][2 * jj + 1]), u2 = pk2(acc[1][c][m][2 * jj], acc[1][c][m][2 * jj + 1]);
          const f32x2 gg = {bflo(g2), bfhi(g2)};
          const f32x2 tt = gg * -1.44269504088896340736f;
          const f32x2 dn = (f32x2){__builtin_amdgcn_exp2f(tt[0]), __builtin_amdgcn_exp2f(tt[1])} + 1.f;
          const f32x2 sv = gg * (f32x2){__builtin_amdgcn_rcpf(dn[0]), __builtin_amdgcn_rcpf(dn[1])};
          const uint32_t s2 = pk2(sv[0], sv[1]);
          const f32x2 hv = (f32x2){bflo(s2), bfhi(s2)} * (f32x2){bflo(u2), bfhi(u2)};
          o[2 * c + jj] = pk2(hv[0], hv[1]);
        }
      const int mt64 = (m0 >> 6) + wm, ks = 4 * ft + wn;
      st16<WT_H>(p.h, ((size_t)((mt64 * KS2 + ks) * 4 + m) * 64 + lane) * 16, o);
    }
    TS(12);
  } else {
    BAR();
    const int a = xcd * 4 + (idx - 28);
    constexpr int RS = 1056;
    const int t0 = 64 * a;
    u32x4 uv[8], wv[8];
#pragma unroll
    for (int r = 0; r < 8; ++r) uv[r] = *(const u32x4*)(p.u + (size_t)(t0 + 8 * w + r) * D + lane * 8);
    const int ks2 = a + 32 < KS2 ? a + 32 : a;
#pragma unroll
    for (int r = 0; r < 4; ++r) {
      wv[r] = *(const u32x4*)(p.wd + (size_t)(32 * a + 4 * w + r) * D + lane * 8);
      wv[4 + r] = *(const u32x4*)(p.wd + (size_t)(32 * ks2 + 4 * w + r) * D + lane * 8);
    }
    const u32x4 gwv = *(const u32x4*)(p.gw + lane * 8);
    {
      float s[8];
#pragma unroll
      for (int r = 0; r < 8; ++r) {
        *(u32x4*)(smem + (8 * w + r) * RS + lane * 16) = uv[r];
        s[r] = 0.f;
#pragma unroll
        for (int e = 0; e < 4; ++e) {
          const uint32_t pr = pk2(bflo(uv[r][e]) * bflo(gwv[e]), bfhi(uv[r][e]) * bfhi(gwv[e]));
          s[r] += bflo(pr) + bfhi(pr);
        }
      }
#pragma unroll
      for (int r = 0; r < 4; ++r) { const bool hi = lane & 32; const float keep = hi ? s[r + 4] : s[r], send = hi ? s[r] : s[r + 4]; s[r] = keep + __shfl_xor(send, 32); }
#pragma unroll
      for (int r = 0; r < 2; ++r) { const bool hi = lane & 16; const float keep = hi ? s[r + 2] : s[r], send = hi ? s[r] : s[r + 2]; s[r] = keep + __shfl_xor(send, 16); }
      { const bool hi = lane & 8; const float keep = hi ? s[1] : s[0], send = hi ? s[0] : s[1]; s[0] = keep + __shfl_xor(send, 8); }
      s[0] += __shfl_xor(s[0], 4); s[0] += __shfl_xor(s[0], 2); s[0] += __shfl_xor(s[0], 1);
      if ((lane & 7) == 0) {
        const int r = ((lane >> 5) & 1) * 4 + ((lane >> 4) & 1) * 2 + ((lane >> 3) & 1);
        const float gs = rbf(s[0]);
        p.g[t0 + 8 * w + r] = rbf(1.f / (1.f + __expf(-gs)));
      }
    }
    WAIT_L(0);
    BAR();
    const int g = lane >> 4, q = (lane & 15) >> 2, pp = lane & 3;
#pragma unroll
    for (int c = 0; c < 4; ++c)
#pragma unroll
      for (int hh = 0; hh < 2; ++hh) {
        const int dt_ = 4 * w + c;
        const int r0 = 32 * hh + 8 * g + q;
        const char* base = smem + (16 * dt_ + 4 * pp) * 2;
        const u32x4 v = tr_read2(base + r0 * RS, base + (r0 + 4) * RS);
        st16<WT_AUX>(p.uT, ((size_t)(16 * dt_ + (lane & 15)) * L + t0 + 32 * hh + 8 * g) * 2, v);
      }
    TS(3);
    WAIT_L(0);
    BAR();
#pragma unroll
    for (int r = 0; r < 4; ++r) {
      *(u32x4*)(smem + (4 * w + r) * RS + lane * 16) = wv[r];
      *(u32x4*)(smem + (32 + 4 * w + r) * RS + lane * 16) = wv[4 + r];
    }
    WAIT_L(0);
    BAR();
#pragma unroll
    for (int hh = 0; hh < 2; ++hh) {
      if (hh == 0 || a + 32 < KS2) {
        const int ks = a + 32 * hh;
#pragma unroll
        for (int c = 0; c < 4; ++c) {
          const int nt = 4 * w + c;
          const char* base = smem + 32 * hh * RS + (16 * nt + 4 * pp) * 2;
          const u32x4 v = tr_read2(base + (4 * g + q) * RS, base + (16 + 4 * g + q) * RS);
          st16<WT_AUX>(p.wdt, ((size_t)(((nt >> 2) * KS2 + ks) * 4 + (nt & 3)) * 64 + lane) * 16, v);
        }
      }
    }
    TS(12);
  }
  TSDUMP(0);
}

#define GO_STEP 7
#define FPRIO 1
#define LPRIO 0
struct P1 {
  const u16 *u, *Dskip, *h, *wdt, *uT, *E;
  const float* g;
  u16 *yT, *out;
};

constexpr int CP_STRIDE = 4160;
constexpr int CW_U = 4 * CP_STRIDE, CW_SIZE = CW_U + 4608;
constexpr int K1_XCH = 2 * CW_SIZE;
constexpr int K1_YOFF = K1_XCH + 16384;
constexpr int K1_RED = K1_YOFF + 64 * 144;
constexpr int K1_FLAG = K1_RED + 49152;
constexpr int K1_LDS = K1_FLAG + 64;

DEV void conv_loop(const char* abase, const char* bbase, f32x4 (&y)[8]) {
  u32x4 af[2][8], bf[2][8];
  auto ld = [&](int buf, int it) {
#pragma unroll
    for (int m = 0; m < 8; ++m) bf[buf][m] = *(const u32x4*)(bbase + 512 * m - 64 * it);
#pragma unroll
    for (int m = 0; m < 8; ++m) {
      const char* ap = abase + 512 * m + 64 * it;
      af[buf][m] = tr_read2(ap, ap - 8);
    }
  };
  ld(0, 0);
#pragma unroll
  for (int it = 0; it < 4; ++it) {
    const int cur = it & 1;
    __builtin_amdgcn_sched_barrier(0);
    if (it < 3) ld(cur ^ 1, it + 1);
#pragma unroll
    for (int m = 0; m < 8; ++m)
#pragma unroll
      for (int T = m; T < 8; ++T) y[T] = mfma16(af[cur][m], bf[cur][T - m], y[T]);
    if (it < 3) {
#pragma unroll
      for (int i = 0; i < 12; ++i) {
        __builtin_amdgcn_sched_group_barrier(0x008, 3, 0);
        __builtin_amdgcn_sched_group_barrier(0x100, 2, 0);
      }
    }
  }
  __builtin_amdgcn_sched_barrier(0);
}
DEV void pair_sync(char* smem, int cw, int which) { pair_sync_at(lds_addr(smem + K1_FLAG + cw * 8 + which * 4)); }

__global__ __launch_bounds__(512, 2) void k1_kernel(P1 p) {
  extern __shared__ __attribute__((aligned(16))) char smem[];
  const int tid = threadIdx.x, lane = tid & 63, w = __builtin_amdgcn_readfirstlane(tid >> 6);
  const int b = blockIdx.x, xcd = b & 7, idx = b >> 3;
  const int mt = (xcd >> 1) * 8 + (idx >> 2), nt = (xcd & 1) * 4 + (idx & 3);
  const int t0 = mt * 64, d0 = nt * 64;
  const int g = lane >> 4, n = lane & 15;
  TSDECL;
  TS(0);
  if (w >= 4) __builtin_amdgcn_s_setprio(3);
  if (w == 4) {
    asm volatile("ds_write_b32 %0, %1\n\ts_waitcnt lgkmcnt(0)" ::"v"(lds_addr(smem + K1_FLAG + (lane & 15) * 4)), "v"(0u) : "memory");
  }
  BAR();
  __builtin_amdgcn_sched_barrier(0);
  if (w < 4) {
    __builtin_amdgcn_s_setprio(FPRIO);
    f32x4 acc[4][4];
#pragma unroll
    for (int a = 0; a < 4; ++a)
#pragma unroll
      for (int c = 0; c < 4; ++c) acc[a][c] = (f32x4){0, 0, 0, 0};
    const u16* hp = p.h + ((size_t)mt * KS2 + 14 * w) * 2048 + lane * 8;
    const u16* wp = p.wdt + ((size_t)nt * KS2 + 14 * w) * 2048 + lane * 8;
#define PFD 4
    constexpr int PF = PFD;
    u32x4 hf[PF][4], wf[PF][4];
    const int hrot = w * 512;
#pragma unroll
    for (int s = 0; s < PF; ++s)
#pragma unroll
      for (int a = 0; a < 4; ++a) {
        hf[s][a] = *(const u32x4*)(hp + s * 2048 + ((a * 512 + hrot) & 2047));
        wf[s][a] = *(const u32x4*)(wp + s * 2048 + a * 512);
      }
    const int t = t0 + 16 * w + n;
    float gt;
    __builtin_amdgcn_sched_barrier(0);
    TS(15);
    __builtin_amdgcn_sched_barrier(0);
#pragma unroll
    for (int s = 0; s < 14; ++s) {
#pragma unroll
      for (int a = 0; a < 4; ++a)
#pragma unroll
        for (int c = 0; c < 4; ++c) acc[a][c] = mfma16(wf[s % PF][a], hf[s % PF][c], acc[a][c]);
      __builtin_amdgcn_sched_barrier(0);
      if (s + PF < 14) {
#pragma unroll
        for (int a = 0; a < 4; ++a) {
          hf[s % PF][a] = *(const u32x4*)(hp + (s + PF) * 2048 + ((a * 512 + hrot) & 2047));
          wf[s % PF][a] = *(const u32x4*)(wp + (s + PF) * 2048 + a * 512);
        }
      }
      if (s == GO_STEP) asm volatile("ds_write_b32 %0, %1" ::"v"(lds_addr(smem + K1_FLAG + 32 + w * 4)), "v"(1u) : "memory");
      if (s + PF == 14) gt = p.g[t];
      __builtin_amdgcn_sched_barrier(0);
    }
    TS(1);
#pragma unroll
    for (int c = 1; c < 4; ++c)
#pragma unroll
      for (int a = 0; a < 4; ++a) *(f32x4*)(smem + K1_RED + ((((w + c) & 3) * 3 + (c - 1)) * 4 + a) * 1024 + lane * 16) = acc[a][c];
    WAIT_L(0);
    BAR();
    TS(7);
    const float omg = rbf(1.f - gt);
    const int q = n >> 2, pp = lane & 3;
#pragma unroll
    for (int a = 0; a < 4; ++a) {
      f32x4 bs = acc[a][0];
#pragma unroll
      for (int s = 0; s < 3; ++s) bs += *(const f32x4*)(smem + K1_RED + ((w * 3 + s) * 4 + a) * 1024 + lane * 16);
      const u32x2 yv = tr_read(smem + K1_YOFF + (16 * a + 4 * g + q) * 144 + (16 * w + 4 * pp) * 2);
      u32x2 o;
#pragma unroll
      for (int e = 0; e < 2; ++e) {
        const uint32_t ga = pk2(gt * bflo(yv[e]), gt * bfhi(yv[e]));
        const uint32_t bb = pk2(bs[2 * e], bs[2 * e + 1]);
        const uint32_t gb = pk2(omg * bflo(bb), omg * bfhi(bb));
        o[e] = pk2(bflo(ga) + bflo(gb), bfhi(ga) + bfhi(gb));
      }
      *(u32x2*)(p.out + (size_t)t * D + d0 + 16 * a + 4 * g) = o;
    }
    TS(9);
  } else {
    const int cw = w - 4, c = cw >> 1, half = cw & 1, d = 2 * b + c;
    char* base = smem + c * CW_SIZE;
    const uint32_t dsk2 = *(const uint32_t*)(p.Dskip + 2 * b);
    const float dsk = c ? bfhi(dsk2) : bflo(dsk2);
    {
      const u16* eg = p.E + (size_t)d * ES;
      u32x4 cur[3];
      u32x2 prv[3];
#pragma unroll
      for (int it = 0; it < 3; ++it) {
        const int un = it < 2 ? (2 * half + it) * 64 + lane : 256 + (lane & 1);
        asm volatile("global_load_dwordx4 %0, %2, %3\n\tglobal_load_dwordx2 %1, %2, %3 offset:-8"
                     : "=&v"(cur[it]), "=&v"(prv[it]) : "v"(un * 16u), "s"(eg) : "memory");
      }
      const uint32_t lb = lds_addr(base + CW_U + 512) + half * 2048;
#pragma unroll
      for (int i = 0; i < 2; ++i) glds16(p.uT + (size_t)d * L + (2 * half + i) * 512, lane * 16u, lb + i * 1024);
      if (lane < 32 && half == 0) *(u32x4*)(base + CW_U + lane * 16) = (u32x4){0, 0, 0, 0};
      asm volatile("s_waitcnt vmcnt(2)" : "+v"(cur[0]), "+v"(cur[1]), "+v"(cur[2]), "+v"(prv[0]), "+v"(prv[1]), "+v"(prv[2])::"memory");
      TS(2);
      if (half == 0) {
        if (lane < 2) cur[0] = (u32x4){0, 0, 0, 0};
        if (lane < 3) prv[0] = (u32x2){0, 0};
      }
#pragma unroll
      for (int it = 0; it < 3; ++it) {
        const int un = it < 2 ? (2 * half + it) * 64 + lane : 256 + (lane & 1);
        const u32x4 cu = cur[it];
        const u32x2 pv = prv[it];
        u32x4 o1, o2, o3;
        o1[0] = __builtin_amdgcn_alignbyte(cu[0], pv[1], 2); o1[1] = __builtin_amdgcn_alignbyte(cu[1], cu[0], 2);
        o1[2] = __builtin_amdgcn_alignbyte(cu[2], cu[1], 2); o1[3] = __builtin_amdgcn_alignbyte(cu[3], cu[2], 2);
        o2[0] = pv[1]; o2[1] = cu[0]; o2[2] = cu[1]; o2[3] = cu[2];
        o3[0] = __builtin_amdgcn_alignbyte(pv[1], pv[0], 2); o3[1] = o1[0]; o3[2] = o1[1]; o3[3] = o1[2];
        if (it < 2 || (lane < 2 && half == 1)) {
          *(u32x4*)(base + 0 * CP_STRIDE + un * 16) = cu;
          *(u32x4*)(base + 1 * CP_STRIDE + un * 16) = o1;
          *(u32x4*)(base + 2 * CP_STRIDE + un * 16) = o2;
          *(u32x4*)(base + 3 * CP_STRIDE + un * 16) = o3;
        }
      }
    }
    WAIT_V(0);
    pair_sync(smem, cw, 0);
    __builtin_amdgcn_s_setprio(LPRIO);
    TS(3);
    f32x4 y[8];
#pragma unroll
    for (int a = 0; a < 8; ++a) y[a] = (f32x4){0, 0, 0, 0};
    {
      const int q = n >> 2, pp = lane & 3;
      const char* abase = base + q * CP_STRIDE + (16 - 8 * g + 4 * pp) * 2 + 32 * (8 * half + 1);
      const char* bbase = base + CW_U + 512 + (16 * n + 8 * g) * 2 - 32 * (8 * half + 1);
      conv_loop(abase, bbase, y);
    }
    TS(4);
    __builtin_amdgcn_s_setprio(3);
#pragma unroll
    for (int a = 0; a < 4; ++a) *(f32x4*)(smem + K1_XCH + (cw * 4 + a) * 1024 + lane * 16) = half ? y[a] : y[4 + a];
    pair_sync(smem, cw, 1);
    TS(8);
#pragma unroll
    for (int a = 0; a < 4; ++a) {
      const f32x4 o = *(const f32x4*)(smem + K1_XCH + ((cw ^ 1) * 4 + a) * 1024 + lane * 16);
      const f32x4 v = half ? o + y[4 + a] : y[a] + o;
      const int ntile = 4 * half + a;
      uint32_t v0 = pk2(v[0], v[1]), v1 = pk2(v[2], v[3]);
      {
        const u32x2 uu = *(const u32x2*)(base + CW_U + 512 + (256 * ntile + 16 * n + 4 * g) * 2);
        const uint32_t s0 = pk2(bflo(uu[0]) * dsk, bfhi(uu[0]) * dsk), s1 = pk2(bflo(uu[1]) * dsk, bfhi(uu[1]) * dsk);
        v0 = pk2(bflo(v0) + bflo(s0), bfhi(v0) + bfhi(s0));
        v1 = pk2(bflo(v1) + bflo(s1), bfhi(v1) + bfhi(s1));
      }
      v0 = v0 == ~0u ? 0xfffffffeu : v0;
      v1 = v1 == ~0u ? 0xfffffffeu : v1;
      __hip_atomic_store((gu64*)(p.yT + (size_t)d * L + 256 * ntile + 16 * n + 4 * g), (unsigned long long)v0 | ((unsigned long long)v1 << 32), RLX_AGENT);
    }
    __builtin_amdgcn_s_setprio(0);
    TS(5);
    {
      const int dd = 16 * cw + (lane >> 2), ch = lane & 3;
      const uint32_t off = (uint32_t)((d0 + dd) * L + t0 + ch * 8) * 2u;
      u32x4 v0, v1;
      for (unsigned spins = 0; spins < (1u << 22); ++spins) {
        uint32_t x;
        asm volatile("ds_read_b32 %0, %1\n\ts_waitcnt lgkmcnt(0)" : "=v"(x) : "v"(lds_addr(smem + K1_FLAG + 32 + cw * 4)) : "memory");
        if (__builtin_amdgcn_readfirstlane(x) == 1u) break;
        __builtin_amdgcn_s_sleep(2);
      }
      for (unsigned spins = 0; spins < (1u << 20); ++spins) {
        asm volatile("global_load_dwordx4 %0, %2, %3 sc1\n\tglobal_load_dwordx4 %1, %2, %3 offset:64 sc1\n\ts_waitcnt vmcnt(0)"
                     : "=&v"(v0), "=&v"(v1) : "v"(off), "s"(p.yT) : "memory");
        const bool ok = v0[0] != ~0u && v0[1] != ~0u && v0[2] != ~0u && v0[3] != ~0u && v1[0] != ~0u && v1[1] != ~0u && v1[2] != ~0u && v1[3] != ~0u;
        if (__all(ok)) break;
        __builtin_amdgcn_s_sleep(4);
      }
      *(u32x4*)(smem + K1_YOFF + dd * 144 + ch * 16) = v0;
      *(u32x4*)(smem + K1_YOFF + dd * 144 + ch * 16 + 64) = v1;
    }
    TS(6);
    WAIT_L(0);
    BAR();
  }
  TSDUMP(1);
}

extern "C" void kernel_launch(void* const* d_in, const int* in_sizes, int n_in, void* d_out, int out_size, void* d_ws, size_t ws_size,
                              hipStream_t stream) {
  char* ws = (char*)d_ws;
  P0 p0;
  p0.u = (const u16*)d_in[0]; p0.Are = (const u16*)d_in[1]; p0.Aim = (const u16*)d_in[2]; p0.Bre = (const u16*)d_in[3];
  p0.Bim = (const u16*)d_in[4]; p0.Cre = (const u16*)d_in[5]; p0.Cim = (const u16*)d_in[6]; p0.logdt = (const u16*)d_in[7];
  p0.wg = (const u16*)d_in[10]; p0.wu = (const u16*)d_in[11]; p0.wd = (const u16*)d_in[12]; p0.gw = (const u16*)d_in[13];
  p0.h = (u16*)(ws + WS_H); p0.wdt = (u16*)(ws + WS_WDT); p0.uT = (u16*)(ws + WS_UT); p0.E = (u16*)(ws + WS_E);
  p0.yT = (u16*)(ws + WS_YT); p0.g = (float*)(ws + WS_G);
  P1 p1;
  p1.u = p0.u; p1.Dskip = (const u16*)d_in[8]; p1.h = p0.h; p1.wdt = p0.wdt; p1.uT = p0.uT; p1.E = p0.E; p1.g = p0.g;
  p1.yT = p0.yT; p1.out = (u16*)d_out;
  (void)hipFuncSetAttribute((const void*)k0_kernel, hipFuncAttributeMaxDynamicSharedMemorySize, K0_LDS);
  (void)hipFuncSetAttribute((const void*)k1_kernel, hipFuncAttributeMaxDynamicSharedMemorySize, K1_LDS);
  k0_kernel<<<256, 768, K0_LDS, stream>>>(p0);
  k1_kernel<<<256, 512, K1_LDS, stream>>>(p1);
}
```

```cpp
#include <hip/hip_runtime.h>
#include <hip/hip_bf16.h>
#include <stdint.h>

typedef unsigned short u16;
typedef __attribute__((ext_vector_type(4))) short s16x4;
typedef __attribute__((ext_vector_type(8))) short s16x8;
typedef __attribute__((ext_vector_type(8))) __bf16 bf16x8;
typedef __attribute__((ext_vector_type(2))) __bf16 bf16x2;
typedef __attribute__((ext_vector_type(2))) float f32x2;
typedef __attribute__((ext_vector_type(4))) float f32x4;
typedef __attribute__((ext_vector_type(4))) unsigned u32x4;
typedef __attribute__((ext_vector_type(2))) unsigned u32x2;
typedef __attribute__((address_space(3))) s16x4* lds_s16x4_ptr;
typedef __attribute__((address_space(1))) unsigned gu32;
typedef __attribute__((address_space(1))) unsigned long long gu64;
#define RLX_AGENT __ATOMIC_RELAXED, __HIP_MEMORY_SCOPE_AGENT
#define DEV __device__ __forceinline__

constexpr int L = 2048, D = 512, NM = 128, F = 1792;
constexpr int KS2 = F / 32;

constexpr size_t WS_H = 0;
constexpr size_t WS_WDT = WS_H + (size_t)L * F * 2;
constexpr size_t WS_UT = WS_WDT + (size_t)F * D * 2;
constexpr size_t WS_YT = WS_UT + (size_t)L * D * 2;
constexpr int ES = 2080;
constexpr size_t WS_E = WS_YT + (size_t)L * D * 2 + 64;
constexpr size_t WS_G = WS_E + (size_t)D * ES * 2;

#define TSDECL
#define TS(ph)
#define TSDUMP(K)
#define TS_ARG
#define TS_PASS
DEV float bf2f(u16 v) { return __uint_as_float(((uint32_t)v) << 16); }
DEV float bflo(uint32_t v) { return __uint_as_float(v << 16); }
DEV float bfhi(uint32_t v) { return __uint_as_float(v & 0xffff0000u); }
DEV uint32_t pk2(float a, float b) { f32x2 v = {a, b}; return __builtin_bit_cast(uint32_t, __builtin_convertvector(v, bf16x2)); }
DEV float rbf(float a) { return bflo(pk2(a, 0.f) & 0xffffu); }
DEV f32x4 mfma16(u32x4 a, u32x4 b, f32x4 c) {
  return __builtin_amdgcn_mfma_f32_16x16x32_bf16(__builtin_bit_cast(bf16x8, a), __builtin_bit_cast(bf16x8, b), c, 0, 0, 0);
}
DEV u32x2 tr_read(const char* p) {
  return __builtin_bit_cast(u32x2, __builtin_amdgcn_ds_read_tr16_b64_v4i16((lds_s16x4_ptr)p));
}
DEV u32x4 tr_read2(const char* p0, const char* p1) {
  u32x2 a = tr_read(p0), b = tr_read(p1);
  u32x4 r = {a.x, a.y, b.x, b.y};
  return r;
}
DEV uint32_t lds_addr(const void* p) { return (uint32_t)(uintptr_t)(__attribute__((address_space(3))) const char*)p; }
DEV void glds16(const void* sbase, uint32_t voff, uint32_t lds) {
  unsigned keep;
  asm volatile("s_mov_b32 %0, m0\n\ts_mov_b32 m0, %3\n\ts_nop 0\n\tglobal_load_lds_dwordx4 %1, %2\n\ts_mov_b32 m0, %0"
               : "=&s"(keep) : "v"(voff), "s"(sbase), "s"(lds) : "memory");
}
#define WAIT_V(n) asm volatile("s_waitcnt vmcnt(" #n ")" ::: "memory")
#define WAIT_L(n) asm volatile("s_waitcnt lgkmcnt(" #n ")" ::: "memory")
#define BAR() __builtin_amdgcn_s_barrier()

template <bool WT>
DEV void st16(void* base, size_t byte_off, u32x4 v) {
  if (WT) {
    __builtin_amdgcn_raw_buffer_store_b128(v, __builtin_amdgcn_make_buffer_rsrc(base, 0, 0x7fffffff, 0x00020000), (uint32_t)byte_off, 0, 16);
  } else {
    *(u32x4*)((char*)base + byte_off) = v;
  }
}
#define WT_AUX 1
#define WT_H 1

struct P0 {
  const u16 *u, *Are, *Aim, *Bre, *Bim, *Cre, *Cim, *logdt, *wg, *wu, *wd, *gw, *Dskip, *out;
  u16 *h, *wdt, *uT, *E, *yT;
  float* g;
};
typedef const __attribute__((address_space(4))) uint32_t* cu32p;

constexpr int STAGE_B = 49152;
constexpr int K0_LDS = 3 * STAGE_B + 15360;

DEV int wswz(int row) { return ((row & 3) << 2) | ((row >> 2) & 3); }

struct SsmPar { u32x4 are, aim, bre, bim, cre, cim; };

DEV void ssm_load(const P0& p, uint32_t off, SsmPar& s) {
  asm volatile("global_load_dwordx4 %0, %1, %2" : "=&v"(s.are) : "v"(off), "s"(p.Are) : "memory");
  asm volatile("global_load_dwordx4 %0, %1, %2" : "=&v"(s.aim) : "v"(off), "s"(p.Aim) : "memory");
  asm volatile("global_load_dwordx4 %0, %1, %2" : "=&v"(s.bre) : "v"(off), "s"(p.Bre) : "memory");
  asm volatile("global_load_dwordx4 %0, %1, %2" : "=&v"(s.bim) : "v"(off), "s"(p.Bim) : "memory");
  asm volatile("global_load_dwordx4 %0, %1, %2" : "=&v"(s.cre) : "v"(off), "s"(p.Cre) : "memory");
  asm volatile("global_load_dwordx4 %0, %1, %2" : "=&v"(s.cim) : "v"(off), "s"(p.Cim) : "memory");
}
#define SSM_WAIT(n, s) SSM_WAIT_(n, s)
#define SSM_WAIT_(n, s) asm volatile("s_waitcnt vmcnt(" #n ")" : "+v"(s.are), "+v"(s.aim), "+v"(s.bre), "+v"(s.bim), "+v"(s.cre), "+v"(s.cim)::"memory")

DEV void pair_sync_at(uint32_t mine) {
  const uint32_t other = mine ^ 8u;
  asm volatile("s_waitcnt lgkmcnt(0)\n\tds_write_b32 %0, %1" ::"v"(mine), "v"(1u) : "memory");
  for (unsigned spins = 0; spins < (1u << 24); ++spins) {
    uint32_t x;
    asm volatile("ds_read_b32 %0, %1\n\ts_waitcnt lgkmcnt(0)" : "=v"(x) : "v"(other) : "memory");
    if (__builtin_amdgcn_readfirstlane(x) == 1u) break;
  }
}

struct KgFrag { u32x4 ah[3], al[3], bh, bl; };
template <int H>
DEV void kgen_half(const SsmPar& sp, float dt, float p0, float p1, float p2, float q, KgFrag& f, f32x4 (&acc)[3]) {
  constexpr int W = H & 3, JH = H & 1;
  uint32_t wa = sp.are[W], wi = sp.aim[W], wbr = sp.bre[W], wbi = sp.bim[W], wcr = sp.cre[W], wci = sp.cim[W];
  asm volatile("" : "+v"(wa), "+v"(wi), "+v"(wbr), "+v"(wbi), "+v"(wcr), "+v"(wci));
#pragma unroll
  for (int jj = 0; jj < 2; ++jj) {
    const int j = 2 * JH + jj;
    const float ar = dt * (jj ? bfhi(wa) : bflo(wa)), ai = dt * (jj ? bfhi(wi) : bflo(wi));
    const float br = dt * (jj ? bfhi(wbr) : bflo(wbr)), bi = dt * (jj ? bfhi(wbi) : bflo(wbi));
    const float cr = jj ? bfhi(wcr) : bflo(wcr), ci = jj ? bfhi(wci) : bflo(wci);
    const float a2 = ar * 1.44269504088896340736f, wv = ai * 0.15915494309189533577f;
    const float xr = cr * br - ci * bi, xi = cr * bi + ci * br;
    {
      const float e = __builtin_amdgcn_exp2f(a2 * q), r = __builtin_amdgcn_fractf(wv * q);
      const float s = __builtin_amdgcn_sinf(r) * e, co = __builtin_amdgcn_cosf(r) * e;
      const float vr = xr * co - xi * s, vi = -(xr * s + xi * co);
      const uint32_t hi = pk2(vr, vi);
      f.bh[j] = hi;
      f.bl[j] = pk2(vr - bflo(hi), vi - bfhi(hi));
    }
#pragma unroll
    for (int T = 0; T < 3; ++T) {
      const float pp = T == 0 ? p0 : (T == 1 ? p1 : p2);
      const float e = __builtin_amdgcn_exp2f(a2 * pp), r = __builtin_amdgcn_fractf(wv * pp);
      const float wr = __builtin_amdgcn_cosf(r) * e, wim = __builtin_amdgcn_sinf(r) * e;
      const uint32_t hi = pk2(wr, wim);
      f.ah[T][j] = hi;
      f.al[T][j] = pk2(wr - bflo(hi), wim - bfhi(hi));
    }
  }
  if (JH == 1) {
#pragma unroll
    for (int T = 0; T < 3; ++T) {
      acc[T] = mfma16(f.al[T], f.bh, acc[T]);
      acc[T] = mfma16(f.ah[T], f.bl, acc[T]);
      acc[T] = mfma16(f.ah[T], f.bh, acc[T]);
    }
    asm volatile("" : "+v"(acc[0]), "+v"(acc[1]), "+v"(acc[2]));
  } else {
    asm volatile("" : "+v"(f.ah[0]), "+v"(f.ah[1]), "+v"(f.ah[2]), "+v"(f.al[0]), "+v"(f.al[1]), "+v"(f.al[2]), "+v"(f.bh), "+v"(f.bl));
  }
  __builtin_amdgcn_sched_barrier(0);
}

#define KTS(i)
DEV void kgen_waves(const P0& p, char* scratch, int b, bool aux, int tid, uint32_t ld2, uint32_t ds2 TS_ARG) {
  const int t = tid - 512, kw = __builtin_amdgcn_readfirstlane(t >> 6), lane = t & 63, i = lane & 15, ch0 = 2 * b, cc = kw >> 1;
#define TOUCH 0
#define SSMW0 6
#define SSMW1 0
#define KPRIO 3
#define KPRIO2 KPRIO
#define NPRE 4
  __builtin_amdgcn_s_setprio(KPRIO);
  SsmPar sp0, sp1;
  const uint32_t off = (uint32_t)((ch0 + cc) * NM + 64 * (kw & 1) + 8 * (lane >> 4)) * 2u;
  ssm_load(p, off, sp0);
  ssm_load(p, off + 64u, sp1);
  __builtin_amdgcn_sched_barrier(0);
  BAR();
  __builtin_amdgcn_sched_barrier(0);
  const float dt = rbf(__expf(cc ? bfhi(ld2) : bflo(ld2)));
  const float p0 = (float)((i & 7) + 128 * (i >> 3));
  const float p1 = (i < 8) ? (float)(256 + 2 * (i & 3) + 128 * (i >> 2)) : (float)(512 + 4 * (i & 1) + 128 * ((i - 8) >> 1));
  const float p2 = (i < 8) ? (float)(1024 + 128 * i) : 2048.f;
  const float q = (float)(8 * i);
  f32x4 acc[3] = {{0, 0, 0, 0}, {0, 0, 0, 0}, {0, 0, 0, 0}};
  KgFrag f;
#pragma unroll
  for (int T = 0; T < 3; ++T) f.ah[T] = f.al[T] = (u32x4){0, 0, 0, 0};
  f.bh = f.bl = (u32x4){0, 0, 0, 0};
  SSM_WAIT(SSMW0, sp0);
  TS(1);
  if (0 == NPRE || (0 > NPRE && !aux)) BAR();
  kgen_half<0>(sp0, dt, p0, p1, p2, q, f, acc);
  KTS(3);
  if (1 == NPRE || (1 > NPRE && !aux)) BAR();
  kgen_half<1>(sp0, dt, p0, p1, p2, q, f, acc);
  KTS(4);
  if (2 == NPRE || (2 > NPRE && !aux)) BAR();
  kgen_half<2>(sp0, dt, p0, p1, p2, q, f, acc);
  KTS(5);
  if (3 == NPRE || (3 > NPRE && !aux)) BAR();
  kgen_half<3>(sp0, dt, p0, p1, p2, q, f, acc);
  KTS(6);
  if (4 == NPRE || (4 > NPRE && !aux)) BAR();
  SSM_WAIT(SSMW1, sp1);
  kgen_half<4>(sp1, dt, p0, p1, p2, q, f, acc);
  KTS(7);
  if (5 == NPRE || (5 > NPRE && !aux)) BAR();
  kgen_half<5>(sp1, dt, p0, p1, p2, q, f, acc);
  KTS(8);
  if (6 == NPRE || (6 > NPRE && !aux)) BAR();
  kgen_half<6>(sp1, dt, p0, p1, p2, q, f, acc);
  KTS(9);
  if (7 == NPRE || (7 > NPRE && !aux)) BAR();
  kgen_half<7>(sp1, dt, p0, p1, p2, q, f, acc);
  float* sc = (float*)scratch;
  u16* kc = (u16*)(scratch + 12288) + cc * 768;
#pragma unroll
  for (int T = 0; T < 3; ++T) *(f32x4*)(sc + ((kw * 3 + T) * 64 + lane) * 4) = acc[T];
  WAIT_L(0);
  TS(15);
  BAR();
  const int tt = t & 127;
#pragma unroll
  for (int T = 0; T < 3; ++T) {
    if (T == 2 && !aux) BAR();
#pragma unroll
    for (int hf = 0; hf < 2; ++hf) {
      const int e = hf * 128 + tt, ln = e >> 2, j = e & 3;
      const int ii = 4 * (ln >> 4) + j, bb = ln & 15;
      const float s = sc[((cc * 2) * 3 + T) * 256 + e] + sc[((cc * 2 + 1) * 3 + T) * 256 + e];
      int idx;
      if (T == 0) idx = (ii & 7) + 128 * (ii >> 3) + 8 * bb;
      else if (T == 1) idx = ii < 8 ? 256 + (ii & 3) + 64 * (ii >> 2) + 4 * bb : 384 + (ii & 1) + 32 * ((ii - 8) >> 1) + 2 * bb;
      else idx = ii < 8 ? 512 + 16 * ii + bb : ((ii == 8 && bb == 0) ? 640 : 700);
      kc[idx] = (u16)(pk2(s, 0.f) & 0xffffu);
    }
    __builtin_amdgcn_sched_barrier(0);
  }
  WAIT_L(0);
  BAR();
  if ((t & 127) == 0) {
    const uint32_t dv = cc ? ds2 >> 16 : ds2 & 0xffffu;
    __builtin_amdgcn_raw_buffer_store_b32(dv, __builtin_amdgcn_make_buffer_rsrc(p.E, 0, 0x7fffffff, 0x00020000), (uint32_t)((ch0 + cc) * ES * 2), 0, 16);
  }
#pragma unroll
  for (int hf = 0; hf < 2; ++hf) {
    if (hf == 1 && !aux) BAR();
    int l0 = 1024 * hf + 8 * tt;
    asm volatile("" : "+v"(l0));
    u32x4 o;
#pragma unroll
    for (int e = 0; e < 4; ++e) {
      const int la = l0 + 2 * e, lb = la + 1;
      const uint32_t pb = pk2((float)la, (float)lb);
      const int ia = la < 128 ? la : (int)(pb & 0xffffu) - 17024, ib = lb < 128 ? lb : (int)(pb >> 16) - 17024;
      o[e] = (uint32_t)kc[ia] | ((uint32_t)kc[ib] << 16);
    }
    st16<WT_AUX>(p.E, ((size_t)(ch0 + cc) * ES + 16 + l0) * 2, o);
    __builtin_amdgcn_sched_barrier(0);
  }
  st16<WT_AUX>(p.yT, ((size_t)b * 4096 + t * 8) * 2, (u32x4){~0u, ~0u, ~0u, ~0u});
  st16<WT_AUX>(p.yT, ((size_t)b * 4096 + 2048 + t * 8) * 2, (u32x4){~0u, ~0u, ~0u, ~0u});
  TS(14);
  if (!aux)
    for (int i_ = 0; i_ < NPRE - 4; ++i_) BAR();
}

DEV void k0_glds(const P0& p, uint32_t lb, const uint32_t (&voff)[3], int m0, int f0, int k0, int piece) {
  if (piece < 2) glds16(p.u + (size_t)m0 * D + k0, voff[0] + piece * (8 * D * 2), lb + piece * 1024);
  else if (piece < 4) glds16(p.wg + (size_t)k0 * F + f0, voff[piece - 1], lb + 16384 + (piece - 2) * 1024);
  else glds16(p.wu + (size_t)k0 * F + f0, voff[piece - 3], lb + 32768 + (piece - 4) * 1024);
}

__global__ __launch_bounds__(768, 3) void k0_kernel(P0 p) {
  extern __shared__ __attribute__((aligned(16))) char smem[];
  const int tid = threadIdx.x, lane = tid & 63, w = __builtin_amdgcn_readfirstlane(tid >> 6);
  const int b = blockIdx.x, xcd = b & 7, idx = b >> 3;
  TSDECL;
  TS(0);
  if (w >= 8) {
    const uint32_t ld2 = ((cu32p)(uintptr_t)p.logdt)[b], ds2 = ((cu32p)(uintptr_t)p.Dskip)[b];
    kgen_waves(p, smem + 3 * STAGE_B, b, idx >= 28, tid, ld2, ds2 TS_PASS);
  } else if (idx < 28) {
    const int mt = (xcd >> 1) * 4 + idx / 7, ft = (xcd & 1) * 7 + idx % 7;
    const int m0 = mt * 128, f0 = ft * 128;
#define K0ROT 2
    const int rot = K0ROT == 0 ? 2 * (idx / 7) + idx % 7 : K0ROT == 1 ? 0 : K0ROT == 2 ? (idx & 1) * 4 : K0ROT == 3 ? 2 * (idx / 7) : idx % 7;
    uint32_t voff[3];
    {
      const int row = 16 * w + (lane >> 3);
      voff[0] = (uint32_t)(row * D + 8 * ((lane & 7) ^ (row & 7))) * 2u;
      const int r0 = 8 * w + (lane >> 4), r1 = r0 + 4;
      voff[1] = (uint32_t)(r0 * F + 8 * ((lane & 15) ^ wswz(r0))) * 2u;
      voff[2] = (uint32_t)(r1 * F + 8 * ((lane & 15) ^ wswz(r1))) * 2u;
    }
    const uint32_t lb = lds_addr(smem) + 2048 * w;
#pragma unroll
    for (int s = 0; s < 3; ++s) {
      if (s == 1) BAR();
#pragma unroll
      for (int pc = 0; pc < 6; ++pc) k0_glds(p, lb + s * STAGE_B, voff, m0, f0, 64 * ((rot + s) & 7), pc);
    }
    const int wm = w >> 2, wn = w & 3, g = lane >> 4, q = (lane & 15) >> 2, pp = lane & 3;
    f32x4 acc[2][2][4];
#pragma unroll
    for (int a = 0; a < 2; ++a)
#pragma unroll
      for (int c = 0; c < 2; ++c)
#pragma unroll
        for (int m = 0; m < 4; ++m) acc[a][c][m] = (f32x4){0, 0, 0, 0};
    u32x4 xf[2][4], wf[2][2][2];
    auto frags = [&](const char* buf, int ss) {
#pragma unroll
      for (int m = 0; m < 4; ++m) {
        const int row = 64 * wm + 16 * m + (lane & 15), c = 4 * ss + g;
        xf[ss][m] = *(const u32x4*)(buf + row * 128 + ((c ^ (row & 7)) << 4));
      }
#pragma unroll
      for (int a = 0; a < 2; ++a)
#pragma unroll
        for (int c = 0; c < 2; ++c) {
          const int ch = 4 * wn + 2 * c + (pp >> 1);
          const int r0 = 32 * ss + 8 * g + q, r1 = r0 + 4;
          const char* wb = buf + 16384 + a * 16384 + 8 * (pp & 1);
          wf[ss][a][c] = tr_read2(wb + 256 * r0 + 16 * (ch ^ wswz(r0)), wb + 256 * r1 + 16 * (ch ^ wswz(r1)));
        }
    };
    auto mm = [&](int ss, bool stage, int sb, int k0, int pc0) {
#pragma unroll
      for (int ac = 0; ac < 4; ++ac) {
#pragma unroll
        for (int m = 0; m < 4; ++m) acc[ac >> 1][ac & 1][m] = mfma16(wf[ss][ac >> 1][ac & 1], xf[ss][m], acc[ac >> 1][ac & 1][m]);
        if (stage && ac < 3) {
          __builtin_amdgcn_sched_barrier(0);
          k0_glds(p, lb + sb * STAGE_B, voff, m0, f0, k0, pc0 + ac);
          __builtin_amdgcn_sched_barrier(0);
        }
      }
    };
#pragma unroll
    for (int s = 0; s < 8; ++s) {
      if (s == 7) WAIT_V(0); else if (s == 0) WAIT_V(12); else WAIT_V(6);
      WAIT_L(0);
      BAR();
      TS(3 + s);
      int bo = (s % 3) * STAGE_B;
      asm volatile("" : "+s"(bo));
      const char* buf = smem + bo;
      const bool st = (s >= 1 && s + 2 < 8);
      const int k0 = 64 * ((rot + s + 2) & 7);
      frags(buf, 0);
      __builtin_amdgcn_sched_barrier(0);
      if (s > 0) mm(1, st, (s + 2) % 3, k0, 0);
      __builtin_amdgcn_sched_barrier(0);
      frags(buf, 1);
      __builtin_amdgcn_sched_barrier(0);
      mm(0, st, (s + 2) % 3, k0, 3);
      __builtin_amdgcn_sched_barrier(0);
    }
    mm(1, false, 0, 0, 0);
    TS(11);
#pragma unroll
    for (int m = 0; m < 4; ++m) {
      u32x4 o;
#pragma unroll
      for (int c = 0; c < 2; ++c)
#pragma unroll
        for (int jj = 0; jj < 2; ++jj) {
          const uint32_t g2 = pk2(acc[0][c][m][2 * jj], acc[0][c][m][2 * jj + 1]), u2 = pk2(acc[1][c][m][2 * jj], acc[1][c][m][2 * jj + 1]);
          const f32x2 gg = {bflo(g2), bfhi(g2)};
          const f32x2 tt = gg * -1.44269504088896340736f;
          const f32x2 dn = (f32x2){__builtin_amdgcn_exp2f(tt[0]), __builtin_amdgcn_exp2f(tt[1])} + 1.f;
          const f32x2 sv = gg * (f32x2){__builtin_amdgcn_rcpf(dn[0]), __builtin_amdgcn_rcpf(dn[1])};
          const uint32_t s2 = pk2(sv[0], sv[1]);
          const f32x2 hv = (f32x2){bflo(s2), bfhi(s2)} * (f32x2){bflo(u2), bfhi(u2)};
          o[2 * c + jj] = pk2(hv[0], hv[1]);
        }
      const int mt64 = (m0 >> 6) + wm, ks = 4 * ft + wn;
      st16<WT_H>(p.h, ((size_t)((mt64 * KS2 + ks) * 4 + m) * 64 + lane) * 16, o);
    }
    TS(12);
  } else {
    BAR();
    const int a = xcd * 4 + (idx - 28);
    constexpr int RS = 1056;
    const int t0 = 64 * a;
    u32x4 uv[8], wv[8];
#pragma unroll
    for (int r = 0; r < 8; ++r) uv[r] = *(const u32x4*)(p.u + (size_t)(t0 + 8 * w + r) * D + lane * 8);
    const int ks2 = a + 32 < KS2 ? a + 32 : a;
#pragma unroll
    for (int r = 0; r < 4; ++r) {
      wv[r] = *(const u32x4*)(p.wd + (size_t)(32 * a + 4 * w + r) * D + lane * 8);
      wv[4 + r] = *(const u32x4*)(p.wd + (size_t)(32 * ks2 + 4 * w + r) * D + lane * 8);
    }
    const u32x4 gwv = *(const u32x4*)(p.gw + lane * 8);
    {
      float s[8];
#pragma unroll
      for (int r = 0; r < 8; ++r) {
        *(u32x4*)(smem + (8 * w + r) * RS + lane * 16) = uv[r];
        s[r] = 0.f;
#pragma unroll
        for (int e = 0; e < 4; ++e) {
          const uint32_t pr = pk2(bflo(uv[r][e]) * bflo(gwv[e]), bfhi(uv[r][e]) * bfhi(gwv[e]));
          s[r] += bflo(pr) + bfhi(pr);
        }
      }
#pragma unroll
      for (int r = 0; r < 4; ++r) { const bool hi = lane & 32; const float keep = hi ? s[r + 4] : s[r], send = hi ? s[r] : s[r + 4]; s[r] = keep + __shfl_xor(send, 32); }
#pragma unroll
      for (int r = 0; r < 2; ++r) { const bool hi = lane & 16; const float keep = hi ? s[r + 2] : s[r], send = hi ? s[r] : s[r + 2]; s[r] = keep + __shfl_xor(send, 16); }
      { const bool hi = lane & 8; const float keep = hi ? s[1] : s[0], send = hi ? s[0] : s[1]; s[0] = keep + __shfl_xor(send, 8); }
      s[0] += __shfl_xor(s[0], 4); s[0] += __shfl_xor(s[0], 2); s[0] += __shfl_xor(s[0], 1);
      if ((lane & 7) == 0) {
        const int r = ((lane >> 5) & 1) * 4 + ((lane >> 4) & 1) * 2 + ((lane >> 3) & 1);
        const float gs = rbf(s[0]);
        p.g[t0 + 8 * w + r] = rbf(1.f / (1.f + __expf(-gs)));
      }
    }
    WAIT_L(0);
    BAR();
    const int g = lane >> 4, q = (lane & 15) >> 2, pp = lane & 3;
#pragma unroll
    for (int c = 0; c < 4; ++c)
#pragma unroll
      for (int hh = 0; hh < 2; ++hh) {
        const int dt_ = 4 * w + c;
        const int r0 = 32 * hh + 8 * g + q;
        const char* base = smem + (16 * dt_ + 4 * pp) * 2;
        const u32x4 v = tr_read2(base + r0 * RS, base + (r0 + 4) * RS);
        st16<WT_AUX>(p.uT, ((size_t)(16 * dt_ + (lane & 15)) * L + t0 + 32 * hh + 8 * g) * 2, v);
      }
    TS(3);
    WAIT_L(0);
    BAR();
#pragma unroll
    for (int r = 0; r < 4; ++r) {
      *(u32x4*)(smem + (4 * w + r) * RS + lane * 16) = wv[r];
      *(u32x4*)(smem + (32 + 4 * w + r) * RS + lane * 16) = wv[4 + r];
    }
    WAIT_L(0);
    BAR();
#pragma unroll
    for (int hh = 0; hh < 2; ++hh) {
      if (hh == 0 || a + 32 < KS2) {
        const int ks = a + 32 * hh;
#pragma unroll
        for (int c = 0; c < 4; ++c) {
          const int nt = 4 * w + c;
          const char* base = smem + 32 * hh * RS + (16 * nt + 4 * pp) * 2;
          const u32x4 v = tr_read2(base + (4 * g + q) * RS, base + (16 + 4 * g + q) * RS);
          st16<WT_AUX>(p.wdt, ((size_t)(((nt >> 2) * KS2 + ks) * 4 + (nt & 3)) * 64 + lane) * 16, v);
        }
      }
    }
    TS(12);
  }
  TSDUMP(0);
}

#define GO_STEP 7
#define FPRIO 1
#define LPRIO 0
struct P1 {
  const u16 *u, *Dskip, *h, *wdt, *uT, *E;
  const float* g;
  u16 *yT, *out;
};

constexpr int CP_STRIDE = 4160;
constexpr int CW_U = 4 * CP_STRIDE, CW_SIZE = CW_U + 4608;
constexpr int K1_XCH = 2 * CW_SIZE;
constexpr int K1_YOFF = K1_XCH + 16384;
constexpr int K1_RED = K1_YOFF + 64 * 144;
constexpr int K1_FLAG = K1_RED + 49152;
constexpr int K1_LDS = K1_FLAG + 64;

DEV void conv_loop(const char* abase, const char* bbase, f32x4 (&y)[8]) {
  u32x4 af[2][8], bf[2][8];
  auto ld = [&](int buf, int it) {
#pragma unroll
    for (int m = 0; m < 8; ++m) bf[buf][m] = *(const u32x4*)(bbase + 512 * m - 64 * it);
#pragma unroll
    for (int m = 0; m < 8; ++m) {
      const char* ap = abase + 512 * m + 64 * it;
      af[buf][m] = tr_read2(ap, ap - 8);
    }
  };
  ld(0, 0);
#pragma unroll
  for (int it = 0; it < 4; ++it) {
    const int cur = it & 1;
    __builtin_amdgcn_sched_barrier(0);
    if (it < 3) ld(cur ^ 1, it + 1);
#pragma unroll
    for (int m = 0; m < 8; ++m)
#pragma unroll
      for (int T = m; T < 8; ++T) y[T] = mfma16(af[cur][m], bf[cur][T - m], y[T]);
    if (it < 3) {
#pragma unroll
      for (int i = 0; i < 12; ++i) {
        __builtin_amdgcn_sched_group_barrier(0x008, 3, 0);
        __builtin_amdgcn_sched_group_barrier(0x100, 2, 0);
      }
    }
  }
  __builtin_amdgcn_sched_barrier(0);
}
DEV void pair_sync(char* smem, int cw, int which) { pair_sync_at(lds_addr(smem + K1_FLAG + cw * 8 + which * 4)); }

__global__ __launch_bounds__(512, 2) void k1_kernel(P1 p) {
  extern __shared__ __attribute__((aligned(16))) char smem[];
  const int tid = threadIdx.x, lane = tid & 63, w = __builtin_amdgcn_readfirstlane(tid >> 6);
  const int b = blockIdx.x, xcd = b & 7, idx = b >> 3;
  const int mt = (xcd >> 1) * 8 + (idx >> 2), nt = (xcd & 1) * 4 + (idx & 3);
  const int t0 = mt * 64, d0 = nt * 64;
  const int g = lane >> 4, n = lane & 15;
  TSDECL;
  TS(0);
  if (w >= 4) __builtin_amdgcn_s_setprio(3);
  if (w == 4) {
    asm volatile("ds_write_b32 %0, %1\n\ts_waitcnt lgkmcnt(0)" ::"v"(lds_addr(smem + K1_FLAG + (lane & 15) * 4)), "v"(0u) : "memory");
  }
  BAR();
  __builtin_amdgcn_sched_barrier(0);
  if (w < 4) {
    __builtin_amdgcn_s_setprio(FPRIO);
    f32x4 acc[4][4];
#pragma unroll
    for (int a = 0; a < 4; ++a)
#pragma unroll
      for (int c = 0; c < 4; ++c) acc[a][c] = (f32x4){0, 0, 0, 0};
    const u16* hp = p.h + ((size_t)mt * KS2 + 14 * w) * 2048 + lane * 8;
    const u16* wp = p.wdt + ((size_t)nt * KS2 + 14 * w) * 2048 + lane * 8;
#define PFD 4
    constexpr int PF = PFD;
    u32x4 hf[PF][4], wf[PF][4];
    const int hrot = w * 512;
#pragma unroll
    for (int s = 0; s < PF; ++s)
#pragma unroll
      for (int a = 0; a < 4; ++a) {
        hf[s][a] = *(const u32x4*)(hp + s * 2048 + ((a * 512 + hrot) & 2047));
        wf[s][a] = *(const u32x4*)(wp + s * 2048 + a * 512);
      }
    const int t = t0 + 16 * w + n;
    float gt;
    __builtin_amdgcn_sched_barrier(0);
    TS(15);
    __builtin_amdgcn_sched_barrier(0);
#pragma unroll
    for (int s = 0; s < 14; ++s) {
#pragma unroll
      for (int a = 0; a < 4; ++a)
#pragma unroll
        for (int c = 0; c < 4; ++c) acc[a][c] = mfma16(wf[s % PF][a], hf[s % PF][c], acc[a][c]);
      __builtin_amdgcn_sched_barrier(0);
      if (s + PF < 14) {
#pragma unroll
        for (int a = 0; a < 4; ++a) {
          hf[s % PF][a] = *(const u32x4*)(hp + (s + PF) * 2048 + ((a * 512 + hrot) & 2047));
          wf[s % PF][a] = *(const u32x4*)(wp + (s + PF) * 2048 + a * 512);
        }
      }
      if (s == GO_STEP) asm volatile("ds_write_b32 %0, %1" ::"v"(lds_addr(smem + K1_FLAG + 32 + w * 4)), "v"(1u) : "memory");
      if (s + PF == 14) gt = p.g[t];
      __builtin_amdgcn_sched_barrier(0);
    }
    TS(1);
#pragma unroll
    for (int c = 1; c < 4; ++c)
#pragma unroll
      for (int a = 0; a < 4; ++a) *(f32x4*)(smem + K1_RED + ((((w + c) & 3) * 3 + (c - 1)) * 4 + a) * 1024 + lane * 16) = acc[a][c];
    WAIT_L(0);
    BAR();
    TS(7);
    const float omg = rbf(1.f - gt);
    const int q = n >> 2, pp = lane & 3;
#pragma unroll
    for (int a = 0; a < 4; ++a) {
      f32x4 bs = acc[a][0];
#pragma unroll
      for (int s = 0; s < 3; ++s) bs += *(const f32x4*)(smem + K1_RED + ((w * 3 + s) * 4 + a) * 1024 + lane * 16);
      const u32x2 yv = tr_read(smem + K1_YOFF + (16 * a + 4 * g + q) * 144 + (16 * w + 4 * pp) * 2);
      u32x2 o;
#pragma unroll
      for (int e = 0; e < 2; ++e) {
        const uint32_t ga = pk2(gt * bflo(yv[e]), gt * bfhi(yv[e]));
        const uint32_t bb = pk2(bs[2 * e], bs[2 * e + 1]);
        const uint32_t gb = pk2(omg * bflo(bb), omg * bfhi(bb));
        o[e] = pk2(bflo(ga) + bflo(gb), bfhi(ga) + bfhi(gb));
      }
      *(u32x2*)(p.out + (size_t)t * D + d0 + 16 * a + 4 * g) = o;
    }
    TS(9);
  } else {
    const int cw = w - 4, c = cw >> 1, half = cw & 1, d = 2 * b + c;
    char* base = smem + c * CW_SIZE;
    const float dsk = bflo(*(const uint32_t*)(p.E + (size_t)d * ES));
    {
      const u16* eg = p.E + (size_t)d * ES;
      u32x4 cur[3];
      u32x2 prv[3];
#pragma unroll
      for (int it = 0; it < 3; ++it) {
        const int un = it < 2 ? (2 * half + it) * 64 + lane : 256 + (lane & 1);
        asm volatile("global_load_dwordx4 %0, %2, %3\n\tglobal_load_dwordx2 %1, %2, %3 offset:-8"
                     : "=&v"(cur[it]), "=&v"(prv[it]) : "v"(un * 16u), "s"(eg) : "memory");
      }
      const uint32_t lb = lds_addr(base + CW_U + 512) + half * 2048;
#pragma unroll
      for (int i = 0; i < 2; ++i) glds16(p.uT + (size_t)d * L + (2 * half + i) * 512, lane * 16u, lb + i * 1024);
      if (lane < 32 && half == 0) *(u32x4*)(base + CW_U + lane * 16) = (u32x4){0, 0, 0, 0};
      asm volatile("s_waitcnt vmcnt(2)" : "+v"(cur[0]), "+v"(cur[1]), "+v"(cur[2]), "+v"(prv[0]), "+v"(prv[1]), "+v"(prv[2])::"memory");
      TS(2);
      if (half == 0) {
        if (lane < 2) cur[0] = (u32x4){0, 0, 0, 0};
        if (lane < 3) prv[0] = (u32x2){0, 0};
      }
#pragma unroll
      for (int it = 0; it < 3; ++it) {
        const int un = it < 2 ? (2 * half + it) * 64 + lane : 256 + (lane & 1);
        const u32x4 cu = cur[it];
        const u32x2 pv = prv[it];
        u32x4 o1, o2, o3;
        o1[0] = __builtin_amdgcn_alignbyte(cu[0], pv[1], 2); o1[1] = __builtin_amdgcn_alignbyte(cu[1], cu[0], 2);
        o1[2] = __builtin_amdgcn_alignbyte(cu[2], cu[1], 2); o1[3] = __builtin_amdgcn_alignbyte(cu[3], cu[2], 2);
        o2[0] = pv[1]; o2[1] = cu[0]; o2[2] = cu[1]; o2[3] = cu[2];
        o3[0] = __builtin_amdgcn_alignbyte(pv[1], pv[0], 2); o3[1] = o1[0]; o3[2] = o1[1]; o3[3] = o1[2];
        if (it < 2 || (lane < 2 && half == 1)) {
          *(u32x4*)(base + 0 * CP_STRIDE + un * 16) = cu;
          *(u32x4*)(base + 1 * CP_STRIDE + un * 16) = o1;
          *(u32x4*)(base + 2 * CP_STRIDE + un * 16) = o2;
          *(u32x4*)(base + 3 * CP_STRIDE + un * 16) = o3;
        }
      }
    }
    WAIT_V(0);
    pair_sync(smem, cw, 0);
    __builtin_amdgcn_s_setprio(LPRIO);
    TS(3);
    f32x4 y[8];
#pragma unroll
    for (int a = 0; a < 8; ++a) y[a] = (f32x4){0, 0, 0, 0};
    {
      const int q = n >> 2, pp = lane & 3;
      const char* abase = base + q * CP_STRIDE + (16 - 8 * g + 4 * pp) * 2 + 32 * (8 * half + 1);
      const char* bbase = base + CW_U + 512 + (16 * n + 8 * g) * 2 - 32 * (8 * half + 1);
      conv_loop(abase, bbase, y);
    }
    TS(4);
    __builtin_amdgcn_s_setprio(3);
#pragma unroll
    for (int a = 0; a < 4; ++a) *(f32x4*)(smem + K1_XCH + (cw * 4 + a) * 1024 + lane * 16) = half ? y[a] : y[4 + a];
    pair_sync(smem, cw, 1);
    TS(8);
#pragma unroll
    for (int a = 0; a < 4; ++a) {
      const f32x4 o = *(const f32x4*)(smem + K1_XCH + ((cw ^ 1) * 4 + a) * 1024 + lane * 16);
      const f32x4 v = half ? o + y[4 + a] : y[a] + o;
      const int ntile = 4 * half + a;
      uint32_t v0 = pk2(v[0], v[1]), v1 = pk2(v[2], v[3]);
      {
        const u32x2 uu = *(const u32x2*)(base + CW_U + 512 + (256 * ntile + 16 * n + 4 * g) * 2);
        const uint32_t s0 = pk2(bflo(uu[0]) * dsk, bfhi(uu[0]) * dsk), s1 = pk2(bflo(uu[1]) * dsk, bfhi(uu[1]) * dsk);
        v0 = pk2(bflo(v0) + bflo(s0), bfhi(v0) + bfhi(s0));
        v1 = pk2(bflo(v1) + bflo(s1), bfhi(v1) + bfhi(s1));
      }
      v0 = v0 == ~0u ? 0xfffffffeu : v0;
      v1 = v1 == ~0u ? 0xfffffffeu : v1;
      __hip_atomic_store((gu64*)(p.yT + (size_t)d * L + 256 * ntile + 16 * n + 4 * g), (unsigned long long)v0 | ((unsigned long long)v1 << 32), RLX_AGENT);
    }
    __builtin_amdgcn_s_setprio(0);
    TS(5);
    {
      const int dd = 16 * cw + (lane >> 2), ch = lane & 3;
      const uint32_t off = (uint32_t)((d0 + dd) * L + t0 + ch * 8) * 2u;
      u32x4 v0, v1;
      for (unsigned spins = 0; spins < (1u << 22); ++spins) {
        uint32_t x;
        asm volatile("ds_read_b32 %0, %1\n\ts_waitcnt lgkmcnt(0)" : "=v"(x) : "v"(lds_addr(smem + K1_FLAG + 32 + cw * 4)) : "memory");
        if (__builtin_amdgcn_readfirstlane(x) == 1u) break;
        __builtin_amdgcn_s_sleep(2);
      }
      for (unsigned spins = 0; spins < (1u << 20); ++spins) {
        asm volatile("global_load_dwordx4 %0, %2, %3 sc1\n\tglobal_load_dwordx4 %1, %2, %3 offset:64 sc1\n\ts_waitcnt vmcnt(0)"
                     : "=&v"(v0), "=&v"(v1) : "v"(off), "s"(p.yT) : "memory");
        const bool ok = v0[0] != ~0u && v0[1] != ~0u && v0[2] != ~0u && v0[3] != ~0u && v1[0] != ~0u && v1[1] != ~0u && v1[2] != ~0u && v1[3] != ~0u;
        if (__all(ok)) break;
        __builtin_amdgcn_s_sleep(4);
      }
      *(u32x4*)(smem + K1_YOFF + dd * 144 + ch * 16) = v0;
      *(u32x4*)(smem + K1_YOFF + dd * 144 + ch * 16 + 64) = v1;
    }
    TS(6);
    WAIT_L(0);
    BAR();
  }
  TSDUMP(1);
}

extern "C" void kernel_launch(void* const* d_in, const int* in_sizes, int n_in, void* d_out, int out_size, void* d_ws, size_t ws_size,
                              hipStream_t stream) {
  char* ws = (char*)d_ws;
  P0 p0;
  p0.u = (const u16*)d_in[0]; p0.Are = (const u16*)d_in[1]; p0.Aim = (const u16*)d_in[2]; p0.Bre = (const u16*)d_in[3];
  p0.Bim = (const u16*)d_in[4]; p0.Cre = (const u16*)d_in[5]; p0.Cim = (const u16*)d_in[6]; p0.logdt = (const u16*)d_in[7];
  p0.wg = (const u16*)d_in[10]; p0.wu = (const u16*)d_in[11]; p0.wd = (const u16*)d_in[12]; p0.gw = (const u16*)d_in[13];
  p0.h = (u16*)(ws + WS_H); p0.wdt = (u16*)(ws + WS_WDT); p0.uT = (u16*)(ws + WS_UT); p0.E = (u16*)(ws + WS_E);
  p0.yT = (u16*)(ws + WS_YT); p0.g = (float*)(ws + WS_G);
  p0.Dskip = (const u16*)d_in[8]; p0.out = (const u16*)d_out;
  P1 p1;
  p1.u = p0.u; p1.Dskip = (const u16*)d_in[8]; p1.h = p0.h; p1.wdt = p0.wdt; p1.uT = p0.uT; p1.E = p0.E; p1.g = p0.g;
  p1.yT = p0.yT; p1.out = (u16*)d_out;
  (void)hipFuncSetAttribute((const void*)k0_kernel, hipFuncAttributeMaxDynamicSharedMemorySize, K0_LDS);
  (void)hipFuncSetAttribute((const void*)k1_kernel, hipFuncAttributeMaxDynamicSharedMemorySize, K1_LDS);
  k0_kernel<<<256, 768, K0_LDS, stream>>>(p0);
  k1_kernel<<<256, 512, K1_LDS, stream>>>(p1);
}
```

```cpp
#include <hip/hip_runtime.h>
#include <hip/hip_bf16.h>
#include <stdint.h>

typedef unsigned short u16;
typedef __attribute__((ext_vector_type(4))) short s16x4;
typedef __attribute__((ext_vector_type(8))) short s16x8;
typedef __attribute__((ext_vector_type(8))) __bf16 bf16x8;
typedef __attribute__((ext_vector_type(2))) __bf16 bf16x2;
typedef __attribute__((ext_vector_type(2))) float f32x2;
typedef __attribute__((ext_vector_type(4))) float f32x4;
typedef __attribute__((ext_vector_type(4))) unsigned u32x4;
typedef __attribute__((ext_vector_type(2))) unsigned u32x2;
typedef __attribute__((address_space(3))) s16x4* lds_s16x4_ptr;
typedef __attribute__((address_space(1))) unsigned gu32;
typedef __attribute__((address_space(1))) unsigned long long gu64;
#define RLX_AGENT __ATOMIC_RELAXED, __HIP_MEMORY_SCOPE_AGENT
#define DEV __device__ __forceinline__

constexpr int L = 2048, D = 512, NM = 128, F = 1792;
constexpr int KS2 = F / 32;

constexpr size_t WS_H = 0;
constexpr size_t WS_WDT = WS_H + (size_t)L * F * 2;
constexpr size_t WS_UT = WS_WDT + (size_t)F * D * 2;
constexpr size_t WS_YT = WS_UT + (size_t)L * D * 2;
constexpr int ES = 2080;
constexpr size_t WS_E = WS_YT + (size_t)L * D * 2 + 128;
constexpr size_t WS_G = WS_E + (size_t)D * ES * 2;

#define TSDECL
#define TS(ph)
#define TSDUMP(K)
#define TS_ARG
#define TS_PASS
DEV float bf2f(u16 v) { return __uint_as_float(((uint32_t)v) << 16); }
DEV float bflo(uint32_t v) { return __uint_as_float(v << 16); }
DEV float bfhi(uint32_t v) { return __uint_as_float(v & 0xffff0000u); }
DEV uint32_t pk2(float a, float b) { f32x2 v = {a, b}; return __builtin_bit_cast(uint32_t, __builtin_convertvector(v, bf16x2)); }
DEV float rbf(float a) { return bflo(pk2(a, 0.f) & 0xffffu); }
DEV f32x4 mfma16(u32x4 a, u32x4 b, f32x4 c) {
  return __builtin_amdgcn_mfma_f32_16x16x32_bf16(__builtin_bit_cast(bf16x8, a), __builtin_bit_cast(bf16x8, b), c, 0, 0, 0);
}
DEV u32x2 tr_read(const char* p) {
  return __builtin_bit_cast(u32x2, __builtin_amdgcn_ds_read_tr16_b64_v4i16((lds_s16x4_ptr)p));
}
DEV u32x4 tr_read2(const char* p0, const char* p1) {
  u32x2 a = tr_read(p0), b = tr_read(p1);
  u32x4 r = {a.x, a.y, b.x, b.y};
  return r;
}
DEV uint32_t lds_addr(const void* p) { return (uint32_t)(uintptr_t)(__attribute__((address_space(3))) const char*)p; }
DEV void glds16(const void* sbase, uint32_t voff, uint32_t lds) {
  unsigned keep;
  asm volatile("s_mov_b32 %0, m0\n\ts_mov_b32 m0, %3\n\ts_nop 0\n\tglobal_load_lds_dwordx4 %1, %2\n\ts_mov_b32 m0, %0"
               : "=&s"(keep) : "v"(voff), "s"(sbase), "s"(lds) : "memory");
}
#define WAIT_V(n) asm volatile("s_waitcnt vmcnt(" #n ")" ::: "memory")
#define WAIT_L(n) asm volatile("s_waitcnt lgkmcnt(" #n ")" ::: "memory")
#define BAR() __builtin_amdgcn_s_barrier()

template <bool WT>
DEV void st16(void* base, size_t byte_off, u32x4 v) {
  if (WT) {
    __builtin_amdgcn_raw_buffer_store_b128(v, __builtin_amdgcn_make_buffer_rsrc(base, 0, 0x7fffffff, 0x00020000), (uint32_t)byte_off, 0, 16);
  } else {
    *(u32x4*)((char*)base + byte_off) = v;
  }
}
#define WT_AUX 1
#define WT_H 1

struct P0 {
  const u16 *u, *Are, *Aim, *Bre, *Bim, *Cre, *Cim, *logdt, *wg, *wu, *wd, *gw, *Dskip;
  u16 *h, *wdt, *uT, *E, *yT;
  float* g;
};

constexpr int STAGE_B = 49152;
constexpr int K0_LDS = 3 * STAGE_B + 15360;

DEV int wswz(int row) { return ((row & 3) << 2) | ((row >> 2) & 3); }

struct SsmPar { u32x4 are, aim, bre, bim, cre, cim; };

DEV void ssm_load(const P0& p, uint32_t off, SsmPar& s) {
  asm volatile("global_load_dwordx4 %0, %1, %2" : "=&v"(s.are) : "v"(off), "s"(p.Are) : "memory");
  asm volatile("global_load_dwordx4 %0, %1, %2" : "=&v"(s.aim) : "v"(off), "s"(p.Aim) : "memory");
  asm volatile("global_load_dwordx4 %0, %1, %2" : "=&v"(s.bre) : "v"(off), "s"(p.Bre) : "memory");
  asm volatile("global_load_dwordx4 %0, %1, %2" : "=&v"(s.bim) : "v"(off), "s"(p.Bim) : "memory");
  asm volatile("global_load_dwordx4 %0, %1, %2" : "=&v"(s.cre) : "v"(off), "s"(p.Cre) : "memory");
  asm volatile("global_load_dwordx4 %0, %1, %2" : "=&v"(s.cim) : "v"(off), "s"(p.Cim) : "memory");
}
#define SSM_WAIT(n, s) SSM_WAIT_(n, s)
#define SSM_WAIT_(n, s) asm volatile("s_waitcnt vmcnt(" #n ")" : "+v"(s.are), "+v"(s.aim), "+v"(s.bre), "+v"(s.bim), "+v"(s.cre), "+v"(s.cim)::"memory")

DEV void pair_sync_at(uint32_t mine) {
  const uint32_t other = mine ^ 8u;
  asm volatile("s_waitcnt lgkmcnt(0)\n\tds_write_b32 %0, %1" ::"v"(mine), "v"(1u) : "memory");
  for (unsigned spins = 0; spins < (1u << 24); ++spins) {
    uint32_t x;
    asm volatile("ds_read_b32 %0, %1\n\ts_waitcnt lgkmcnt(0)" : "=v"(x) : "v"(other) : "memory");
    if (__builtin_amdgcn_readfirstlane(x) == 1u) break;
  }
}

struct KgFrag { u32x4 ah[3], al[3], bh, bl; };
template <int H>
DEV void kgen_half(const SsmPar& sp, float dt, float p0, float p1, float p2, float q, KgFrag& f, f32x4 (&acc)[3]) {
  constexpr int W = H & 3, JH = H & 1;
  uint32_t wa = sp.are[W], wi = sp.aim[W], wbr = sp.bre[W], wbi = sp.bim[W], wcr = sp.cre[W], wci = sp.cim[W];
  asm volatile("" : "+v"(wa), "+v"(wi), "+v"(wbr), "+v"(wbi), "+v"(wcr), "+v"(wci));
#pragma unroll
  for (int jj = 0; jj < 2; ++jj) {
    const int j = 2 * JH + jj;
    const float ar = dt * (jj ? bfhi(wa) : bflo(wa)), ai = dt * (jj ? bfhi(wi) : bflo(wi));
    const float br = dt * (jj ? bfhi(wbr) : bflo(wbr)), bi = dt * (jj ? bfhi(wbi) : bflo(wbi));
    const float cr = jj ? bfhi(wcr) : bflo(wcr), ci = jj ? bfhi(wci) : bflo(wci);
    const float a2 = ar * 1.44269504088896340736f, wv = ai * 0.15915494309189533577f;
    const float xr = cr * br - ci * bi, xi = cr * bi + ci * br;
    {
      const float e = __builtin_amdgcn_exp2f(a2 * q), r = __builtin_amdgcn_fractf(wv * q);
      const float s = __builtin_amdgcn_sinf(r) * e, co = __builtin_amdgcn_cosf(r) * e;
      const float vr = xr * co - xi * s, vi = -(xr * s + xi * co);
      const uint32_t hi = pk2(vr, vi);
      f.bh[j] = hi;
      f.bl[j] = pk2(vr - bflo(hi), vi - bfhi(hi));
    }
#pragma unroll
    for (int T = 0; T < 3; ++T) {
      const float pp = T == 0 ? p0 : (T == 1 ? p1 : p2);
      const float e = __builtin_amdgcn_exp2f(a2 * pp), r = __builtin_amdgcn_fractf(wv * pp);
      const float wr = __builtin_amdgcn_cosf(r) * e, wim = __builtin_amdgcn_sinf(r) * e;
      const uint32_t hi = pk2(wr, wim);
      f.ah[T][j] = hi;
      f.al[T][j] = pk2(wr - bflo(hi), wim - bfhi(hi));
    }
  }
  if (JH == 1) {
#pragma unroll
    for (int T = 0; T < 3; ++T) {
      acc[T] = mfma16(f.al[T], f.bh, acc[T]);
      acc[T] = mfma16(f.ah[T], f.bl, acc[T]);
      acc[T] = mfma16(f.ah[T], f.bh, acc[T]);
    }
    asm volatile("" : "+v"(acc[0]), "+v"(acc[1]), "+v"(acc[2]));
  } else {
    asm volatile("" : "+v"(f.ah[0]), "+v"(f.ah[1]), "+v"(f.ah[2]), "+v"(f.al[0]), "+v"(f.al[1]), "+v"(f.al[2]), "+v"(f.bh), "+v"(f.bl));
  }
  __builtin_amdgcn_sched_barrier(0);
}

#define KTS(i)
DEV void kgen_waves(const P0& p, char* scratch, int b, bool aux, int tid TS_ARG) {
  const int t = tid - 512, kw = __builtin_amdgcn_readfirstlane(t >> 6), lane = t & 63, i = lane & 15, ch0 = 2 * b, cc = kw >> 1;
#define KPRIO 3
#define KPRIO2 KPRIO
#define NPRE 4
  __builtin_amdgcn_s_setprio(KPRIO);
  SsmPar sp0, sp1;
  uint32_t ld2, ds2;
  asm volatile("global_load_dword %0, %2, %3\n\tglobal_load_dword %1, %2, %4"
               : "=&v"(ld2), "=&v"(ds2) : "v"((uint32_t)b * 4u), "s"(p.logdt), "s"(p.Dskip) : "memory");
  const uint32_t off = (uint32_t)((ch0 + cc) * NM + 64 * (kw & 1) + 8 * (lane >> 4)) * 2u;
  ssm_load(p, off, sp0);
  ssm_load(p, off + 64u, sp1);
  __builtin_amdgcn_sched_barrier(0);
  BAR();
  __builtin_amdgcn_sched_barrier(0);
  SSM_WAIT(6, sp0);
  asm volatile("" : "+v"(ld2), "+v"(ds2));
  const float dt = rbf(__expf(cc ? bfhi(ld2) : bflo(ld2)));
  const float p0 = (float)((i & 7) + 128 * (i >> 3));
  const float p1 = (i < 8) ? (float)(256 + 2 * (i & 3) + 128 * (i >> 2)) : (float)(512 + 4 * (i & 1) + 128 * ((i - 8) >> 1));
  const float p2 = (i < 8) ? (float)(1024 + 128 * i) : 2048.f;
  const float q = (float)(8 * i);
  f32x4 acc[3] = {{0, 0, 0, 0}, {0, 0, 0, 0}, {0, 0, 0, 0}};
  KgFrag f;
#pragma unroll
  for (int T = 0; T < 3; ++T) f.ah[T] = f.al[T] = (u32x4){0, 0, 0, 0};
  f.bh = f.bl = (u32x4){0, 0, 0, 0};
  TS(1);
  if (0 == NPRE || (0 > NPRE && !aux)) BAR();
  kgen_half<0>(sp0, dt, p0, p1, p2, q, f, acc);
  KTS(3);
  if (1 == NPRE || (1 > NPRE && !aux)) BAR();
  kgen_half<1>(sp0, dt, p0, p1, p2, q, f, acc);
  KTS(4);
  if (2 == NPRE || (2 > NPRE && !aux)) BAR();
  kgen_half<2>(sp0, dt, p0, p1, p2, q, f, acc);
  KTS(5);
  if (3 == NPRE || (3 > NPRE && !aux)) BAR();
  kgen_half<3>(sp0, dt, p0, p1, p2, q, f, acc);
  KTS(6);
  if (4 == NPRE || (4 > NPRE && !aux)) BAR();
  SSM_WAIT(0, sp1);
  kgen_half<4>(sp1, dt, p0, p1, p2, q, f, acc);
  KTS(7);
  if (5 == NPRE || (5 > NPRE && !aux)) BAR();
  kgen_half<5>(sp1, dt, p0, p1, p2, q, f, acc);
  KTS(8);
  if (6 == NPRE || (6 > NPRE && !aux)) BAR();
  kgen_half<6>(sp1, dt, p0, p1, p2, q, f, acc);
  KTS(9);
  if (7 == NPRE || (7 > NPRE && !aux)) BAR();
  kgen_half<7>(sp1, dt, p0, p1, p2, q, f, acc);
  float* sc = (float*)scratch;
  u16* kc = (u16*)(scratch + 12288) + cc * 768;
#pragma unroll
  for (int T = 0; T < 3; ++T) *(f32x4*)(sc + ((kw * 3 + T) * 64 + lane) * 4) = acc[T];
  WAIT_L(0);
  TS(15);
  BAR();
  const int tt = t & 127;
#pragma unroll
  for (int T = 0; T < 3; ++T) {
    if (T == 2 && !aux) BAR();
#pragma unroll
    for (int hf = 0; hf < 2; ++hf) {
      const int e = hf * 128 + tt, ln = e >> 2, j = e & 3;
      const int ii = 4 * (ln >> 4) + j, bb = ln & 15;
      const float s = sc[((cc * 2) * 3 + T) * 256 + e] + sc[((cc * 2 + 1) * 3 + T) * 256 + e];
      int idx;
      if (T == 0) idx = (ii & 7) + 128 * (ii >> 3) + 8 * bb;
      else if (T == 1) idx = ii < 8 ? 256 + (ii & 3) + 64 * (ii >> 2) + 4 * bb : 384 + (ii & 1) + 32 * ((ii - 8) >> 1) + 2 * bb;
      else idx = ii < 8 ? 512 + 16 * ii + bb : ((ii == 8 && bb == 0) ? 640 : 700);
      kc[idx] = (u16)(pk2(s, 0.f) & 0xffffu);
    }
    __builtin_amdgcn_sched_barrier(0);
  }
  WAIT_L(0);
  BAR();
  if ((t & 127) == 0) {
    const uint32_t dv = cc ? ds2 >> 16 : ds2 & 0xffffu;
    __builtin_amdgcn_raw_buffer_store_b32(dv, __builtin_amdgcn_make_buffer_rsrc(p.E, 0, 0x7fffffff, 0x00020000), (uint32_t)((ch0 + cc) * ES * 2), 0, 16);
  }
#pragma unroll
  for (int hf = 0; hf < 2; ++hf) {
    if (hf == 1 && !aux) BAR();
    int l0 = 1024 * hf + 8 * tt;
    asm volatile("" : "+v"(l0));
    u32x4 o;
#pragma unroll
    for (int e = 0; e < 4; ++e) {
      const int la = l0 + 2 * e, lb = la + 1;
      const uint32_t pb = pk2((float)la, (float)lb);
      const int ia = la < 128 ? la : (int)(pb & 0xffffu) - 17024, ib = lb < 128 ? lb : (int)(pb >> 16) - 17024;
      o[e] = (uint32_t)kc[ia] | ((uint32_t)kc[ib] << 16);
    }
    st16<WT_AUX>(p.E, ((size_t)(ch0 + cc) * ES + 16 + l0) * 2, o);
    __builtin_amdgcn_sched_barrier(0);
  }
  st16<WT_AUX>(p.yT, ((size_t)b * 4096 + t * 8) * 2, (u32x4){~0u, ~0u, ~0u, ~0u});
  st16<WT_AUX>(p.yT, ((size_t)b * 4096 + 2048 + t * 8) * 2, (u32x4){~0u, ~0u, ~0u, ~0u});
  TS(14);
  if (!aux)
    for (int i_ = 0; i_ < NPRE - 4; ++i_) BAR();
}

DEV void k0_glds(const P0& p, uint32_t lb, const uint32_t (&voff)[3], int m0, int f0, int k0, int piece) {
  if (piece < 2) glds16(p.u + (size_t)m0 * D + k0, voff[0] + piece * (8 * D * 2), lb + piece * 1024);
  else if (piece < 4) glds16(p.wg + (size_t)k0 * F + f0, voff[piece - 1], lb + 16384 + (piece - 2) * 1024);
  else glds16(p.wu + (size_t)k0 * F + f0, voff[piece - 3], lb + 32768 + (piece - 4) * 1024);
}

__global__ __launch_bounds__(768, 3) void k0_kernel(P0 p) {
  extern __shared__ __attribute__((aligned(16))) char smem[];
  const int tid = threadIdx.x, lane = tid & 63, w = __builtin_amdgcn_readfirstlane(tid >> 6);
  const int b = blockIdx.x, xcd = b & 7, idx = b >> 3;
  TSDECL;
  TS(0);
  if (w >= 8) {
    kgen_waves(p, smem + 3 * STAGE_B, b, idx >= 28, tid TS_PASS);
  } else if (idx < 28) {
    const int mt = (xcd >> 1) * 4 + idx / 7, ft = (xcd & 1) * 7 + idx % 7;
    const int m0 = mt * 128, f0 = ft * 128;
#define K0ROT 2
    const int rot = K0ROT == 0 ? 2 * (idx / 7) + idx % 7 : K0ROT == 1 ? 0 : K0ROT == 2 ? (idx & 1) * 4 : K0ROT == 3 ? 2 * (idx / 7) : idx % 7;
    uint32_t voff[3];
    {
      const int row = 16 * w + (lane >> 3);
      voff[0] = (uint32_t)(row * D + 8 * ((lane & 7) ^ (row & 7))) * 2u;
      const int r0 = 8 * w + (lane >> 4), r1 = r0 + 4;
      voff[1] = (uint32_t)(r0 * F + 8 * ((lane & 15) ^ wswz(r0))) * 2u;
      voff[2] = (uint32_t)(r1 * F + 8 * ((lane & 15) ^ wswz(r1))) * 2u;
    }
    const uint32_t lb = lds_addr(smem) + 2048 * w;
#pragma unroll
    for (int s = 0; s < 3; ++s) {
      if (s == 1) BAR();
#pragma unroll
      for (int pc = 0; pc < 6; ++pc) k0_glds(p, lb + s * STAGE_B, voff, m0, f0, 64 * ((rot + s) & 7), pc);
    }
    const int wm = w >> 2, wn = w & 3, g = lane >> 4, q = (lane & 15) >> 2, pp = lane & 3;
    f32x4 acc[2][2][4];
#pragma unroll
    for (int a = 0; a < 2; ++a)
#pragma unroll
      for (int c = 0; c < 2; ++c)
#pragma unroll
        for (int m = 0; m < 4; ++m) acc[a][c][m] = (f32x4){0, 0, 0, 0};
    u32x4 xf[2][4], wf[2][2][2];
    auto frags = [&](const char* buf, int ss) {
#pragma unroll
      for (int m = 0; m < 4; ++m) {
        const int row = 64 * wm + 16 * m + (lane & 15), c = 4 * ss + g;
        xf[ss][m] = *(const u32x4*)(buf + row * 128 + ((c ^ (row & 7)) << 4));
      }
#pragma unroll
      for (int a = 0; a < 2; ++a)
#pragma unroll
        for (int c = 0; c < 2; ++c) {
          const int ch = 4 * wn + 2 * c + (pp >> 1);
          const int r0 = 32 * ss + 8 * g + q, r1 = r0 + 4;
          const char* wb = buf + 16384 + a * 16384 + 8 * (pp & 1);
          wf[ss][a][c] = tr_read2(wb + 256 * r0 + 16 * (ch ^ wswz(r0)), wb + 256 * r1 + 16 * (ch ^ wswz(r1)));
        }
    };
    auto mm = [&](int ss, bool stage, int sb, int k0, int pc0) {
#pragma unroll
      for (int ac = 0; ac < 4; ++ac) {
#pragma unroll
        for (int m = 0; m < 4; ++m) acc[ac >> 1][ac & 1][m] = mfma16(wf[ss][ac >> 1][ac & 1], xf[ss][m], acc[ac >> 1][ac & 1][m]);
        if (stage && ac < 3) {
          __builtin_amdgcn_sched_barrier(0);
          k0_glds(p, lb + sb * STAGE_B, voff, m0, f0, k0, pc0 + ac);
          __builtin_amdgcn_sched_barrier(0);
        }
      }
    };
#pragma unroll
    for (int s = 0; s < 8; ++s) {
      if (s == 7) WAIT_V(0); else if (s == 0) WAIT_V(12); else WAIT_V(6);
      WAIT_L(0);
      BAR();
      TS(3 + s);
      int bo = (s % 3) * STAGE_B;
      asm volatile("" : "+s"(bo));
      const char* buf = smem + bo;
      const bool st = (s >= 1 && s + 2 < 8);
      const int k0 = 64 * ((rot + s + 2) & 7);
      frags(buf, 0);
      __builtin_amdgcn_sched_barrier(0);
      if (s > 0) mm(1, st, (s + 2) % 3, k0, 0);
      __builtin_amdgcn_sched_barrier(0);
      frags(buf, 1);
      __builtin_amdgcn_sched_barrier(0);
      mm(0, st, (s + 2) % 3, k0, 3);
      __builtin_amdgcn_sched_barrier(0);
    }
    mm(1, false, 0, 0, 0);
    TS(11);
#pragma unroll
    for (int m = 0; m < 4; ++m) {
      u32x4 o;
#pragma unroll
      for (int c = 0; c < 2; ++c)
#pragma unroll
        for (int jj = 0; jj < 2; ++jj) {
          const uint32_t g2 = pk2(acc[0][c][m][2 * jj], acc[0][c][m][2 * jj + 1]), u2 = pk2(acc[1][c][m][2 * jj], acc[1][c][m][2 * jj + 1]);
          const f32x2 gg = {bflo(g2), bfhi(g2)};
          const f32x2 tt = gg * -1.44269504088896340736f;
          const f32x2 dn = (f32x2){__builtin_amdgcn_exp2f(tt[0]), __builtin_amdgcn_exp2f(tt[1])} + 1.f;
          const f32x2 sv = gg * (f32x2){__builtin_amdgcn_rcpf(dn[0]), __builtin_amdgcn_rcpf(dn[1])};
          const uint32_t s2 = pk2(sv[0], sv[1]);
          const f32x2 hv = (f32x2){bflo(s2), bfhi(s2)} * (f32x2){bflo(u2), bfhi(u2)};
          o[2 * c + jj] = pk2(hv[0], hv[1]);
        }
      const int mt64 = (m0 >> 6) + wm, ks = 4 * ft + wn;
      st16<WT_H>(p.h, ((size_t)((mt64 * KS2 + ks) * 4 + m) * 64 + lane) * 16, o);
    }
    TS(12);
  } else {
    BAR();
    const int a = xcd * 4 + (idx - 28);
    constexpr int RS = 1056;
    const int t0 = 64 * a;
    u32x4 uv[8], wv[8];
#pragma unroll
    for (int r = 0; r < 8; ++r) uv[r] = *(const u32x4*)(p.u + (size_t)(t0 + 8 * w + r) * D + lane * 8);
    const int ks2 = a + 32 < KS2 ? a + 32 : a;
#pragma unroll
    for (int r = 0; r < 4; ++r) {
      wv[r] = *(const u32x4*)(p.wd + (size_t)(32 * a + 4 * w + r) * D + lane * 8);
      wv[4 + r] = *(const u32x4*)(p.wd + (size_t)(32 * ks2 + 4 * w + r) * D + lane * 8);
    }
    const u32x4 gwv = *(const u32x4*)(p.gw + lane * 8);
    {
      float s[8];
#pragma unroll
      for (int r = 0; r < 8; ++r) {
        *(u32x4*)(smem + (8 * w + r) * RS + lane * 16) = uv[r];
        s[r] = 0.f;
#pragma unroll
        for (int e = 0; e < 4; ++e) {
          const uint32_t pr = pk2(bflo(uv[r][e]) * bflo(gwv[e]), bfhi(uv[r][e]) * bfhi(gwv[e]));
          s[r] += bflo(pr) + bfhi(pr);
        }
      }
#pragma unroll
      for (int r = 0; r < 4; ++r) { const bool hi = lane & 32; const float keep = hi ? s[r + 4] : s[r], send = hi ? s[r] : s[r + 4]; s[r] = keep + __shfl_xor(send, 32); }
#pragma unroll
      for (int r = 0; r < 2; ++r) { const bool hi = lane & 16; const float keep = hi ? s[r + 2] : s[r], send = hi ? s[r] : s[r + 2]; s[r] = keep + __shfl_xor(send, 16); }
      { const bool hi = lane & 8; const float keep = hi ? s[1] : s[0], send = hi ? s[0] : s[1]; s[0] = keep + __shfl_xor(send, 8); }
      s[0] += __shfl_xor(s[0], 4); s[0] += __shfl_xor(s[0], 2); s[0] += __shfl_xor(s[0], 1);
      if ((lane & 7) == 0) {
        const int r = ((lane >> 5) & 1) * 4 + ((lane >> 4) & 1) * 2 + ((lane >> 3) & 1);
        const float gs = rbf(s[0]);
        p.g[t0 + 8 * w + r] = rbf(1.f / (1.f + __expf(-gs)));
      }
    }
    WAIT_L(0);
    BAR();
    const int g = lane >> 4, q = (lane & 15) >> 2, pp = lane & 3;
#pragma unroll
    for (int c = 0; c < 4; ++c)
#pragma unroll
      for (int hh = 0; hh < 2; ++hh) {
        const int dt_ = 4 * w + c;
        const int r0 = 32 * hh + 8 * g + q;
        const char* base = smem + (16 * dt_ + 4 * pp) * 2;
        const u32x4 v = tr_read2(base + r0 * RS, base + (r0 + 4) * RS);
        st16<WT_AUX>(p.uT, ((size_t)(16 * dt_ + (lane & 15)) * L + t0 + 32 * hh + 8 * g) * 2, v);
      }
    TS(3);
    WAIT_L(0);
    BAR();
#pragma unroll
    for (int r = 0; r < 4; ++r) {
      *(u32x4*)(smem + (4 * w + r) * RS + lane * 16) = wv[r];
      *(u32x4*)(smem + (32 + 4 * w + r) * RS + lane * 16) = wv[4 + r];
    }
    WAIT_L(0);
    BAR();
#pragma unroll
    for (int hh = 0; hh < 2; ++hh) {
      if (hh == 0 || a + 32 < KS2) {
        const int ks = a + 32 * hh;
#pragma unroll
        for (int c = 0; c < 4; ++c) {
          const int nt = 4 * w + c;
          const char* base = smem + 32 * hh * RS + (16 * nt + 4 * pp) * 2;
          const u32x4 v = tr_read2(base + (4 * g + q) * RS, base + (16 + 4 * g + q) * RS);
          st16<WT_AUX>(p.wdt, ((size_t)(((nt >> 2) * KS2 + ks) * 4 + (nt & 3)) * 64 + lane) * 16, v);
        }
      }
    }
    TS(12);
  }
  TSDUMP(0);
}

#define GO_STEP 7
#define FPRIO 1
#define LPRIO 0
struct P1 {
  const u16 *u, *Dskip, *h, *wdt, *uT, *E;
  const float* g;
  u16 *yT, *out;
};

constexpr int CP_STRIDE = 4160;
constexpr int CW_U = 4 * CP_STRIDE, CW_SIZE = CW_U + 4608;
constexpr int K1_XCH = 2 * CW_SIZE;
constexpr int K1_YOFF = K1_XCH + 16384;
constexpr int K1_RED = K1_YOFF + 64 * 144;
constexpr int K1_FLAG = K1_RED + 49152;
constexpr int K1_LDS = K1_FLAG + 64;

DEV void conv_loop(const char* abase, const char* bbase, f32x4 (&y)[8]) {
  u32x4 af[2][8], bf[2][8];
  auto ld = [&](int buf, int it) {
#pragma unroll
    for (int m = 0; m < 8; ++m) bf[buf][m] = *(const u32x4*)(bbase + 512 * m - 64 * it);
#pragma unroll
    for (int m = 0; m < 8; ++m) {
      const char* ap = abase + 512 * m + 64 * it;
      af[buf][m] = tr_read2(ap, ap - 8);
    }
  };
  ld(0, 0);
#pragma unroll
  for (int it = 0; it < 4; ++it) {
    const int cur = it & 1;
    __builtin_amdgcn_sched_barrier(0);
    if (it < 3) ld(cur ^ 1, it + 1);
#pragma unroll
    for (int m = 0; m < 8; ++m)
#pragma unroll
      for (int T = m; T < 8; ++T) y[T] = mfma16(af[cur][m], bf[cur][T - m], y[T]);
    if (it < 3) {
#pragma unroll
      for (int i = 0; i < 12; ++i) {
        __builtin_amdgcn_sched_group_barrier(0x008, 3, 0);
        __builtin_amdgcn_sched_group_barrier(0x100, 2, 0);
      }
    }
  }
  __builtin_amdgcn_sched_barrier(0);
}
DEV void pair_sync(char* smem, int cw, int which) { pair_sync_at(lds_addr(smem + K1_FLAG + cw * 8 + which * 4)); }

__global__ __launch_bounds__(512, 2) void k1_kernel(P1 p) {
  extern __shared__ __attribute__((aligned(16))) char smem[];
  const int tid = threadIdx.x, lane = tid & 63, w = __builtin_amdgcn_readfirstlane(tid >> 6);
  const int b = blockIdx.x, xcd = b & 7, idx = b >> 3;
  const int mt = (xcd >> 1) * 8 + (idx >> 2), nt = (xcd & 1) * 4 + (idx & 3);
  const int t0 = mt * 64, d0 = nt * 64;
  const int g = lane >> 4, n = lane & 15;
  TSDECL;
  TS(0);
  if (w >= 4) __builtin_amdgcn_s_setprio(3);
  if (w == 4) {
    asm volatile("ds_write_b32 %0, %1\n\ts_waitcnt lgkmcnt(0)" ::"v"(lds_addr(smem + K1_FLAG + (lane & 15) * 4)), "v"(0u) : "memory");
  }
  BAR();
  __builtin_amdgcn_sched_barrier(0);
  if (w < 4) {
    __builtin_amdgcn_s_setprio(FPRIO);
    f32x4 acc[4][4];
#pragma unroll
    for (int a = 0; a < 4; ++a)
#pragma unroll
      for (int c = 0; c < 4; ++c) acc[a][c] = (f32x4){0, 0, 0, 0};
    const u16* hp = p.h + ((size_t)mt * KS2 + 14 * w) * 2048 + lane * 8;
    const u16* wp = p.wdt + ((size_t)nt * KS2 + 14 * w) * 2048 + lane * 8;
#define PFD 4
    constexpr int PF = PFD;
    u32x4 hf[PF][4], wf[PF][4];
    const int hrot = w * 512;
#pragma unroll
    for (int s = 0; s < PF; ++s)
#pragma unroll
      for (int a = 0; a < 4; ++a) {
        hf[s][a] = *(const u32x4*)(hp + s * 2048 + ((a * 512 + hrot) & 2047));
        wf[s][a] = *(const u32x4*)(wp + s * 2048 + a * 512);
      }
    const int t = t0 + 16 * w + n;
    float gt;
    __builtin_amdgcn_sched_barrier(0);
    TS(15);
    __builtin_amdgcn_sched_barrier(0);
#pragma unroll
    for (int s = 0; s < 14; ++s) {
#pragma unroll
      for (int a = 0; a < 4; ++a)
#pragma unroll
        for (int c = 0; c < 4; ++c) acc[a][c] = mfma16(wf[s % PF][a], hf[s % PF][c], acc[a][c]);
      __builtin_amdgcn_sched_barrier(0);
      if (s + PF < 14) {
#pragma unroll
        for (int a = 0; a < 4; ++a) {
          hf[s % PF][a] = *(const u32x4*)(hp + (s + PF) * 2048 + ((a * 512 + hrot) & 2047));
          wf[s % PF][a] = *(const u32x4*)(wp + (s + PF) * 2048 + a * 512);
        }
      }
      if (s == GO_STEP) asm volatile("ds_write_b32 %0, %1" ::"v"(lds_addr(smem + K1_FLAG + 32 + w * 4)), "v"(1u) : "memory");
      if (s + PF == 14) gt = p.g[t];
      __builtin_amdgcn_sched_barrier(0);
    }
    TS(1);
#pragma unroll
    for (int c = 1; c < 4; ++c)
#pragma unroll
      for (int a = 0; a < 4; ++a) *(f32x4*)(smem + K1_RED + ((((w + c) & 3) * 3 + (c - 1)) * 4 + a) * 1024 + lane * 16) = acc[a][c];
    WAIT_L(0);
    BAR();
    TS(7);
    const float omg = rbf(1.f - gt);
    const int q = n >> 2, pp = lane & 3;
#pragma unroll
    for (int a = 0; a < 4; ++a) {
      f32x4 bs = acc[a][0];
#pragma unroll
      for (int s = 0; s < 3; ++s) bs += *(const f32x4*)(smem + K1_RED + ((w * 3 + s) * 4 + a) * 1024 + lane * 16);
      const u32x2 yv = tr_read(smem + K1_YOFF + (16 * a + 4 * g + q) * 144 + (16 * w + 4 * pp) * 2);
      u32x2 o;
#pragma unroll
      for (int e = 0; e < 2; ++e) {
        const uint32_t ga = pk2(gt * bflo(yv[e]), gt * bfhi(yv[e]));
        const uint32_t bb = pk2(bs[2 * e], bs[2 * e + 1]);
        const uint32_t gb = pk2(omg * bflo(bb), omg * bfhi(bb));
        o[e] = pk2(bflo(ga) + bflo(gb), bfhi(ga) + bfhi(gb));
      }
      *(u32x2*)(p.out + (size_t)t * D + d0 + 16 * a + 4 * g) = o;
    }
    TS(9);
  } else {
    const int cw = w - 4, c = cw >> 1, half = cw & 1, d = 2 * b + c;
    char* base = smem + c * CW_SIZE;
#define CSLEEP 10
    if (CSLEEP) __builtin_amdgcn_s_sleep(CSLEEP);
    const float dsk = bflo(*(const uint32_t*)(p.E + (size_t)d * ES));
    {
      const u16* eg = p.E + (size_t)d * ES;
      u32x4 cur[3];
      u32x2 prv[3];
#pragma unroll
      for (int it = 0; it < 3; ++it) {
        const int un = it < 2 ? (2 * half + it) * 64 + lane : 256 + (lane & 1);
        asm volatile("global_load_dwordx4 %0, %2, %3\n\tglobal_load_dwordx2 %1, %2, %3 offset:-8"
                     : "=&v"(cur[it]), "=&v"(prv[it]) : "v"(un * 16u), "s"(eg) : "memory");
      }
      const uint32_t lb = lds_addr(base + CW_U + 512) + half * 2048;
#pragma unroll
      for (int i = 0; i < 2; ++i) glds16(p.uT + (size_t)d * L + (2 * half + i) * 512, lane * 16u, lb + i * 1024);
      if (lane < 32 && half == 0) *(u32x4*)(base + CW_U + lane * 16) = (u32x4){0, 0, 0, 0};
      asm volatile("s_waitcnt vmcnt(2)" : "+v"(cur[0]), "+v"(cur[1]), "+v"(cur[2]), "+v"(prv[0]), "+v"(prv[1]), "+v"(prv[2])::"memory");
      TS(2);
      if (half == 0) {
        if (lane < 2) cur[0] = (u32x4){0, 0, 0, 0};
        if (lane < 3) prv[0] = (u32x2){0, 0};
      }
#pragma unroll
      for (int it = 0; it < 3; ++it) {
        const int un = it < 2 ? (2 * half + it) * 64 + lane : 256 + (lane & 1);
        const u32x4 cu = cur[it];
        const u32x2 pv = prv[it];
        u32x4 o1, o2, o3;
        o1[0] = __builtin_amdgcn_alignbyte(cu[0], pv[1], 2); o1[1] = __builtin_amdgcn_alignbyte(cu[1], cu[0], 2);
        o1[2] = __builtin_amdgcn_alignbyte(cu[2], cu[1], 2); o1[3] = __builtin_amdgcn_alignbyte(cu[3], cu[2], 2);
        o2[0] = pv[1]; o2[1] = cu[0]; o2[2] = cu[1]; o2[3] = cu[2];
        o3[0] = __builtin_amdgcn_alignbyte(pv[1], pv[0], 2); o3[1] = o1[0]; o3[2] = o1[1]; o3[3] = o1[2];
        if (it < 2 || (lane < 2 && half == 1)) {
          *(u32x4*)(base + 0 * CP_STRIDE + un * 16) = cu;
          *(u32x4*)(base + 1 * CP_STRIDE + un * 16) = o1;
          *(u32x4*)(base + 2 * CP_STRIDE + un * 16) = o2;
          *(u32x4*)(base + 3 * CP_STRIDE + un * 16) = o3;
        }
      }
    }
    WAIT_V(0);
    pair_sync(smem, cw, 0);
    __builtin_amdgcn_s_setprio(LPRIO);
    TS(3);
    f32x4 y[8];
#pragma unroll
    for (int a = 0; a < 8; ++a) y[a] = (f32x4){0, 0, 0, 0};
    {
      const int q = n >> 2, pp = lane & 3;
      const char* abase = base + q * CP_STRIDE + (16 - 8 * g + 4 * pp) * 2 + 32 * (8 * half + 1);
      const char* bbase = base + CW_U + 512 + (16 * n + 8 * g) * 2 - 32 * (8 * half + 1);
      conv_loop(abase, bbase, y);
    }
    TS(4);
    __builtin_amdgcn_s_setprio(3);
#pragma unroll
    for (int a = 0; a < 4; ++a) *(f32x4*)(smem + K1_XCH + (cw * 4 + a) * 1024 + lane * 16) = half ? y[a] : y[4 + a];
    pair_sync(smem, cw, 1);
    TS(8);
#pragma unroll
    for (int a = 0; a < 4; ++a) {
      const f32x4 o = *(const f32x4*)(smem + K1_XCH + ((cw ^ 1) * 4 + a) * 1024 + lane * 16);
      const f32x4 v = half ? o + y[4 + a] : y[a] + o;
      const int ntile = 4 * half + a;
      uint32_t v0 = pk2(v[0], v[1]), v1 = pk2(v[2], v[3]);
      {
        const u32x2 uu = *(const u32x2*)(base + CW_U + 512 + (256 * ntile + 16 * n + 4 * g) * 2);
        const uint32_t s0 = pk2(bflo(uu[0]) * dsk, bfhi(uu[0]) * dsk), s1 = pk2(bflo(uu[1]) * dsk, bfhi(uu[1]) * dsk);
        v0 = pk2(bflo(v0) + bflo(s0), bfhi(v0) + bfhi(s0));
        v1 = pk2(bflo(v1) + bflo(s1), bfhi(v1) + bfhi(s1));
      }
      v0 = v0 == ~0u ? 0xfffffffeu : v0;
      v1 = v1 == ~0u ? 0xfffffffeu : v1;
      __hip_atomic_store((gu64*)(p.yT + (size_t)d * L + 256 * ntile + 16 * n + 4 * g), (unsigned long long)v0 | ((unsigned long long)v1 << 32), RLX_AGENT);
    }
    __builtin_amdgcn_s_setprio(0);
    TS(5);
    {
      const int dd = 16 * cw + (lane >> 2), ch = lane & 3;
      const uint32_t off = (uint32_t)((d0 + dd) * L + t0 + ch * 8) * 2u;
      u32x4 v0, v1;
      for (unsigned spins = 0; spins < (1u << 22); ++spins) {
        uint32_t x;
        asm volatile("ds_read_b32 %0, %1\n\ts_waitcnt lgkmcnt(0)" : "=v"(x) : "v"(lds_addr(smem + K1_FLAG + 32 + cw * 4)) : "memory");
        if (__builtin_amdgcn_readfirstlane(x) == 1u) break;
        __builtin_amdgcn_s_sleep(2);
      }
      for (unsigned spins = 0; spins < (1u << 20); ++spins) {
        asm volatile("global_load_dwordx4 %0, %2, %3 sc1\n\tglobal_load_dwordx4 %1, %2, %3 offset:64 sc1\n\ts_waitcnt vmcnt(0)"
                     : "=&v"(v0), "=&v"(v1) : "v"(off), "s"(p.yT) : "memory");
        const bool ok = v0[0] != ~0u && v0[1] != ~0u && v0[2] != ~0u && v0[3] != ~0u && v1[0] != ~0u && v1[1] != ~0u && v1[2] != ~0u && v1[3] != ~0u;
        if (__all(ok)) break;
        __builtin_amdgcn_s_sleep(4);
      }
      *(u32x4*)(smem + K1_YOFF + dd * 144 + ch * 16) = v0;
      *(u32x4*)(smem + K1_YOFF + dd * 144 + ch * 16 + 64) = v1;
    }
    TS(6);
    WAIT_L(0);
    BAR();
  }
  TSDUMP(1);
}

extern "C" void kernel_launch(void* const* d_in, const int* in_sizes, int n_in, void* d_out, int out_size, void* d_ws, size_t ws_size,
                              hipStream_t stream) {
  char* ws = (char*)d_ws;
  P0 p0;
  p0.u = (const u16*)d_in[0]; p0.Are = (const u16*)d_in[1]; p0.Aim = (const u16*)d_in[2]; p0.Bre = (const u16*)d_in[3];
  p0.Bim = (const u16*)d_in[4]; p0.Cre = (const u16*)d_in[5]; p0.Cim = (const u16*)d_in[6]; p0.logdt = (const u16*)d_in[7];
  p0.wg = (const u16*)d_in[10]; p0.wu = (const u16*)d_in[11]; p0.wd = (const u16*)d_in[12]; p0.gw = (const u16*)d_in[13];
  p0.h = (u16*)(ws + WS_H); p0.wdt = (u16*)(ws + WS_WDT); p0.uT = (u16*)(ws + WS_UT); p0.E = (u16*)(ws + WS_E);
  p0.yT = (u16*)(ws + WS_YT); p0.g = (float*)(ws + WS_G);
  p0.Dskip = (const u16*)d_in[8];
  P1 p1;
  p1.u = p0.u; p1.Dskip = (const u16*)d_in[8]; p1.h = p0.h; p1.wdt = p0.wdt; p1.uT = p0.uT; p1.E = p0.E; p1.g = p0.g;
  p1.yT = p0.yT; p1.out = (u16*)d_out;
  (void)hipFuncSetAttribute((const void*)k0_kernel, hipFuncAttributeMaxDynamicSharedMemorySize, K0_LDS);
  (void)hipFuncSetAttribute((const void*)k1_kernel, hipFuncAttributeMaxDynamicSharedMemorySize, K1_LDS);
  k0_kernel<<<256, 768, K0_LDS, stream>>>(p0);
  k1_kernel<<<256, 512, K1_LDS, stream>>>(p1);
}
```

```cpp
#include <hip/hip_runtime.h>
#include <hip/hip_bf16.h>
#include <stdint.h>

typedef unsigned short u16;
typedef __attribute__((ext_vector_type(4))) short s16x4;
typedef __attribute__((ext_vector_type(8))) short s16x8;
typedef __attribute__((ext_vector_type(8))) __bf16 bf16x8;
typedef __attribute__((ext_vector_type(2))) __bf16 bf16x2;
typedef __attribute__((ext_vector_type(2))) float f32x2;
typedef __attribute__((ext_vector_type(4))) float f32x4;
typedef __attribute__((ext_vector_type(4))) unsigned u32x4;
typedef __attribute__((ext_vector_type(2))) unsigned u32x2;
typedef __attribute__((address_space(3))) s16x4* lds_s16x4_ptr;
typedef __attribute__((address_space(1))) unsigned gu32;
typedef __attribute__((address_space(1))) unsigned long long gu64;
#define RLX_AGENT __ATOMIC_RELAXED, __HIP_MEMORY_SCOPE_AGENT
#define DEV __device__ __forceinline__

constexpr int L = 2048, D = 512, NM = 128, F = 1792;
constexpr int KS2 = F / 32;

constexpr size_t WS_H = 0;
constexpr size_t WS_WDT = WS_H + (size_t)L * F * 2;
constexpr size_t WS_UT = WS_WDT + (size_t)F * D * 2;
constexpr size_t WS_YT = WS_UT + (size_t)L * D * 2;
constexpr int ES = 2080;
constexpr size_t WS_E = WS_YT + (size_t)L * D * 2 + 128;
constexpr size_t WS_G = WS_E + (size_t)D * ES * 2;

#define TSDECL
#define TS(ph)
#define TSDUMP(K)
#define TS_ARG
#define TS_PASS
DEV float bf2f(u16 v) { return __uint_as_float(((uint32_t)v) << 16); }
DEV float bflo(uint32_t v) { return __uint_as_float(v << 16); }
DEV float bfhi(uint32_t v) { return __uint_as_float(v & 0xffff0000u); }
DEV uint32_t pk2(float a, float b) { f32x2 v = {a, b}; return __builtin_bit_cast(uint32_t, __builtin_convertvector(v, bf16x2)); }
DEV float rbf(float a) { return bflo(pk2(a, 0.f) & 0xffffu); }
DEV f32x4 mfma16(u32x4 a, u32x4 b, f32x4 c) {
  return __builtin_amdgcn_mfma_f32_16x16x32_bf16(__builtin_bit_cast(bf16x8, a), __builtin_bit_cast(bf16x8, b), c, 0, 0, 0);
}
DEV u32x2 tr_read(const char* p) {
  return __builtin_bit_cast(u32x2, __builtin_amdgcn_ds_read_tr16_b64_v4i16((lds_s16x4_ptr)p));
}
DEV u32x4 tr_read2(const char* p0, const char* p1) {
  u32x2 a = tr_read(p0), b = tr_read(p1);
  u32x4 r = {a.x, a.y, b.x, b.y};
  return r;
}
DEV uint32_t lds_addr(const void* p) { return (uint32_t)(uintptr_t)(__attribute__((address_space(3))) const char*)p; }
DEV void glds16(const void* sbase, uint32_t voff, uint32_t lds) {
  unsigned keep;
  asm volatile("s_mov_b32 %0, m0\n\ts_mov_b32 m0, %3\n\ts_nop 0\n\tglobal_load_lds_dwordx4 %1, %2\n\ts_mov_b32 m0, %0"
               : "=&s"(keep) : "v"(voff), "s"(sbase), "s"(lds) : "memory");
}
#define WAIT_V(n) asm volatile("s_waitcnt vmcnt(" #n ")" ::: "memory")
#define WAIT_L(n) asm volatile("s_waitcnt lgkmcnt(" #n ")" ::: "memory")
#define BAR() __builtin_amdgcn_s_barrier()

template <bool WT>
DEV void st16(void* base, size_t byte_off, u32x4 v) {
  if (WT) {
    __builtin_amdgcn_raw_buffer_store_b128(v, __builtin_amdgcn_make_buffer_rsrc(base, 0, 0x7fffffff, 0x00020000), (uint32_t)byte_off, 0, 16);
  } else {
    *(u32x4*)((char*)base + byte_off) = v;
  }
}
#define WT_AUX 1
#define WT_H 1

struct P0 {
  const u16 *u, *Are, *Aim, *Bre, *Bim, *Cre, *Cim, *logdt, *wg, *wu, *wd, *gw, *Dskip;
  u16 *h, *wdt, *uT, *E, *yT;
  float* g;
};

constexpr int STAGE_B = 49152;
constexpr int K0_LDS = 3 * STAGE_B + 15360;

DEV int wswz(int row) { return ((row & 3) << 2) | ((row >> 2) & 3); }

struct SsmPar { u32x4 are, aim, bre, bim, cre, cim; };

DEV void ssm_load(const P0& p, uint32_t off, SsmPar& s) {
  asm volatile("global_load_dwordx4 %0, %1, %2" : "=&v"(s.are) : "v"(off), "s"(p.Are) : "memory");
  asm volatile("global_load_dwordx4 %0, %1, %2" : "=&v"(s.aim) : "v"(off), "s"(p.Aim) : "memory");
  asm volatile("global_load_dwordx4 %0, %1, %2" : "=&v"(s.bre) : "v"(off), "s"(p.Bre) : "memory");
  asm volatile("global_load_dwordx4 %0, %1, %2" : "=&v"(s.bim) : "v"(off), "s"(p.Bim) : "memory");
  asm volatile("global_load_dwordx4 %0, %1, %2" : "=&v"(s.cre) : "v"(off), "s"(p.Cre) : "memory");
  asm volatile("global_load_dwordx4 %0, %1, %2" : "=&v"(s.cim) : "v"(off), "s"(p.Cim) : "memory");
}
#define SSM_WAIT(n, s) SSM_WAIT_(n, s)
#define SSM_WAIT_(n, s) asm volatile("s_waitcnt vmcnt(" #n ")" : "+v"(s.are), "+v"(s.aim), "+v"(s.bre), "+v"(s.bim), "+v"(s.cre), "+v"(s.cim)::"memory")

DEV void pair_sync_at(uint32_t mine) {
  const uint32_t other = mine ^ 8u;
  asm volatile("s_waitcnt lgkmcnt(0)\n\tds_write_b32 %0, %1" ::"v"(mine), "v"(1u) : "memory");
  for (unsigned spins = 0; spins < (1u << 24); ++spins) {
    uint32_t x;
    asm volatile("ds_read_b32 %0, %1\n\ts_waitcnt lgkmcnt(0)" : "=v"(x) : "v"(other) : "memory");
    if (__builtin_amdgcn_readfirstlane(x) == 1u) break;
  }
}

struct KgFrag { u32x4 ah[3], al[3], bh, bl; };
template <int H>
DEV void kgen_half(const SsmPar& sp, float dt, float p0, float p1, float p2, float q, KgFrag& f, f32x4 (&acc)[3]) {
  constexpr int W = H & 3, JH = H & 1;
  uint32_t wa = sp.are[W], wi = sp.aim[W], wbr = sp.bre[W], wbi = sp.bim[W], wcr = sp.cre[W], wci = sp.cim[W];
  asm volatile("" : "+v"(wa), "+v"(wi), "+v"(wbr), "+v"(wbi), "+v"(wcr), "+v"(wci));
#pragma unroll
  for (int jj = 0; jj < 2; ++jj) {
    const int j = 2 * JH + jj;
    const float ar = dt * (jj ? bfhi(wa) : bflo(wa)), ai = dt * (jj ? bfhi(wi) : bflo(wi));
    const float br = dt * (jj ? bfhi(wbr) : bflo(wbr)), bi = dt * (jj ? bfhi(wbi) : bflo(wbi));
    const float cr = jj ? bfhi(wcr) : bflo(wcr), ci = jj ? bfhi(wci) : bflo(wci);
    const float a2 = ar * 1.44269504088896340736f, wv = ai * 0.15915494309189533577f;
    const float xr = cr * br - ci * bi, xi = cr * bi + ci * br;
    {
      const float e = __builtin_amdgcn_exp2f(a2 * q), r = __builtin_amdgcn_fractf(wv * q);
      const float s = __builtin_amdgcn_sinf(r) * e, co = __builtin_amdgcn_cosf(r) * e;
      const float vr = xr * co - xi * s, vi = -(xr * s + xi * co);
      const uint32_t hi = pk2(vr, vi);
      f.bh[j] = hi;
      f.bl[j] = pk2(vr - bflo(hi), vi - bfhi(hi));
    }
#pragma unroll
    for (int T = 0; T < 3; ++T) {
      const float pp = T == 0 ? p0 : (T == 1 ? p1 : p2);
      const float e = __builtin_amdgcn_exp2f(a2 * pp), r = __builtin_amdgcn_fractf(wv * pp);
      const float wr = __builtin_amdgcn_cosf(r) * e, wim = __builtin_amdgcn_sinf(r) * e;
      const uint32_t hi = pk2(wr, wim);
      f.ah[T][j] = hi;
      f.al[T][j] = pk2(wr - bflo(hi), wim - bfhi(hi));
    }
  }
  if (JH == 1) {
#pragma unroll
    for (int T = 0; T < 3; ++T) {
      acc[T] = mfma16(f.al[T], f.bh, acc[T]);
      acc[T] = mfma16(f.ah[T], f.bl, acc[T]);
      acc[T] = mfma16(f.ah[T], f.bh, acc[T]);
    }
    asm volatile("" : "+v"(acc[0]), "+v"(acc[1]), "+v"(acc[2]));
  } else {
    asm volatile("" : "+v"(f.ah[0]), "+v"(f.ah[1]), "+v"(f.ah[2]), "+v"(f.al[0]), "+v"(f.al[1]), "+v"(f.al[2]), "+v"(f.bh), "+v"(f.bl));
  }
  __builtin_amdgcn_sched_barrier(0);
}

#define KTS(i)
DEV void kgen_waves(const P0& p, char* scratch, int b, bool aux, int tid TS_ARG) {
  const int t = tid - 512, kw = __builtin_amdgcn_readfirstlane(t >> 6), lane = t & 63, i = lane & 15, ch0 = 2 * b, cc = kw >> 1;
#define KPRIO 3
#define KPRIO2 1
#define NPRE 3
  __builtin_amdgcn_s_setprio(KPRIO);
  SsmPar sp0, sp1;
  uint32_t ld2, ds2;
  asm volatile("global_load_dword %0, %2, %3\n\tglobal_load_dword %1, %2, %4"
               : "=&v"(ld2), "=&v"(ds2) : "v"((uint32_t)b * 4u), "s"(p.logdt), "s"(p.Dskip) : "memory");
  const uint32_t off = (uint32_t)((ch0 + cc) * NM + 64 * (kw & 1) + 8 * (lane >> 4)) * 2u;
  ssm_load(p, off, sp0);
  ssm_load(p, off + 64u, sp1);
  __builtin_amdgcn_sched_barrier(0);
  BAR();
  __builtin_amdgcn_sched_barrier(0);
  SSM_WAIT(6, sp0);
  asm volatile("" : "+v"(ld2), "+v"(ds2));
  const float dt = rbf(__expf(cc ? bfhi(ld2) : bflo(ld2)));
  const float p0 = (float)((i & 7) + 128 * (i >> 3));
  const float p1 = (i < 8) ? (float)(256 + 2 * (i & 3) + 128 * (i >> 2)) : (float)(512 + 4 * (i & 1) + 128 * ((i - 8) >> 1));
  const float p2 = (i < 8) ? (float)(1024 + 128 * i) : 2048.f;
  const float q = (float)(8 * i);
  f32x4 acc[3] = {{0, 0, 0, 0}, {0, 0, 0, 0}, {0, 0, 0, 0}};
  KgFrag f;
#pragma unroll
  for (int T = 0; T < 3; ++T) f.ah[T] = f.al[T] = (u32x4){0, 0, 0, 0};
  f.bh = f.bl = (u32x4){0, 0, 0, 0};
  TS(1);
  if (0 == NPRE || (0 > NPRE && !aux)) BAR();
  kgen_half<0>(sp0, dt, p0, p1, p2, q, f, acc);
  KTS(3);
  if (1 == NPRE || (1 > NPRE && !aux)) BAR();
  kgen_half<1>(sp0, dt, p0, p1, p2, q, f, acc);
  KTS(4);
  if (2 == NPRE || (2 > NPRE && !aux)) BAR();
  kgen_half<2>(sp0, dt, p0, p1, p2, q, f, acc);
  KTS(5);
  if (3 == NPRE || (3 > NPRE && !aux)) BAR();
  kgen_half<3>(sp0, dt, p0, p1, p2, q, f, acc);
  KTS(6);
  if (!aux) __builtin_amdgcn_s_setprio(KPRIO2);
  if (4 == NPRE || (4 > NPRE && !aux)) BAR();
  SSM_WAIT(0, sp1);
  kgen_half<4>(sp1, dt, p0, p1, p2, q, f, acc);
  KTS(7);
  if (5 == NPRE || (5 > NPRE && !aux)) BAR();
  kgen_half<5>(sp1, dt, p0, p1, p2, q, f, acc);
  KTS(8);
  if (6 == NPRE || (6 > NPRE && !aux)) BAR();
  kgen_half<6>(sp1, dt, p0, p1, p2, q, f, acc);
  KTS(9);
  if (7 == NPRE || (7 > NPRE && !aux)) BAR();
  kgen_half<7>(sp1, dt, p0, p1, p2, q, f, acc);
  float* sc = (float*)scratch;
  u16* kc = (u16*)(scratch + 12288) + cc * 768;
#pragma unroll
  for (int T = 0; T < 3; ++T) *(f32x4*)(sc + ((kw * 3 + T) * 64 + lane) * 4) = acc[T];
  WAIT_L(0);
  TS(15);
  BAR();
  const int tt = t & 127;
#pragma unroll
  for (int T = 0; T < 3; ++T) {
    if (NPRE >= 4 && T == 2 && !aux) BAR();
#pragma unroll
    for (int hf = 0; hf < 2; ++hf) {
      const int e = hf * 128 + tt, ln = e >> 2, j = e & 3;
      const int ii = 4 * (ln >> 4) + j, bb = ln & 15;
      const float s = sc[((cc * 2) * 3 + T) * 256 + e] + sc[((cc * 2 + 1) * 3 + T) * 256 + e];
      int idx;
      if (T == 0) idx = (ii & 7) + 128 * (ii >> 3) + 8 * bb;
      else if (T == 1) idx = ii < 8 ? 256 + (ii & 3) + 64 * (ii >> 2) + 4 * bb : 384 + (ii & 1) + 32 * ((ii - 8) >> 1) + 2 * bb;
      else idx = ii < 8 ? 512 + 16 * ii + bb : ((ii == 8 && bb == 0) ? 640 : 700);
      kc[idx] = (u16)(pk2(s, 0.f) & 0xffffu);
    }
    __builtin_amdgcn_sched_barrier(0);
  }
  WAIT_L(0);
  BAR();
  if ((t & 127) == 0) {
    const uint32_t dv = cc ? ds2 >> 16 : ds2 & 0xffffu;
    __builtin_amdgcn_raw_buffer_store_b32(dv, __builtin_amdgcn_make_buffer_rsrc(p.E, 0, 0x7fffffff, 0x00020000), (uint32_t)((ch0 + cc) * ES * 2), 0, 16);
  }
#pragma unroll
  for (int hf = 0; hf < 2; ++hf) {
    if (NPRE >= 3 && hf == 1 && !aux) BAR();
    int l0 = 1024 * hf + 8 * tt;
    asm volatile("" : "+v"(l0));
    u32x4 o;
#pragma unroll
    for (int e = 0; e < 4; ++e) {
      const int la = l0 + 2 * e, lb = la + 1;
      const uint32_t pb = pk2((float)la, (float)lb);
      const int ia = la < 128 ? la : (int)(pb & 0xffffu) - 17024, ib = lb < 128 ? lb : (int)(pb >> 16) - 17024;
      o[e] = (uint32_t)kc[ia] | ((uint32_t)kc[ib] << 16);
    }
    st16<WT_AUX>(p.E, ((size_t)(ch0 + cc) * ES + 16 + l0) * 2, o);
    __builtin_amdgcn_sched_barrier(0);
  }
  st16<WT_AUX>(p.yT, ((size_t)b * 4096 + t * 8) * 2, (u32x4){~0u, ~0u, ~0u, ~0u});
  st16<WT_AUX>(p.yT, ((size_t)b * 4096 + 2048 + t * 8) * 2, (u32x4){~0u, ~0u, ~0u, ~0u});
  TS(14);
  if (!aux)
    for (int i_ = 0; i_ < NPRE - 2 - (NPRE >= 4) - (NPRE >= 3); ++i_) BAR();
}

DEV void k0_glds(const P0& p, uint32_t lb, const uint32_t (&voff)[3], int m0, int f0, int k0, int piece) {
  if (piece < 2) glds16(p.u + (size_t)m0 * D + k0, voff[0] + piece * (8 * D * 2), lb + piece * 1024);
  else if (piece < 4) glds16(p.wg + (size_t)k0 * F + f0, voff[piece - 1], lb + 16384 + (piece - 2) * 1024);
  else glds16(p.wu + (size_t)k0 * F + f0, voff[piece - 3], lb + 32768 + (piece - 4) * 1024);
}

__global__ __launch_bounds__(768, 3) void k0_kernel(P0 p) {
  extern __shared__ __attribute__((aligned(16))) char smem[];
  const int tid = threadIdx.x, lane = tid & 63, w = __builtin_amdgcn_readfirstlane(tid >> 6);
  const int b = blockIdx.x, xcd = b & 7, idx = b >> 3;
  TSDECL;
  TS(0);
  if (w >= 8) {
    kgen_waves(p, smem + 3 * STAGE_B, b, idx >= 28, tid TS_PASS);
  } else if (idx < 28) {
    const int mt = (xcd >> 1) * 4 + idx / 7, ft = (xcd & 1) * 7 + idx % 7;
    const int m0 = mt * 128, f0 = ft * 128;
#define K0ROT 2
    const int rot = K0ROT == 0 ? 2 * (idx / 7) + idx % 7 : K0ROT == 1 ? 0 : K0ROT == 2 ? (idx & 1) * 4 : K0ROT == 3 ? 2 * (idx / 7) : idx % 7;
    uint32_t voff[3];
    {
      const int row = 16 * w + (lane >> 3);
      voff[0] = (uint32_t)(row * D + 8 * ((lane & 7) ^ (row & 7))) * 2u;
      const int r0 = 8 * w + (lane >> 4), r1 = r0 + 4;
      voff[1] = (uint32_t)(r0 * F + 8 * ((lane & 15) ^ wswz(r0))) * 2u;
      voff[2] = (uint32_t)(r1 * F + 8 * ((lane & 15) ^ wswz(r1))) * 2u;
    }
    const uint32_t lb = lds_addr(smem) + 2048 * w;
#pragma unroll
    for (int s = 0; s < 3; ++s) {
      if (s == 1) BAR();
#pragma unroll
      for (int pc = 0; pc < 6; ++pc) k0_glds(p, lb + s * STAGE_B, voff, m0, f0, 64 * ((rot + s) & 7), pc);
    }
    const int wm = w >> 2, wn = w & 3, g = lane >> 4, q = (lane & 15) >> 2, pp = lane & 3;
    f32x4 acc[2][2][4];
#pragma unroll
    for (int a = 0; a < 2; ++a)
#pragma unroll
      for (int c = 0; c < 2; ++c)
#pragma unroll
        for (int m = 0; m < 4; ++m) acc[a][c][m] = (f32x4){0, 0, 0, 0};
    u32x4 xf[2][4], wf[2][2][2];
    auto frags = [&](const char* buf, int ss) {
#pragma unroll
      for (int m = 0; m < 4; ++m) {
        const int row = 64 * wm + 16 * m + (lane & 15), c = 4 * ss + g;
        xf[ss][m] = *(const u32x4*)(buf + row * 128 + ((c ^ (row & 7)) << 4));
      }
#pragma unroll
      for (int a = 0; a < 2; ++a)
#pragma unroll
        for (int c = 0; c < 2; ++c) {
          const int ch = 4 * wn + 2 * c + (pp >> 1);
          const int r0 = 32 * ss + 8 * g + q, r1 = r0 + 4;
          const char* wb = buf + 16384 + a * 16384 + 8 * (pp & 1);
          wf[ss][a][c] = tr_read2(wb + 256 * r0 + 16 * (ch ^ wswz(r0)), wb + 256 * r1 + 16 * (ch ^ wswz(r1)));
        }
    };
    auto mm = [&](int ss, bool stage, int sb, int k0, int pc0) {
#pragma unroll
      for (int ac = 0; ac < 4; ++ac) {
#pragma unroll
        for (int m = 0; m < 4; ++m) acc[ac >> 1][ac & 1][m] = mfma16(wf[ss][ac >> 1][ac & 1], xf[ss][m], acc[ac >> 1][ac & 1][m]);
        if (stage && ac < 3) {
          __builtin_amdgcn_sched_barrier(0);
          k0_glds(p, lb + sb * STAGE_B, voff, m0, f0, k0, pc0 + ac);
          __builtin_amdgcn_sched_barrier(0);
        }
      }
    };
#pragma unroll
    for (int s = 0; s < 8; ++s) {
      if (s == 7) WAIT_V(0); else if (s == 0) WAIT_V(12); else WAIT_V(6);
      WAIT_L(0);
      BAR();
      TS(3 + s);
      int bo = (s % 3) * STAGE_B;
      asm volatile("" : "+s"(bo));
      const char* buf = smem + bo;
      const bool st = (s >= 1 && s + 2 < 8);
      const int k0 = 64 * ((rot + s + 2) & 7);
      frags(buf, 0);
      __builtin_amdgcn_sched_barrier(0);
      if (s > 0) mm(1, st, (s + 2) % 3, k0, 0);
      __builtin_amdgcn_sched_barrier(0);
      frags(buf, 1);
      __builtin_amdgcn_sched_barrier(0);
      mm(0, st, (s + 2) % 3, k0, 3);
      __builtin_amdgcn_sched_barrier(0);
    }
    mm(1, false, 0, 0, 0);
    TS(11);
#pragma unroll
    for (int m = 0; m < 4; ++m) {
      u32x4 o;
#pragma unroll
      for (int c = 0; c < 2; ++c)
#pragma unroll
        for (int jj = 0; jj < 2; ++jj) {
          const uint32_t g2 = pk2(acc[0][c][m][2 * jj], acc[0][c][m][2 * jj + 1]), u2 = pk2(acc[1][c][m][2 * jj], acc[1][c][m][2 * jj + 1]);
          const f32x2 gg = {bflo(g2), bfhi(g2)};
          const f32x2 tt = gg * -1.44269504088896340736f;
          const f32x2 dn = (f32x2){__builtin_amdgcn_exp2f(tt[0]), __builtin_amdgcn_exp2f(tt[1])} + 1.f;
          const f32x2 sv = gg * (f32x2){__builtin_amdgcn_rcpf(dn[0]), __builtin_amdgcn_rcpf(dn[1])};
          const uint32_t s2 = pk2(sv[0], sv[1]);
          const f32x2 hv = (f32x2){bflo(s2), bfhi(s2)} * (f32x2){bflo(u2), bfhi(u2)};
          o[2 * c + jj] = pk2(hv[0], hv[1]);
        }
      const int mt64 = (m0 >> 6) + wm, ks = 4 * ft + wn;
      st16<WT_H>(p.h, ((size_t)((mt64 * KS2 + ks) * 4 + m) * 64 + lane) * 16, o);
    }
    TS(12);
  } else {
    BAR();
    const int a = xcd * 4 + (idx - 28);
    constexpr int RS = 1056;
    const int t0 = 64 * a;
    u32x4 uv[8], wv[8];
#pragma unroll
    for (int r = 0; r < 8; ++r) uv[r] = *(const u32x4*)(p.u + (size_t)(t0 + 8 * w + r) * D + lane * 8);
    const int ks2 = a + 32 < KS2 ? a + 32 : a;
#pragma unroll
    for (int r = 0; r < 4; ++r) {
      wv[r] = *(const u32x4*)(p.wd + (size_t)(32 * a + 4 * w + r) * D + lane * 8);
      wv[4 + r] = *(const u32x4*)(p.wd + (size_t)(32 * ks2 + 4 * w + r) * D + lane * 8);
    }
    const u32x4 gwv = *(const u32x4*)(p.gw + lane * 8);
    {
      float s[8];
#pragma unroll
      for (int r = 0; r < 8; ++r) {
        *(u32x4*)(smem + (8 * w + r) * RS + lane * 16) = uv[r];
        s[r] = 0.f;
#pragma unroll
        for (int e = 0; e < 4; ++e) {
          const uint32_t pr = pk2(bflo(uv[r][e]) * bflo(gwv[e]), bfhi(uv[r][e]) * bfhi(gwv[e]));
          s[r] += bflo(pr) + bfhi(pr);
        }
      }
#pragma unroll
      for (int r = 0; r < 4; ++r) { const bool hi = lane & 32; const float keep = hi ? s[r + 4] : s[r], send = hi ? s[r] : s[r + 4]; s[r] = keep + __shfl_xor(send, 32); }
#pragma unroll
      for (int r = 0; r < 2; ++r) { const bool hi = lane & 16; const float keep = hi ? s[r + 2] : s[r], send = hi ? s[r] : s[r + 2]; s[r] = keep + __shfl_xor(send, 16); }
      { const bool hi = lane & 8; const float keep = hi ? s[1] : s[0], send = hi ? s[0] : s[1]; s[0] = keep + __shfl_xor(send, 8); }
      s[0] += __shfl_xor(s[0], 4); s[0] += __shfl_xor(s[0], 2); s[0] += __shfl_xor(s[0], 1);
      if ((lane & 7) == 0) {
        const int r = ((lane >> 5) & 1) * 4 + ((lane >> 4) & 1) * 2 + ((lane >> 3) & 1);
        const float gs = rbf(s[0]);
        p.g[t0 + 8 * w + r] = rbf(1.f / (1.f + __expf(-gs)));
      }
    }
    WAIT_L(0);
    BAR();
    const int g = lane >> 4, q = (lane & 15) >> 2, pp = lane & 3;
#pragma unroll
    for (int c = 0; c < 4; ++c)
#pragma unroll
      for (int hh = 0; hh < 2; ++hh) {
        const int dt_ = 4 * w + c;
        const int r0 = 32 * hh + 8 * g + q;
        const char* base = smem + (16 * dt_ + 4 * pp) * 2;
        const u32x4 v = tr_read2(base + r0 * RS, base + (r0 + 4) * RS);
        st16<WT_AUX>(p.uT, ((size_t)(16 * dt_ + (lane & 15)) * L + t0 + 32 * hh + 8 * g) * 2, v);
      }
    TS(3);
    WAIT_L(0);
    BAR();
#pragma unroll
    for (int r = 0; r < 4; ++r) {
      *(u32x4*)(smem + (4 * w + r) * RS + lane * 16) = wv[r];
      *(u32x4*)(smem + (32 + 4 * w + r) * RS + lane * 16) = wv[4 + r];
    }
    WAIT_L(0);
    BAR();
#pragma unroll
    for (int hh = 0; hh < 2; ++hh) {
      if (hh == 0 || a + 32 < KS2) {
        const int ks = a + 32 * hh;
#pragma unroll
        for (int c = 0; c < 4; ++c) {
          const int nt = 4 * w + c;
          const char* base = smem + 32 * hh * RS + (16 * nt + 4 * pp) * 2;
          const u32x4 v = tr_read2(base + (4 * g + q) * RS, base + (16 + 4 * g + q) * RS);
          st16<WT_AUX>(p.wdt, ((size_t)(((nt >> 2) * KS2 + ks) * 4 + (nt & 3)) * 64 + lane) * 16, v);
        }
      }
    }
    TS(12);
  }
  TSDUMP(0);
}

#define GO_STEP 7
#define FPRIO 1
#define LPRIO 0
struct P1 {
  const u16 *u, *Dskip, *h, *wdt, *uT, *E;
  const float* g;
  u16 *yT, *out;
};

constexpr int CP_STRIDE = 4160;
constexpr int CW_U = 4 * CP_STRIDE, CW_SIZE = CW_U + 4608;
constexpr int K1_XCH = 2 * CW_SIZE;
constexpr int K1_YOFF = K1_XCH + 16384;
constexpr int K1_RED = K1_YOFF + 64 * 144;
constexpr int K1_FLAG = K1_RED + 49152;
constexpr int K1_LDS = K1_FLAG + 64;

DEV void conv_loop(const char* abase, const char* bbase, f32x4 (&y)[8]) {
  u32x4 af[2][8], bf[2][8];
  auto ld = [&](int buf, int it) {
#pragma unroll
    for (int m = 0; m < 8; ++m) bf[buf][m] = *(const u32x4*)(bbase + 512 * m - 64 * it);
#pragma unroll
    for (int m = 0; m < 8; ++m) {
      const char* ap = abase + 512 * m + 64 * it;
      af[buf][m] = tr_read2(ap, ap - 8);
    }
  };
  ld(0, 0);
#pragma unroll
  for (int it = 0; it < 4; ++it) {
    const int cur = it & 1;
    __builtin_amdgcn_sched_barrier(0);
    if (it < 3) ld(cur ^ 1, it + 1);
#pragma unroll
    for (int m = 0; m < 8; ++m)
#pragma unroll
      for (int T = m; T < 8; ++T) y[T] = mfma16(af[cur][m], bf[cur][T - m], y[T]);
    if (it < 3) {
#pragma unroll
      for (int i = 0; i < 12; ++i) {
        __builtin_amdgcn_sched_group_barrier(0x008, 3, 0);
        __builtin_amdgcn_sched_group_barrier(0x100, 2, 0);
      }
    }
  }
  __builtin_amdgcn_sched_barrier(0);
}
DEV void pair_sync(char* smem, int cw, int which) { pair_sync_at(lds_addr(smem + K1_FLAG + cw * 8 + which * 4)); }

__global__ __launch_bounds__(512, 2) void k1_kernel(P1 p) {
  extern __shared__ __attribute__((aligned(16))) char smem[];
  const int tid = threadIdx.x, lane = tid & 63, w = __builtin_amdgcn_readfirstlane(tid >> 6);
  const int b = blockIdx.x, xcd = b & 7, idx = b >> 3;
  const int mt = (xcd >> 1) * 8 + (idx >> 2), nt = (xcd & 1) * 4 + (idx & 3);
  const int t0 = mt * 64, d0 = nt * 64;
  const int g = lane >> 4, n = lane & 15;
  TSDECL;
  TS(0);
  if (w >= 4) __builtin_amdgcn_s_setprio(3);
  if (w == 4) {
    asm volatile("ds_write_b32 %0, %1\n\ts_waitcnt lgkmcnt(0)" ::"v"(lds_addr(smem + K1_FLAG + (lane & 15) * 4)), "v"(0u) : "memory");
  }
  BAR();
  __builtin_amdgcn_sched_barrier(0);
  if (w < 4) {
    __builtin_amdgcn_s_setprio(FPRIO);
    f32x4 acc[4][4];
#pragma unroll
    for (int a = 0; a < 4; ++a)
#pragma unroll
      for (int c = 0; c < 4; ++c) acc[a][c] = (f32x4){0, 0, 0, 0};
    const u16* hp = p.h + ((size_t)mt * KS2 + 14 * w) * 2048 + lane * 8;
    const u16* wp = p.wdt + ((size_t)nt * KS2 + 14 * w) * 2048 + lane * 8;
#define PFD 4
    constexpr int PF = PFD;
    u32x4 hf[PF][4], wf[PF][4];
    const int hrot = w * 512;
#pragma unroll
    for (int s = 0; s < PF; ++s)
#pragma unroll
      for (int a = 0; a < 4; ++a) {
        hf[s][a] = *(const u32x4*)(hp + s * 2048 + ((a * 512 + hrot) & 2047));
        wf[s][a] = *(const u32x4*)(wp + s * 2048 + a * 512);
      }
    const int t = t0 + 16 * w + n;
    float gt;
    __builtin_amdgcn_sched_barrier(0);
    TS(15);
    __builtin_amdgcn_sched_barrier(0);
#pragma unroll
    for (int s = 0; s < 14; ++s) {
#pragma unroll
      for (int a = 0; a < 4; ++a)
#pragma unroll
        for (int c = 0; c < 4; ++c) acc[a][c] = mfma16(wf[s % PF][a], hf[s % PF][c], acc[a][c]);
      __builtin_amdgcn_sched_barrier(0);
      if (s + PF < 14) {
#pragma unroll
        for (int a = 0; a < 4; ++a) {
          hf[s % PF][a] = *(const u32x4*)(hp + (s + PF) * 2048 + ((a * 512 + hrot) & 2047));
          wf[s % PF][a] = *(const u32x4*)(wp + (s + PF) * 2048 + a * 512);
        }
      }
      if (s == GO_STEP) asm volatile("ds_write_b32 %0, %1" ::"v"(lds_addr(smem + K1_FLAG + 32 + w * 4)), "v"(1u) : "memory");
      if (s + PF == 14) gt = p.g[t];
      __builtin_amdgcn_sched_barrier(0);
    }
    TS(1);
#pragma unroll
    for (int c = 1; c < 4; ++c)
#pragma unroll
      for (int a = 0; a < 4; ++a) *(f32x4*)(smem + K1_RED + ((((w + c) & 3) * 3 + (c - 1)) * 4 + a) * 1024 + lane * 16) = acc[a][c];
    WAIT_L(0);
    BAR();
    TS(7);
    const float omg = rbf(1.f - gt);
    const int q = n >> 2, pp = lane & 3;
#pragma unroll
    for (int a = 0; a < 4; ++a) {
      f32x4 bs = acc[a][0];
#pragma unroll
      for (int s = 0; s < 3; ++s) bs += *(const f32x4*)(smem + K1_RED + ((w * 3 + s) * 4 + a) * 1024 + lane * 16);
      const u32x2 yv = tr_read(smem + K1_YOFF + (16 * a + 4 * g + q) * 144 + (16 * w + 4 * pp) * 2);
      u32x2 o;
#pragma unroll
      for (int e = 0; e < 2; ++e) {
        const uint32_t ga = pk2(gt * bflo(yv[e]), gt * bfhi(yv[e]));
        const uint32_t bb = pk2(bs[2 * e], bs[2 * e + 1]);
        const uint32_t gb = pk2(omg * bflo(bb), omg * bfhi(bb));
        o[e] = pk2(bflo(ga) + bflo(gb), bfhi(ga) + bfhi(gb));
      }
      *(u32x2*)(p.out + (size_t)t * D + d0 + 16 * a + 4 * g) = o;
    }
    TS(9);
  } else {
    const int cw = w - 4, c = cw >> 1, half = cw & 1, d = 2 * b + c;
    char* base = smem + c * CW_SIZE;
#define CSLEEP 0
    if (CSLEEP) __builtin_amdgcn_s_sleep(CSLEEP);
    const float dsk = bflo(*(const uint32_t*)(p.E + (size_t)d * ES));
    {
      const u16* eg = p.E + (size_t)d * ES;
      u32x4 cur[3];
      u32x2 prv[3];
#pragma unroll
      for (int it = 0; it < 3; ++it) {
        const int un = it < 2 ? (2 * half + it) * 64 + lane : 256 + (lane & 1);
        asm volatile("global_load_dwordx4 %0, %2, %3\n\tglobal_load_dwordx2 %1, %2, %3 offset:-8"
                     : "=&v"(cur[it]), "=&v"(prv[it]) : "v"(un * 16u), "s"(eg) : "memory");
      }
      const uint32_t lb = lds_addr(base + CW_U + 512) + half * 2048;
#pragma unroll
      for (int i = 0; i < 2; ++i) glds16(p.uT + (size_t)d * L + (2 * half + i) * 512, lane * 16u, lb + i * 1024);
      if (lane < 32 && half == 0) *(u32x4*)(base + CW_U + lane * 16) = (u32x4){0, 0, 0, 0};
      asm volatile("s_waitcnt vmcnt(2)" : "+v"(cur[0]), "+v"(cur[1]), "+v"(cur[2]), "+v"(prv[0]), "+v"(prv[1]), "+v"(prv[2])::"memory");
      TS(2);
      if (half == 0) {
        if (lane < 2) cur[0] = (u32x4){0, 0, 0, 0};
        if (lane < 3) prv[0] = (u32x2){0, 0};
      }
#pragma unroll
      for (int it = 0; it < 3; ++it) {
        const int un = it < 2 ? (2 * half + it) * 64 + lane : 256 + (lane & 1);
        const u32x4 cu = cur[it];
        const u32x2 pv = prv[it];
        u32x4 o1, o2, o3;
        o1[0] = __builtin_amdgcn_alignbyte(cu[0], pv[1], 2); o1[1] = __builtin_amdgcn_alignbyte(cu[1], cu[0], 2);
        o1[2] = __builtin_amdgcn_alignbyte(cu[2], cu[1], 2); o1[3] = __builtin_amdgcn_alignbyte(cu[3], cu[2], 2);
        o2[0] = pv[1]; o2[1] = cu[0]; o2[2] = cu[1]; o2[3] = cu[2];
        o3[0] = __builtin_amdgcn_alignbyte(pv[1], pv[0], 2); o3[1] = o1[0]; o3[2] = o1[1]; o3[3] = o1[2];
        if (it < 2 || (lane < 2 && half == 1)) {
          *(u32x4*)(base + 0 * CP_STRIDE + un * 16) = cu;
          *(u32x4*)(base + 1 * CP_STRIDE + un * 16) = o1;
          *(u32x4*)(base + 2 * CP_STRIDE + un * 16) = o2;
          *(u32x4*)(base + 3 * CP_STRIDE + un * 16) = o3;
        }
      }
    }
    WAIT_V(0);
    pair_sync(smem, cw, 0);
    __builtin_amdgcn_s_setprio(LPRIO);
    TS(3);
    f32x4 y[8];
#pragma unroll
    for (int a = 0; a < 8; ++a) y[a] = (f32x4){0, 0, 0, 0};
    {
      const int q = n >> 2, pp = lane & 3;
      const char* abase = base + q * CP_STRIDE + (16 - 8 * g + 4 * pp) * 2 + 32 * (8 * half + 1);
      const char* bbase = base + CW_U + 512 + (16 * n + 8 * g) * 2 - 32 * (8 * half + 1);
      conv_loop(abase, bbase, y);
    }
    TS(4);
    __builtin_amdgcn_s_setprio(3);
#pragma unroll
    for (int a = 0; a < 4; ++a) *(f32x4*)(smem + K1_XCH + (cw * 4 + a) * 1024 + lane * 16) = half ? y[a] : y[4 + a];
    pair_sync(smem, cw, 1);
    TS(8);
#pragma unroll
    for (int a = 0; a < 4; ++a) {
      const f32x4 o = *(const f32x4*)(smem + K1_XCH + ((cw ^ 1) * 4 + a) * 1024 + lane * 16);
      const f32x4 v = half ? o + y[4 + a] : y[a] + o;
      const int ntile = 4 * half + a;
      uint32_t v0 = pk2(v[0], v[1]), v1 = pk2(v[2], v[3]);
      {
        const u32x2 uu = *(const u32x2*)(base + CW_U + 512 + (256 * ntile + 16 * n + 4 * g) * 2);
        const uint32_t s0 = pk2(bflo(uu[0]) * dsk, bfhi(uu[0]) * dsk), s1 = pk2(bflo(uu[1]) * dsk, bfhi(uu[1]) * dsk);
        v0 = pk2(bflo(v0) + bflo(s0), bfhi(v0) + bfhi(s0));
        v1 = pk2(bflo(v1) + bflo(s1), bfhi(v1) + bfhi(s1));
      }
      v0 = v0 == ~0u ? 0xfffffffeu : v0;
      v1 = v1 == ~0u ? 0xfffffffeu : v1;
      __hip_atomic_store((gu64*)(p.yT + (size_t)d * L + 256 * ntile + 16 * n + 4 * g), (unsigned long long)v0 | ((unsigned long long)v1 << 32), RLX_AGENT);
    }
    __builtin_amdgcn_s_setprio(0);
    TS(5);
    {
      const int dd = 16 * cw + (lane >> 2), ch = lane & 3;
      const uint32_t off = (uint32_t)((d0 + dd) * L + t0 + ch * 8) * 2u;
      u32x4 v0, v1;
      for (unsigned spins = 0; spins < (1u << 22); ++spins) {
        uint32_t x;
        asm volatile("ds_read_b32 %0, %1\n\ts_waitcnt lgkmcnt(0)" : "=v"(x) : "v"(lds_addr(smem + K1_FLAG + 32 + cw * 4)) : "memory");
        if (__builtin_amdgcn_readfirstlane(x) == 1u) break;
        __builtin_amdgcn_s_sleep(2);
      }
      for (unsigned spins = 0; spins < (1u << 20); ++spins) {
        asm volatile("global_load_dwordx4 %0, %2, %3 sc1\n\tglobal_load_dwordx4 %1, %2, %3 offset:64 sc1\n\ts_waitcnt vmcnt(0)"
                     : "=&v"(v0), "=&v"(v1) : "v"(off), "s"(p.yT) : "memory");
        const bool ok = v0[0] != ~0u && v0[1] != ~0u && v0[2] != ~0u && v0[3] != ~0u && v1[0] != ~0u && v1[1] != ~0u && v1[2] != ~0u && v1[3] != ~0u;
        if (__all(ok)) break;
        __builtin_amdgcn_s_sleep(4);
      }
      *(u32x4*)(smem + K1_YOFF + dd * 144 + ch * 16) = v0;
      *(u32x4*)(smem + K1_YOFF + dd * 144 + ch * 16 + 64) = v1;
    }
    TS(6);
    WAIT_L(0);
    BAR();
  }
  TSDUMP(1);
}

extern "C" void kernel_launch(void* const* d_in, const int* in_sizes, int n_in, void* d_out, int out_size, void* d_ws, size_t ws_size,
                              hipStream_t stream) {
  char* ws = (char*)d_ws;
  P0 p0;
  p0.u = (const u16*)d_in[0]; p0.Are = (const u16*)d_in[1]; p0.Aim = (const u16*)d_in[2]; p0.Bre = (const u16*)d_in[3];
  p0.Bim = (const u16*)d_in[4]; p0.Cre = (const u16*)d_in[5]; p0.Cim = (const u16*)d_in[6]; p0.logdt = (const u16*)d_in[7];
  p0.wg = (const u16*)d_in[10]; p0.wu = (const u16*)d_in[11]; p0.wd = (const u16*)d_in[12]; p0.gw = (const u16*)d_in[13];
  p0.h = (u16*)(ws + WS_H); p0.wdt = (u16*)(ws + WS_WDT); p0.uT = (u16*)(ws + WS_UT); p0.E = (u16*)(ws + WS_E);
  p0.yT = (u16*)(ws + WS_YT); p0.g = (float*)(ws + WS_G);
  p0.Dskip = (const u16*)d_in[8];
  P1 p1;
  p1.u = p0.u; p1.Dskip = (const u16*)d_in[8]; p1.h = p0.h; p1.wdt = p0.wdt; p1.uT = p0.uT; p1.E = p0.E; p1.g = p0.g;
  p1.yT = p0.yT; p1.out = (u16*)d_out;
  (void)hipFuncSetAttribute((const void*)k0_kernel, hipFuncAttributeMaxDynamicSharedMemorySize, K0_LDS);
  (void)hipFuncSetAttribute((const void*)k1_kernel, hipFuncAttributeMaxDynamicSharedMemorySize, K1_LDS);
  k0_kernel<<<256, 768, K0_LDS, stream>>>(p0);
  k1_kernel<<<256, 512, K1_LDS, stream>>>(p1);
}
```

```cpp
#include <hip/hip_runtime.h>
#include <hip/hip_bf16.h>
#include <stdint.h>

typedef unsigned short u16;
typedef __attribute__((ext_vector_type(4))) short s16x4;
typedef __attribute__((ext_vector_type(8))) short s16x8;
typedef __attribute__((ext_vector_type(8))) __bf16 bf16x8;
typedef __attribute__((ext_vector_type(2))) __bf16 bf16x2;
typedef __attribute__((ext_vector_type(2))) float f32x2;
typedef __attribute__((ext_vector_type(4))) float f32x4;
typedef __attribute__((ext_vector_type(4))) unsigned u32x4;
typedef __attribute__((ext_vector_type(2))) unsigned u32x2;
typedef __attribute__((address_space(3))) s16x4* lds_s16x4_ptr;
typedef __attribute__((address_space(1))) unsigned gu32;
typedef __attribute__((address_space(1))) unsigned long long gu64;
#define RLX_AGENT __ATOMIC_RELAXED, __HIP_MEMORY_SCOPE_AGENT
#define DEV __device__ __forceinline__

constexpr int L = 2048, D = 512, NM = 128, F = 1792;
constexpr int KS2 = F / 32;

constexpr size_t WS_H = 0;
constexpr size_t WS_WDT = WS_H + (size_t)L * F * 2;
constexpr size_t WS_UT = WS_WDT + (size_t)F * D * 2;
constexpr size_t WS_YT = WS_UT + (size_t)L * D * 2;
constexpr int ES = 2080;
constexpr size_t WS_E = WS_YT + (size_t)L * D * 2 + 128;
constexpr size_t WS_G = WS_E + (size_t)D * ES * 2;

#define TSDECL
#define TS(ph)
#define TSDUMP(K)
#define TS_ARG
#define TS_PASS
DEV float bf2f(u16 v) { return __uint_as_float(((uint32_t)v) << 16); }
DEV float bflo(uint32_t v) { return __uint_as_float(v << 16); }
DEV float bfhi(uint32_t v) { return __uint_as_float(v & 0xffff0000u); }
DEV uint32_t pk2(float a, float b) { f32x2 v = {a, b}; return __builtin_bit_cast(uint32_t, __builtin_convertvector(v, bf16x2)); }
DEV float rbf(float a) { return bflo(pk2(a, 0.f) & 0xffffu); }
DEV f32x4 mfma16(u32x4 a, u32x4 b, f32x4 c) {
  return __builtin_amdgcn_mfma_f32_16x16x32_bf16(__builtin_bit_cast(bf16x8, a), __builtin_bit_cast(bf16x8, b), c, 0, 0, 0);
}
DEV u32x2 tr_read(const char* p) {
  return __builtin_bit_cast(u32x2, __builtin_amdgcn_ds_read_tr16_b64_v4i16((lds_s16x4_ptr)p));
}
DEV u32x4 tr_read2(const char* p0, const char* p1) {
  u32x2 a = tr_read(p0), b = tr_read(p1);
  u32x4 r = {a.x, a.y, b.x, b.y};
  return r;
}
DEV uint32_t lds_addr(const void* p) { return (uint32_t)(uintptr_t)(__attribute__((address_space(3))) const char*)p; }
DEV void glds16(const void* sbase, uint32_t voff, uint32_t lds) {
  unsigned keep;
  asm volatile("s_mov_b32 %0, m0\n\ts_mov_b32 m0, %3\n\ts_nop 0\n\tglobal_load_lds_dwordx4 %1, %2\n\ts_mov_b32 m0, %0"
               : "=&s"(keep) : "v"(voff), "s"(sbase), "s"(lds) : "memory");
}
#define WAIT_V(n) asm volatile("s_waitcnt vmcnt(" #n ")" ::: "memory")
#define WAIT_L(n) asm volatile("s_waitcnt lgkmcnt(" #n ")" ::: "memory")
#define BAR() __builtin_amdgcn_s_barrier()

template <bool WT>
DEV void st16(void* base, size_t byte_off, u32x4 v) {
  if (WT) {
    __builtin_amdgcn_raw_buffer_store_b128(v, __builtin_amdgcn_make_buffer_rsrc(base, 0, 0x7fffffff, 0x00020000), (uint32_t)byte_off, 0, 16);
  } else {
    *(u32x4*)((char*)base + byte_off) = v;
  }
}
#define WT_AUX 1
#define WT_H 1

struct P0 {
  const u16 *u, *Are, *Aim, *Bre, *Bim, *Cre, *Cim, *logdt, *wg, *wu, *wd, *gw, *Dskip;
  u16 *h, *wdt, *uT, *E, *yT;
  float* g;
};

constexpr int STAGE_B = 49152;
constexpr int K0_LDS = 3 * STAGE_B + 15360;

DEV int wswz(int row) { return ((row & 3) << 2) | ((row >> 2) & 3); }

struct SsmPar { u32x4 are, aim, bre, bim, cre, cim; };

DEV void ssm_load(const P0& p, uint32_t off, SsmPar& s) {
  asm volatile("global_load_dwordx4 %0, %1, %2" : "=&v"(s.are) : "v"(off), "s"(p.Are) : "memory");
  asm volatile("global_load_dwordx4 %0, %1, %2" : "=&v"(s.aim) : "v"(off), "s"(p.Aim) : "memory");
  asm volatile("global_load_dwordx4 %0, %1, %2" : "=&v"(s.bre) : "v"(off), "s"(p.Bre) : "memory");
  asm volatile("global_load_dwordx4 %0, %1, %2" : "=&v"(s.bim) : "v"(off), "s"(p.Bim) : "memory");
  asm volatile("global_load_dwordx4 %0, %1, %2" : "=&v"(s.cre) : "v"(off), "s"(p.Cre) : "memory");
  asm volatile("global_load_dwordx4 %0, %1, %2" : "=&v"(s.cim) : "v"(off), "s"(p.Cim) : "memory");
}
#define SSM_WAIT(n, s) SSM_WAIT_(n, s)
#define SSM_WAIT_(n, s) asm volatile("s_waitcnt vmcnt(" #n ")" : "+v"(s.are), "+v"(s.aim), "+v"(s.bre), "+v"(s.bim), "+v"(s.cre), "+v"(s.cim)::"memory")

DEV void pair_sync_at(uint32_t mine) {
  const uint32_t other = mine ^ 8u;
  asm volatile("s_waitcnt lgkmcnt(0)\n\tds_write_b32 %0, %1" ::"v"(mine), "v"(1u) : "memory");
  for (unsigned spins = 0; spins < (1u << 24); ++spins) {
    uint32_t x;
    asm volatile("ds_read_b32 %0, %1\n\ts_waitcnt lgkmcnt(0)" : "=v"(x) : "v"(other) : "memory");
    if (__builtin_amdgcn_readfirstlane(x) == 1u) break;
  }
}

struct KgFrag { u32x4 ah[3], al[3], bh, bl; };
template <int H>
DEV void kgen_half(const SsmPar& sp, float dt, float p0, float p1, float p2, float q, KgFrag& f, f32x4 (&acc)[3]) {
  constexpr int W = H & 3, JH = H & 1;
  uint32_t wa = sp.are[W], wi = sp.aim[W], wbr = sp.bre[W], wbi = sp.bim[W], wcr = sp.cre[W], wci = sp.cim[W];
  asm volatile("" : "+v"(wa), "+v"(wi), "+v"(wbr), "+v"(wbi), "+v"(wcr), "+v"(wci));
#pragma unroll
  for (int jj = 0; jj < 2; ++jj) {
    const int j = 2 * JH + jj;
    const float ar = dt * (jj ? bfhi(wa) : bflo(wa)), ai = dt * (jj ? bfhi(wi) : bflo(wi));
    const float br = dt * (jj ? bfhi(wbr) : bflo(wbr)), bi = dt * (jj ? bfhi(wbi) : bflo(wbi));
    const float cr = jj ? bfhi(wcr) : bflo(wcr), ci = jj ? bfhi(wci) : bflo(wci);
    const float a2 = ar * 1.44269504088896340736f, wv = ai * 0.15915494309189533577f;
    const float xr = cr * br - ci * bi, xi = cr * bi + ci * br;
    {
      const float e = __builtin_amdgcn_exp2f(a2 * q), r = __builtin_amdgcn_fractf(wv * q);
      const float s = __builtin_amdgcn_sinf(r) * e, co = __builtin_amdgcn_cosf(r) * e;
      const float vr = xr * co - xi * s, vi = -(xr * s + xi * co);
      const uint32_t hi = pk2(vr, vi);
      f.bh[j] = hi;
      f.bl[j] = pk2(vr - bflo(hi), vi - bfhi(hi));
    }
#pragma unroll
    for (int T = 0; T < 3; ++T) {
      const float pp = T == 0 ? p0 : (T == 1 ? p1 : p2);
      const float e = __builtin_amdgcn_exp2f(a2 * pp), r = __builtin_amdgcn_fractf(wv * pp);
      const float wr = __builtin_amdgcn_cosf(r) * e, wim = __builtin_amdgcn_sinf(r) * e;
      const uint32_t hi = pk2(wr, wim);
      f.ah[T][j] = hi;
      f.al[T][j] = pk2(wr - bflo(hi), wim - bfhi(hi));
    }
  }
  if (JH == 1) {
#pragma unroll
    for (int T = 0; T < 3; ++T) {
      acc[T] = mfma16(f.al[T], f.bh, acc[T]);
      acc[T] = mfma16(f.ah[T], f.bl, acc[T]);
      acc[T] = mfma16(f.ah[T], f.bh, acc[T]);
    }
    asm volatile("" : "+v"(acc[0]), "+v"(acc[1]), "+v"(acc[2]));
  } else {
    asm volatile("" : "+v"(f.ah[0]), "+v"(f.ah[1]), "+v"(f.ah[2]), "+v"(f.al[0]), "+v"(f.al[1]), "+v"(f.al[2]), "+v"(f.bh), "+v"(f.bl));
  }
  __builtin_amdgcn_sched_barrier(0);
}

#define KTS(i)
DEV void kgen_waves(const P0& p, char* scratch, int b, bool aux, int tid TS_ARG) {
  const int t = tid - 512, kw = __builtin_amdgcn_readfirstlane(t >> 6), lane = t & 63, i = lane & 15, ch0 = 2 * b, cc = kw >> 1;
#define KPRIO 3
#define KPRIO2 KPRIO
#define NPRE 3
  __builtin_amdgcn_s_setprio(KPRIO);
  SsmPar sp0, sp1;
  uint32_t ld2, ds2;
  asm volatile("global_load_dword %0, %2, %3\n\tglobal_load_dword %1, %2, %4"
               : "=&v"(ld2), "=&v"(ds2) : "v"((uint32_t)b * 4u), "s"(p.logdt), "s"(p.Dskip) : "memory");
  const uint32_t off = (uint32_t)((ch0 + cc) * NM + 64 * (kw & 1) + 8 * (lane >> 4)) * 2u;
  ssm_load(p, off, sp0);
  ssm_load(p, off + 64u, sp1);
  __builtin_amdgcn_sched_barrier(0);
  BAR();
  __builtin_amdgcn_sched_barrier(0);
  SSM_WAIT(6, sp0);
  asm volatile("" : "+v"(ld2), "+v"(ds2));
  const float dt = rbf(__expf(cc ? bfhi(ld2) : bflo(ld2)));
  const float p0 = (float)((i & 7) + 128 * (i >> 3));
  const float p1 = (i < 8) ? (float)(256 + 2 * (i & 3) + 128 * (i >> 2)) : (float)(512 + 4 * (i & 1) + 128 * ((i - 8) >> 1));
  const float p2 = (i < 8) ? (float)(1024 + 128 * i) : 2048.f;
  const float q = (float)(8 * i);
  f32x4 acc[3] = {{0, 0, 0, 0}, {0, 0, 0, 0}, {0, 0, 0, 0}};
  KgFrag f;
#pragma unroll
  for (int T = 0; T < 3; ++T) f.ah[T] = f.al[T] = (u32x4){0, 0, 0, 0};
  f.bh = f.bl = (u32x4){0, 0, 0, 0};
  TS(1);
  if (0 == NPRE || (0 > NPRE && !aux)) BAR();
  kgen_half<0>(sp0, dt, p0, p1, p2, q, f, acc);
  KTS(3);
  if (1 == NPRE || (1 > NPRE && !aux)) BAR();
  kgen_half<1>(sp0, dt, p0, p1, p2, q, f, acc);
  KTS(4);
  if (2 == NPRE || (2 > NPRE && !aux)) BAR();
  kgen_half<2>(sp0, dt, p0, p1, p2, q, f, acc);
  KTS(5);
  if (3 == NPRE || (3 > NPRE && !aux)) BAR();
  kgen_half<3>(sp0, dt, p0, p1, p2, q, f, acc);
  KTS(6);
  if (4 == NPRE || (4 > NPRE && !aux)) BAR();
  SSM_WAIT(0, sp1);
  kgen_half<4>(sp1, dt, p0, p1, p2, q, f, acc);
  KTS(7);
  if (5 == NPRE || (5 > NPRE && !aux)) BAR();
  kgen_half<5>(sp1, dt, p0, p1, p2, q, f, acc);
  KTS(8);
  if (6 == NPRE || (6 > NPRE && !aux)) BAR();
  kgen_half<6>(sp1, dt, p0, p1, p2, q, f, acc);
  KTS(9);
  if (7 == NPRE || (7 > NPRE && !aux)) BAR();
  kgen_half<7>(sp1, dt, p0, p1, p2, q, f, acc);
  float* sc = (float*)scratch;
  u16* kc = (u16*)(scratch + 12288) + cc * 768;
#pragma unroll
  for (int T = 0; T < 3; ++T) *(f32x4*)(sc + ((kw * 3 + T) * 64 + lane) * 4) = acc[T];
  WAIT_L(0);
  TS(15);
  BAR();
  const int tt = t & 127;
#pragma unroll
  for (int T = 0; T < 3; ++T) {
    if (NPRE >= 4 && T == 2 && !aux) BAR();
#pragma unroll
    for (int hf = 0; hf < 2; ++hf) {
      const int e = hf * 128 + tt, ln = e >> 2, j = e & 3;
      const int ii = 4 * (ln >> 4) + j, bb = ln & 15;
      const float s = sc[((cc * 2) * 3 + T) * 256 + e] + sc[((cc * 2 + 1) * 3 + T) * 256 + e];
      int idx;
      if (T == 0) idx = (ii & 7) + 128 * (ii >> 3) + 8 * bb;
      else if (T == 1) idx = ii < 8 ? 256 + (ii & 3) + 64 * (ii >> 2) + 4 * bb : 384 + (ii & 1) + 32 * ((ii - 8) >> 1) + 2 * bb;
      else idx = ii < 8 ? 512 + 16 * ii + bb : ((ii == 8 && bb == 0) ? 640 : 700);
      kc[idx] = (u16)(pk2(s, 0.f) & 0xffffu);
    }
    __builtin_amdgcn_sched_barrier(0);
  }
  WAIT_L(0);
  BAR();
  if ((t & 127) == 0) {
    const uint32_t dv = cc ? ds2 >> 16 : ds2 & 0xffffu;
    __builtin_amdgcn_raw_buffer_store_b32(dv, __builtin_amdgcn_make_buffer_rsrc(p.E, 0, 0x7fffffff, 0x00020000), (uint32_t)((ch0 + cc) * ES * 2), 0, 16);
  }
#pragma unroll
  for (int hf = 0; hf < 2; ++hf) {
    if (NPRE >= 3 && hf == 1 && !aux) BAR();
    int l0 = 1024 * hf + 8 * tt;
    asm volatile("" : "+v"(l0));
    u32x4 o;
#pragma unroll
    for (int e = 0; e < 4; ++e) {
      const int la = l0 + 2 * e, lb = la + 1;
      const uint32_t pb = pk2((float)la, (float)lb);
      const int ia = la < 128 ? la : (int)(pb & 0xffffu) - 17024, ib = lb < 128 ? lb : (int)(pb >> 16) - 17024;
      o[e] = (uint32_t)kc[ia] | ((uint32_t)kc[ib] << 16);
    }
    st16<WT_AUX>(p.E, ((size_t)(ch0 + cc) * ES + 16 + l0) * 2, o);
    __builtin_amdgcn_sched_barrier(0);
  }
  st16<WT_AUX>(p.yT, ((size_t)b * 4096 + t * 8) * 2, (u32x4){~0u, ~0u, ~0u, ~0u});
  st16<WT_AUX>(p.yT, ((size_t)b * 4096 + 2048 + t * 8) * 2, (u32x4){~0u, ~0u, ~0u, ~0u});
  TS(14);
  if (!aux)
    for (int i_ = 0; i_ < NPRE - 2 - (NPRE >= 4) - (NPRE >= 3); ++i_) BAR();
}

DEV void k0_glds(const P0& p, uint32_t lb, const uint32_t (&voff)[3], int m0, int f0, int k0, int piece) {
  if (piece < 2) glds16(p.u + (size_t)m0 * D + k0, voff[0] + piece * (8 * D * 2), lb + piece * 1024);
  else if (piece < 4) glds16(p.wg + (size_t)k0 * F + f0, voff[piece - 1], lb + 16384 + (piece - 2) * 1024);
  else glds16(p.wu + (size_t)k0 * F + f0, voff[piece - 3], lb + 32768 + (piece - 4) * 1024);
}

__global__ __launch_bounds__(768, 3) void k0_kernel(P0 p) {
  extern __shared__ __attribute__((aligned(16))) char smem[];
  const int tid = threadIdx.x, lane = tid & 63, w = __builtin_amdgcn_readfirstlane(tid >> 6);
  const int b = blockIdx.x, xcd = b & 7, idx = b >> 3;
  TSDECL;
  TS(0);
  if (w >= 8) {
    kgen_waves(p, smem + 3 * STAGE_B, b, idx >= 28, tid TS_PASS);
  } else if (idx < 28) {
    const int mt = (xcd >> 1) * 4 + idx / 7, ft = (xcd & 1) * 7 + idx % 7;
    const int m0 = mt * 128, f0 = ft * 128;
#define K0ROT 2
    const int rot = K0ROT == 0 ? 2 * (idx / 7) + idx % 7 : K0ROT == 1 ? 0 : K0ROT == 2 ? (idx & 1) * 4 : K0ROT == 3 ? 2 * (idx / 7) : idx % 7;
    uint32_t voff[3];
    {
      const int row = 16 * w + (lane >> 3);
      voff[0] = (uint32_t)(row * D + 8 * ((lane & 7) ^ (row & 7))) * 2u;
      const int r0 = 8 * w + (lane >> 4), r1 = r0 + 4;
      voff[1] = (uint32_t)(r0 * F + 8 * ((lane & 15) ^ wswz(r0))) * 2u;
      voff[2] = (uint32_t)(r1 * F + 8 * ((lane & 15) ^ wswz(r1))) * 2u;
    }
    const uint32_t lb = lds_addr(smem) + 2048 * w;
#pragma unroll
    for (int s = 0; s < 2; ++s) {
      if (s == 1) BAR();
#pragma unroll
      for (int pc = 0; pc < 6; ++pc) k0_glds(p, lb + s * STAGE_B, voff, m0, f0, 64 * ((rot + s) & 7), pc);
    }
    const int wm = w >> 2, wn = w & 3, g = lane >> 4, q = (lane & 15) >> 2, pp = lane & 3;
    f32x4 acc[2][2][4];
#pragma unroll
    for (int a = 0; a < 2; ++a)
#pragma unroll
      for (int c = 0; c < 2; ++c)
#pragma unroll
        for (int m = 0; m < 4; ++m) acc[a][c][m] = (f32x4){0, 0, 0, 0};
    u32x4 xf[2][4], wf[2][2][2];
    auto frags = [&](const char* buf, int ss) {
#pragma unroll
      for (int m = 0; m < 4; ++m) {
        const int row = 64 * wm + 16 * m + (lane & 15), c = 4 * ss + g;
        xf[ss][m] = *(const u32x4*)(buf + row * 128 + ((c ^ (row & 7)) << 4));
      }
#pragma unroll
      for (int a = 0; a < 2; ++a)
#pragma unroll
        for (int c = 0; c < 2; ++c) {
          const int ch = 4 * wn + 2 * c + (pp >> 1);
          const int r0 = 32 * ss + 8 * g + q, r1 = r0 + 4;
          const char* wb = buf + 16384 + a * 16384 + 8 * (pp & 1);
          wf[ss][a][c] = tr_read2(wb + 256 * r0 + 16 * (ch ^ wswz(r0)), wb + 256 * r1 + 16 * (ch ^ wswz(r1)));
        }
    };
    auto mm = [&](int ss, bool stage, int sb, int k0, int pc0) {
#pragma unroll
      for (int ac = 0; ac < 4; ++ac) {
#pragma unroll
        for (int m = 0; m < 4; ++m) acc[ac >> 1][ac & 1][m] = mfma16(wf[ss][ac >> 1][ac & 1], xf[ss][m], acc[ac >> 1][ac & 1][m]);
        if (stage && ac < 3) {
          __builtin_amdgcn_sched_barrier(0);
          k0_glds(p, lb + sb * STAGE_B, voff, m0, f0, k0, pc0 + ac);
          __builtin_amdgcn_sched_barrier(0);
        }
      }
    };
#pragma unroll
    for (int s = 0; s < 8; ++s) {
      if (s == 7) WAIT_V(0); else WAIT_V(6);
      WAIT_L(0);
      BAR();
      TS(3 + s);
      if (s == 0) {
#pragma unroll
        for (int pc = 0; pc < 6; ++pc) k0_glds(p, lb + 2 * STAGE_B, voff, m0, f0, 64 * ((rot + 2) & 7), pc);
      }
      int bo = (s % 3) * STAGE_B;
      asm volatile("" : "+s"(bo));
      const char* buf = smem + bo;
      const bool st = (s >= 1 && s + 2 < 8);
      const int k0 = 64 * ((rot + s + 2) & 7);
      frags(buf, 0);
      __builtin_amdgcn_sched_barrier(0);
      if (s > 0) mm(1, st, (s + 2) % 3, k0, 0);
      __builtin_amdgcn_sched_barrier(0);
      frags(buf, 1);
      __builtin_amdgcn_sched_barrier(0);
      mm(0, st, (s + 2) % 3, k0, 3);
      __builtin_amdgcn_sched_barrier(0);
    }
    mm(1, false, 0, 0, 0);
    TS(11);
#pragma unroll
    for (int m = 0; m < 4; ++m) {
      u32x4 o;
#pragma unroll
      for (int c = 0; c < 2; ++c)
#pragma unroll
        for (int jj = 0; jj < 2; ++jj) {
          const uint32_t g2 = pk2(acc[0][c][m][2 * jj], acc[0][c][m][2 * jj + 1]), u2 = pk2(acc[1][c][m][2 * jj], acc[1][c][m][2 * jj + 1]);
          const f32x2 gg = {bflo(g2), bfhi(g2)};
          const f32x2 tt = gg * -1.44269504088896340736f;
          const f32x2 dn = (f32x2){__builtin_amdgcn_exp2f(tt[0]), __builtin_amdgcn_exp2f(tt[1])} + 1.f;
          const f32x2 sv = gg * (f32x2){__builtin_amdgcn_rcpf(dn[0]), __builtin_amdgcn_rcpf(dn[1])};
          const uint32_t s2 = pk2(sv[0], sv[1]);
          const f32x2 hv = (f32x2){bflo(s2), bfhi(s2)} * (f32x2){bflo(u2), bfhi(u2)};
          o[2 * c + jj] = pk2(hv[0], hv[1]);
        }
      const int mt64 = (m0 >> 6) + wm, ks = 4 * ft + wn;
      st16<WT_H>(p.h, ((size_t)((mt64 * KS2 + ks) * 4 + m) * 64 + lane) * 16, o);
    }
    TS(12);
  } else {
    BAR();
    const int a = xcd * 4 + (idx - 28);
    constexpr int RS = 1056;
    const int t0 = 64 * a;
    u32x4 uv[8], wv[8];
#pragma unroll
    for (int r = 0; r < 8; ++r) uv[r] = *(const u32x4*)(p.u + (size_t)(t0 + 8 * w + r) * D + lane * 8);
    const int ks2 = a + 32 < KS2 ? a + 32 : a;
#pragma unroll
    for (int r = 0; r < 4; ++r) {
      wv[r] = *(const u32x4*)(p.wd + (size_t)(32 * a + 4 * w + r) * D + lane * 8);
      wv[4 + r] = *(const u32x4*)(p.wd + (size_t)(32 * ks2 + 4 * w + r) * D + lane * 8);
    }
    const u32x4 gwv = *(const u32x4*)(p.gw + lane * 8);
    {
      float s[8];
#pragma unroll
      for (int r = 0; r < 8; ++r) {
        *(u32x4*)(smem + (8 * w + r) * RS + lane * 16) = uv[r];
        s[r] = 0.f;
#pragma unroll
        for (int e = 0; e < 4; ++e) {
          const uint32_t pr = pk2(bflo(uv[r][e]) * bflo(gwv[e]), bfhi(uv[r][e]) * bfhi(gwv[e]));
          s[r] += bflo(pr) + bfhi(pr);
        }
      }
#pragma unroll
      for (int r = 0; r < 4; ++r) { const bool hi = lane & 32; const float keep = hi ? s[r + 4] : s[r], send = hi ? s[r] : s[r + 4]; s[r] = keep + __shfl_xor(send, 32); }
#pragma unroll
      for (int r = 0; r < 2; ++r) { const bool hi = lane & 16; const float keep = hi ? s[r + 2] : s[r], send = hi ? s[r] : s[r + 2]; s[r] = keep + __shfl_xor(send, 16); }
      { const bool hi = lane & 8; const float keep = hi ? s[1] : s[0], send = hi ? s[0] : s[1]; s[0] = keep + __shfl_xor(send, 8); }
      s[0] += __shfl_xor(s[0], 4); s[0] += __shfl_xor(s[0], 2); s[0] += __shfl_xor(s[0], 1);
      if ((lane & 7) == 0) {
        const int r = ((lane >> 5) & 1) * 4 + ((lane >> 4) & 1) * 2 + ((lane >> 3) & 1);
        const float gs = rbf(s[0]);
        p.g[t0 + 8 * w + r] = rbf(1.f / (1.f + __expf(-gs)));
      }
    }
    WAIT_L(0);
    BAR();
    const int g = lane >> 4, q = (lane & 15) >> 2, pp = lane & 3;
#pragma unroll
    for (int c = 0; c < 4; ++c)
#pragma unroll
      for (int hh = 0; hh < 2; ++hh) {
        const int dt_ = 4 * w + c;
        const int r0 = 32 * hh + 8 * g + q;
        const char* base = smem + (16 * dt_ + 4 * pp) * 2;
        const u32x4 v = tr_read2(base + r0 * RS, base + (r0 + 4) * RS);
        st16<WT_AUX>(p.uT, ((size_t)(16 * dt_ + (lane & 15)) * L + t0 + 32 * hh + 8 * g) * 2, v);
      }
    TS(3);
    WAIT_L(0);
    BAR();
#pragma unroll
    for (int r = 0; r < 4; ++r) {
      *(u32x4*)(smem + (4 * w + r) * RS + lane * 16) = wv[r];
      *(u32x4*)(smem + (32 + 4 * w + r) * RS + lane * 16) = wv[4 + r];
    }
    WAIT_L(0);
    BAR();
#pragma unroll
    for (int hh = 0; hh < 2; ++hh) {
      if (hh == 0 || a + 32 < KS2) {
        const int ks = a + 32 * hh;
#pragma unroll
        for (int c = 0; c < 4; ++c) {
          const int nt = 4 * w + c;
          const char* base = smem + 32 * hh * RS + (16 * nt + 4 * pp) * 2;
          const u32x4 v = tr_read2(base + (4 * g + q) * RS, base + (16 + 4 * g + q) * RS);
          st16<WT_AUX>(p.wdt, ((size_t)(((nt >> 2) * KS2 + ks) * 4 + (nt & 3)) * 64 + lane) * 16, v);
        }
      }
    }
    TS(12);
  }
  TSDUMP(0);
}

#define GO_STEP 7
#define FPRIO 1
#define LPRIO 0
struct P1 {
  const u16 *u, *Dskip, *h, *wdt, *uT, *E;
  const float* g;
  u16 *yT, *out;
};

constexpr int CP_STRIDE = 4160;
constexpr int CW_U = 4 * CP_STRIDE, CW_SIZE = CW_U + 4608;
constexpr int K1_XCH = 2 * CW_SIZE;
constexpr int K1_YOFF = K1_XCH + 16384;
constexpr int K1_RED = K1_YOFF + 64 * 144;
constexpr int K1_FLAG = K1_RED + 49152;
constexpr int K1_LDS = K1_FLAG + 64;

DEV void conv_loop(const char* abase, const char* bbase, f32x4 (&y)[8]) {
  u32x4 af[2][8], bf[2][8];
  auto ld = [&](int buf, int it) {
#pragma unroll
    for (int m = 0; m < 8; ++m) bf[buf][m] = *(const u32x4*)(bbase + 512 * m - 64 * it);
#pragma unroll
    for (int m = 0; m < 8; ++m) {
      const char* ap = abase + 512 * m + 64 * it;
      af[buf][m] = tr_read2(ap, ap - 8);
    }
  };
  ld(0, 0);
#pragma unroll
  for (int it = 0; it < 4; ++it) {
    const int cur = it & 1;
    __builtin_amdgcn_sched_barrier(0);
    if (it < 3) ld(cur ^ 1, it + 1);
#pragma unroll
    for (int m = 0; m < 8; ++m)
#pragma unroll
      for (int T = m; T < 8; ++T) y[T] = mfma16(af[cur][m], bf[cur][T - m], y[T]);
    if (it < 3) {
#pragma unroll
      for (int i = 0; i < 12; ++i) {
        __builtin_amdgcn_sched_group_barrier(0x008, 3, 0);
        __builtin_amdgcn_sched_group_barrier(0x100, 2, 0);
      }
    }
  }
  __builtin_amdgcn_sched_barrier(0);
}
DEV void pair_sync(char* smem, int cw, int which) { pair_sync_at(lds_addr(smem + K1_FLAG + cw * 8 + which * 4)); }

__global__ __launch_bounds__(512, 2) void k1_kernel(P1 p) {
  extern __shared__ __attribute__((aligned(16))) char smem[];
  const int tid = threadIdx.x, lane = tid & 63, w = __builtin_amdgcn_readfirstlane(tid >> 6);
  const int b = blockIdx.x, xcd = b & 7, idx = b >> 3;
  const int mt = (xcd >> 1) * 8 + (idx >> 2), nt = (xcd & 1) * 4 + (idx & 3);
  const int t0 = mt * 64, d0 = nt * 64;
  const int g = lane >> 4, n = lane & 15;
  TSDECL;
  TS(0);
  if (w >= 4) __builtin_amdgcn_s_setprio(3);
  if (w == 4) {
    asm volatile("ds_write_b32 %0, %1\n\ts_waitcnt lgkmcnt(0)" ::"v"(lds_addr(smem + K1_FLAG + (lane & 15) * 4)), "v"(0u) : "memory");
  }
  BAR();
  __builtin_amdgcn_sched_barrier(0);
  if (w < 4) {
    __builtin_amdgcn_s_setprio(FPRIO);
    f32x4 acc[4][4];
#pragma unroll
    for (int a = 0; a < 4; ++a)
#pragma unroll
      for (int c = 0; c < 4; ++c) acc[a][c] = (f32x4){0, 0, 0, 0};
    const u16* hp = p.h + ((size_t)mt * KS2 + 14 * w) * 2048 + lane * 8;
    const u16* wp = p.wdt + ((size_t)nt * KS2 + 14 * w) * 2048 + lane * 8;
#define PFD 4
    constexpr int PF = PFD;
    u32x4 hf[PF][4], wf[PF][4];
    const int hrot = w * 512;
#pragma unroll
    for (int s = 0; s < PF; ++s)
#pragma unroll
      for (int a = 0; a < 4; ++a) {
        hf[s][a] = *(const u32x4*)(hp + s * 2048 + ((a * 512 + hrot) & 2047));
        wf[s][a] = *(const u32x4*)(wp + s * 2048 + a * 512);
      }
    const int t = t0 + 16 * w + n;
    float gt;
    __builtin_amdgcn_sched_barrier(0);
    TS(15);
    __builtin_amdgcn_sched_barrier(0);
#pragma unroll
    for (int s = 0; s < 14; ++s) {
#pragma unroll
      for (int a = 0; a < 4; ++a)
#pragma unroll
        for (int c = 0; c < 4; ++c) acc[a][c] = mfma16(wf[s % PF][a], hf[s % PF][c], acc[a][c]);
      __builtin_amdgcn_sched_barrier(0);
      if (s + PF < 14) {
#pragma unroll
        for (int a = 0; a < 4; ++a) {
          hf[s % PF][a] = *(const u32x4*)(hp + (s + PF) * 2048 + ((a * 512 + hrot) & 2047));
          wf[s % PF][a] = *(const u32x4*)(wp + (s + PF) * 2048 + a * 512);
        }
      }
      if (s == GO_STEP) asm volatile("ds_write_b32 %0, %1" ::"v"(lds_addr(smem + K1_FLAG + 32 + w * 4)), "v"(1u) : "memory");
      if (s + PF == 14) gt = p.g[t];
      __builtin_amdgcn_sched_barrier(0);
    }
    TS(1);
#pragma unroll
    for (int c = 1; c < 4; ++c)
#pragma unroll
      for (int a = 0; a < 4; ++a) *(f32x4*)(smem + K1_RED + ((((w + c) & 3) * 3 + (c - 1)) * 4 + a) * 1024 + lane * 16) = acc[a][c];
    WAIT_L(0);
    BAR();
    TS(7);
    const float omg = rbf(1.f - gt);
    const int q = n >> 2, pp = lane & 3;
#pragma unroll
    for (int a = 0; a < 4; ++a) {
      f32x4 bs = acc[a][0];
#pragma unroll
      for (int s = 0; s < 3; ++s) bs += *(const f32x4*)(smem + K1_RED + ((w * 3 + s) * 4 + a) * 1024 + lane * 16);
      const u32x2 yv = tr_read(smem + K1_YOFF + (16 * a + 4 * g + q) * 144 + (16 * w + 4 * pp) * 2);
      u32x2 o;
#pragma unroll
      for (int e = 0; e < 2; ++e) {
        const uint32_t ga = pk2(gt * bflo(yv[e]), gt * bfhi(yv[e]));
        const uint32_t bb = pk2(bs[2 * e], bs[2 * e + 1]);
        const uint32_t gb = pk2(omg * bflo(bb), omg * bfhi(bb));
        o[e] = pk2(bflo(ga) + bflo(gb), bfhi(ga) + bfhi(gb));
      }
      *(u32x2*)(p.out + (size_t)t * D + d0 + 16 * a + 4 * g) = o;
    }
    TS(9);
  } else {
    const int cw = w - 4, c = cw >> 1, half = cw & 1, d = 2 * b + c;
    char* base = smem + c * CW_SIZE;
#define CSLEEP 0
    if (CSLEEP) __builtin_amdgcn_s_sleep(CSLEEP);
    const float dsk = bflo(*(const uint32_t*)(p.E + (size_t)d * ES));
    {
      const u16* eg = p.E + (size_t)d * ES;
      u32x4 cur[3];
      u32x2 prv[3];
#pragma unroll
      for (int it = 0; it < 3; ++it) {
        const int un = it < 2 ? (2 * half + it) * 64 + lane : 256 + (lane & 1);
        asm volatile("global_load_dwordx4 %0, %2, %3\n\tglobal_load_dwordx2 %1, %2, %3 offset:-8"
                     : "=&v"(cur[it]), "=&v"(prv[it]) : "v"(un * 16u), "s"(eg) : "memory");
      }
      const uint32_t lb = lds_addr(base + CW_U + 512) + half * 2048;
#pragma unroll
      for (int i = 0; i < 2; ++i) glds16(p.uT + (size_t)d * L + (2 * half + i) * 512, lane * 16u, lb + i * 1024);
      if (lane < 32 && half == 0) *(u32x4*)(base + CW_U + lane * 16) = (u32x4){0, 0, 0, 0};
      asm volatile("s_waitcnt vmcnt(2)" : "+v"(cur[0]), "+v"(cur[1]), "+v"(cur[2]), "+v"(prv[0]), "+v"(prv[1]), "+v"(prv[2])::"memory");
      TS(2);
      if (half == 0) {
        if (lane < 2) cur[0] = (u32x4){0, 0, 0, 0};
        if (lane < 3) prv[0] = (u32x2){0, 0};
      }
#pragma unroll
      for (int it = 0; it < 3; ++it) {
        const int un = it < 2 ? (2 * half + it) * 64 + lane : 256 + (lane & 1);
        const u32x4 cu = cur[it];
        const u32x2 pv = prv[it];
        u32x4 o1, o2, o3;
        o1[0] = __builtin_amdgcn_alignbyte(cu[0], pv[1], 2); o1[1] = __builtin_amdgcn_alignbyte(cu[1], cu[0], 2);
        o1[2] = __builtin_amdgcn_alignbyte(cu[2], cu[1], 2); o1[3] = __builtin_amdgcn_alignbyte(cu[3], cu[2], 2);
        o2[0] = pv[1]; o2[1] = cu[0]; o2[2] = cu[1]; o2[3] = cu[2];
        o3[0] = __builtin_amdgcn_alignbyte(pv[1], pv[0], 2); o3[1] = o1[0]; o3[2] = o1[1]; o3[3] = o1[2];
        if (it < 2 || (lane < 2 && half == 1)) {
          *(u32x4*)(base + 0 * CP_STRIDE + un * 16) = cu;
          *(u32x4*)(base + 1 * CP_STRIDE + un * 16) = o1;
          *(u32x4*)(base + 2 * CP_STRIDE + un * 16) = o2;
          *(u32x4*)(base + 3 * CP_STRIDE + un * 16) = o3;
        }
      }
    }
    WAIT_V(0);
    pair_sync(smem, cw, 0);
    __builtin_amdgcn_s_setprio(LPRIO);
    TS(3);
    f32x4 y[8];
#pragma unroll
    for (int a = 0; a < 8; ++a) y[a] = (f32x4){0, 0, 0, 0};
    {
      const int q = n >> 2, pp = lane & 3;
      const char* abase = base + q * CP_STRIDE + (16 - 8 * g + 4 * pp) * 2 + 32 * (8 * half + 1);
      const char* bbase = base + CW_U + 512 + (16 * n + 8 * g) * 2 - 32 * (8 * half + 1);
      conv_loop(abase, bbase, y);
    }
    TS(4);
    __builtin_amdgcn_s_setprio(3);
#pragma unroll
    for (int a = 0; a < 4; ++a) *(f32x4*)(smem + K1_XCH + (cw * 4 + a) * 1024 + lane * 16) = half ? y[a] : y[4 + a];
    pair_sync(smem, cw, 1);
    TS(8);
#pragma unroll
    for (int a = 0; a < 4; ++a) {
      const f32x4 o = *(const f32x4*)(smem + K1_XCH + ((cw ^ 1) * 4 + a) * 1024 + lane * 16);
      const f32x4 v = half ? o + y[4 + a] : y[a] + o;
      const int ntile = 4 * half + a;
      uint32_t v0 = pk2(v[0], v[1]), v1 = pk2(v[2], v[3]);
      {
        const u32x2 uu = *(const u32x2*)(base + CW_U + 512 + (256 * ntile + 16 * n + 4 * g) * 2);
        const uint32_t s0 = pk2(bflo(uu[0]) * dsk, bfhi(uu[0]) * dsk), s1 = pk2(bflo(uu[1]) * dsk, bfhi(uu[1]) * dsk);
        v0 = pk2(bflo(v0) + bflo(s0), bfhi(v0) + bfhi(s0));
        v1 = pk2(bflo(v1) + bflo(s1), bfhi(v1) + bfhi(s1));
      }
      v0 = v0 == ~0u ? 0xfffffffeu : v0;
      v1 = v1 == ~0u ? 0xfffffffeu : v1;
      __hip_atomic_store((gu64*)(p.yT + (size_t)d * L + 256 * ntile + 16 * n + 4 * g), (unsigned long long)v0 | ((unsigned long long)v1 << 32), RLX_AGENT);
    }
    __builtin_amdgcn_s_setprio(0);
    TS(5);
    {
      const int dd = 16 * cw + (lane >> 2), ch = lane & 3;
      const uint32_t off = (uint32_t)((d0 + dd) * L + t0 + ch * 8) * 2u;
      u32x4 v0, v1;
      for (unsigned spins = 0; spins < (1u << 22); ++spins) {
        uint32_t x;
        asm volatile("ds_read_b32 %0, %1\n\ts_waitcnt lgkmcnt(0)" : "=v"(x) : "v"(lds_addr(smem + K1_FLAG + 32 + cw * 4)) : "memory");
        if (__builtin_amdgcn_readfirstlane(x) == 1u) break;
        __builtin_amdgcn_s_sleep(2);
      }
      for (unsigned spins = 0; spins < (1u << 20); ++spins) {
        asm volatile("global_load_dwordx4 %0, %2, %3 sc1\n\tglobal_load_dwordx4 %1, %2, %3 offset:64 sc1\n\ts_waitcnt vmcnt(0)"
                     : "=&v"(v0), "=&v"(v1) : "v"(off), "s"(p.yT) : "memory");
        const bool ok = v0[0] != ~0u && v0[1] != ~0u && v0[2] != ~0u && v0[3] != ~0u && v1[0] != ~0u && v1[1] != ~0u && v1[2] != ~0u && v1[3] != ~0u;
        if (__all(ok)) break;
        __builtin_amdgcn_s_sleep(4);
      }
      *(u32x4*)(smem + K1_YOFF + dd * 144 + ch * 16) = v0;
      *(u32x4*)(smem + K1_YOFF + dd * 144 + ch * 16 + 64) = v1;
    }
    TS(6);
    WAIT_L(0);
    BAR();
  }
  TSDUMP(1);
}

extern "C" void kernel_launch(void* const* d_in, const int* in_sizes, int n_in, void* d_out, int out_size, void* d_ws, size_t ws_size,
                              hipStream_t stream) {
  char* ws = (char*)d_ws;
  P0 p0;
  p0.u = (const u16*)d_in[0]; p0.Are = (const u16*)d_in[1]; p0.Aim = (const u16*)d_in[2]; p0.Bre = (const u16*)d_in[3];
  p0.Bim = (const u16*)d_in[4]; p0.Cre = (const u16*)d_in[5]; p0.Cim = (const u16*)d_in[6]; p0.logdt = (const u16*)d_in[7];
  p0.wg = (const u16*)d_in[10]; p0.wu = (const u16*)d_in[11]; p0.wd = (const u16*)d_in[12]; p0.gw = (const u16*)d_in[13];
  p0.h = (u16*)(ws + WS_H); p0.wdt = (u16*)(ws + WS_WDT); p0.uT = (u16*)(ws + WS_UT); p0.E = (u16*)(ws + WS_E);
  p0.yT = (u16*)(ws + WS_YT); p0.g = (float*)(ws + WS_G);
  p0.Dskip = (const u16*)d_in[8];
  P1 p1;
  p1.u = p0.u; p1.Dskip = (const u16*)d_in[8]; p1.h = p0.h; p1.wdt = p0.wdt; p1.uT = p0.uT; p1.E = p0.E; p1.g = p0.g;
  p1.yT = p0.yT; p1.out = (u16*)d_out;
  (void)hipFuncSetAttribute((const void*)k0_kernel, hipFuncAttributeMaxDynamicSharedMemorySize, K0_LDS);
  (void)hipFuncSetAttribute((const void*)k1_kernel, hipFuncAttributeMaxDynamicSharedMemorySize, K1_LDS);
  k0_kernel<<<256, 768, K0_LDS, stream>>>(p0);
  k1_kernel<<<256, 512, K1_LDS, stream>>>(p1);
}
```

```cpp
#include <hip/hip_runtime.h>
#include <hip/hip_bf16.h>
#include <stdint.h>

typedef unsigned short u16;
typedef __attribute__((ext_vector_type(4))) short s16x4;
typedef __attribute__((ext_vector_type(8))) short s16x8;
typedef __attribute__((ext_vector_type(8))) __bf16 bf16x8;
typedef __attribute__((ext_vector_type(2))) __bf16 bf16x2;
typedef __attribute__((ext_vector_type(2))) float f32x2;
typedef __attribute__((ext_vector_type(4))) float f32x4;
typedef __attribute__((ext_vector_type(4))) unsigned u32x4;
typedef __attribute__((ext_vector_type(2))) unsigned u32x2;
typedef __attribute__((address_space(3))) s16x4* lds_s16x4_ptr;
typedef __attribute__((address_space(1))) unsigned gu32;
typedef __attribute__((address_space(1))) unsigned long long gu64;
#define RLX_AGENT __ATOMIC_RELAXED, __HIP_MEMORY_SCOPE_AGENT
#define DEV __device__ __forceinline__

constexpr int L = 2048, D = 512, NM = 128, F = 1792;
constexpr int KS2 = F / 32;

constexpr size_t WS_H = 0;
constexpr size_t WS_WDT = WS_H + (size_t)L * F * 2;
constexpr size_t WS_UT = WS_WDT + (size_t)F * D * 2;
constexpr size_t WS_YT = WS_UT + (size_t)L * D * 2;
constexpr int ES = 2080;
constexpr size_t WS_E = WS_YT + (size_t)L * D * 2 + 128;
constexpr size_t WS_G = WS_E + (size_t)D * ES * 2;

#define TSDECL
#define TS(ph)
#define TSDUMP(K)
#define TS_ARG
#define TS_PASS
DEV float bf2f(u16 v) { return __uint_as_float(((uint32_t)v) << 16); }
DEV float bflo(uint32_t v) { return __uint_as_float(v << 16); }
DEV float bfhi(uint32_t v) { return __uint_as_float(v & 0xffff0000u); }
DEV uint32_t pk2(float a, float b) { f32x2 v = {a, b}; return __builtin_bit_cast(uint32_t, __builtin_convertvector(v, bf16x2)); }
DEV float rbf(float a) { return bflo(pk2(a, 0.f) & 0xffffu); }
DEV f32x4 mfma16(u32x4 a, u32x4 b, f32x4 c) {
  return __builtin_amdgcn_mfma_f32_16x16x32_bf16(__builtin_bit_cast(bf16x8, a), __builtin_bit_cast(bf16x8, b), c, 0, 0, 0);
}
DEV u32x2 tr_read(const char* p) {
  return __builtin_bit_cast(u32x2, __builtin_amdgcn_ds_read_tr16_b64_v4i16((lds_s16x4_ptr)p));
}
DEV u32x4 tr_read2(const char* p0, const char* p1) {
  u32x2 a = tr_read(p0), b = tr_read(p1);
  u32x4 r = {a.x, a.y, b.x, b.y};
  return r;
}
DEV uint32_t lds_addr(const void* p) { return (uint32_t)(uintptr_t)(__attribute__((address_space(3))) const char*)p; }
DEV void glds16(const void* sbase, uint32_t voff, uint32_t lds) {
  unsigned keep;
  asm volatile("s_mov_b32 %0, m0\n\ts_mov_b32 m0, %3\n\ts_nop 0\n\tglobal_load_lds_dwordx4 %1, %2\n\ts_mov_b32 m0, %0"
               : "=&s"(keep) : "v"(voff), "s"(sbase), "s"(lds) : "memory");
}
#define WAIT_V(n) asm volatile("s_waitcnt vmcnt(" #n ")" ::: "memory")
#define WAIT_L(n) asm volatile("s_waitcnt lgkmcnt(" #n ")" ::: "memory")
#define BAR() __builtin_amdgcn_s_barrier()

template <bool WT>
DEV void st16(void* base, size_t byte_off, u32x4 v) {
  if (WT) {
    __builtin_amdgcn_raw_buffer_store_b128(v, __builtin_amdgcn_make_buffer_rsrc(base, 0, 0x7fffffff, 0x00020000), (uint32_t)byte_off, 0, 16);
  } else {
    *(u32x4*)((char*)base + byte_off) = v;
  }
}
#define WT_AUX 1
#define WT_H 1

struct P0 {
  const u16 *u, *Are, *Aim, *Bre, *Bim, *Cre, *Cim, *logdt, *wg, *wu, *wd, *gw, *Dskip;
  u16 *h, *wdt, *uT, *E, *yT;
  float* g;
};

constexpr int STAGE_B = 49152;
constexpr int K0_LDS = 3 * STAGE_B + 15360;

DEV int wswz(int row) { return ((row & 3) << 2) | ((row >> 2) & 3); }

struct SsmPar { u32x4 are, aim, bre, bim, cre, cim; };

DEV void ssm_load(const P0& p, uint32_t off, SsmPar& s) {
  asm volatile("global_load_dwordx4 %0, %1, %2" : "=&v"(s.are) : "v"(off), "s"(p.Are) : "memory");
  asm volatile("global_load_dwordx4 %0, %1, %2" : "=&v"(s.aim) : "v"(off), "s"(p.Aim) : "memory");
  asm volatile("global_load_dwordx4 %0, %1, %2" : "=&v"(s.bre) : "v"(off), "s"(p.Bre) : "memory");
  asm volatile("global_load_dwordx4 %0, %1, %2" : "=&v"(s.bim) : "v"(off), "s"(p.Bim) : "memory");
  asm volatile("global_load_dwordx4 %0, %1, %2" : "=&v"(s.cre) : "v"(off), "s"(p.Cre) : "memory");
  asm volatile("global_load_dwordx4 %0, %1, %2" : "=&v"(s.cim) : "v"(off), "s"(p.Cim) : "memory");
}
#define SSM_WAIT(n, s) SSM_WAIT_(n, s)
#define SSM_WAIT_(n, s) asm volatile("s_waitcnt vmcnt(" #n ")" : "+v"(s.are), "+v"(s.aim), "+v"(s.bre), "+v"(s.bim), "+v"(s.cre), "+v"(s.cim)::"memory")

DEV void pair_sync_at(uint32_t mine) {
  const uint32_t other = mine ^ 8u;
  asm volatile("s_waitcnt lgkmcnt(0)\n\tds_write_b32 %0, %1" ::"v"(mine), "v"(1u) : "memory");
  for (unsigned spins = 0; spins < (1u << 24); ++spins) {
    uint32_t x;
    asm volatile("ds_read_b32 %0, %1\n\ts_waitcnt lgkmcnt(0)" : "=v"(x) : "v"(other) : "memory");
    if (__builtin_amdgcn_readfirstlane(x) == 1u) break;
  }
}

struct KgFrag { u32x4 ah[3], al[3], bh, bl; };
template <int H>
DEV void kgen_half(const SsmPar& sp, float dt, float p0, float p1, float p2, float q, KgFrag& f, f32x4 (&acc)[3]) {
  constexpr int W = H & 3, JH = H & 1;
  uint32_t wa = sp.are[W], wi = sp.aim[W], wbr = sp.bre[W], wbi = sp.bim[W], wcr = sp.cre[W], wci = sp.cim[W];
  asm volatile("" : "+v"(wa), "+v"(wi), "+v"(wbr), "+v"(wbi), "+v"(wcr), "+v"(wci));
#pragma unroll
  for (int jj = 0; jj < 2; ++jj) {
    const int j = 2 * JH + jj;
    const float ar = dt * (jj ? bfhi(wa) : bflo(wa)), ai = dt * (jj ? bfhi(wi) : bflo(wi));
    const float br = dt * (jj ? bfhi(wbr) : bflo(wbr)), bi = dt * (jj ? bfhi(wbi) : bflo(wbi));
    const float cr = jj ? bfhi(wcr) : bflo(wcr), ci = jj ? bfhi(wci) : bflo(wci);
    const float a2 = ar * 1.44269504088896340736f, wv = ai * 0.15915494309189533577f;
    const float xr = cr * br - ci * bi, xi = cr * bi + ci * br;
    {
      const float e = __builtin_amdgcn_exp2f(a2 * q), r = __builtin_amdgcn_fractf(wv * q);
      const float s = __builtin_amdgcn_sinf(r) * e, co = __builtin_amdgcn_cosf(r) * e;
      const float vr = xr * co - xi * s, vi = -(xr * s + xi * co);
      const uint32_t hi = pk2(vr, vi);
      f.bh[j] = hi;
      f.bl[j] = pk2(vr - bflo(hi), vi - bfhi(hi));
    }
#pragma unroll
    for (int T = 0; T < 3; ++T) {
      const float pp = T == 0 ? p0 : (T == 1 ? p1 : p2);
      const float e = __builtin_amdgcn_exp2f(a2 * pp), r = __builtin_amdgcn_fractf(wv * pp);
      const float wr = __builtin_amdgcn_cosf(r) * e, wim = __builtin_amdgcn_sinf(r) * e;
      const uint32_t hi = pk2(wr, wim);
      f.ah[T][j] = hi;
      f.al[T][j] = pk2(wr - bflo(hi), wim - bfhi(hi));
    }
  }
  if (JH == 1) {
#pragma unroll
    for (int T = 0; T < 3; ++T) {
      acc[T] = mfma16(f.al[T], f.bh, acc[T]);
      acc[T] = mfma16(f.ah[T], f.bl, acc[T]);
      acc[T] = mfma16(f.ah[T], f.bh, acc[T]);
    }
    asm volatile("" : "+v"(acc[0]), "+v"(acc[1]), "+v"(acc[2]));
  } else {
    asm volatile("" : "+v"(f.ah[0]), "+v"(f.ah[1]), "+v"(f.ah[2]), "+v"(f.al[0]), "+v"(f.al[1]), "+v"(f.al[2]), "+v"(f.bh), "+v"(f.bl));
  }
  __builtin_amdgcn_sched_barrier(0);
}

#define KTS(i)
DEV void kgen_waves(const P0& p, char* scratch, int b, bool aux, int tid TS_ARG) {
  const int t = tid - 512, kw = __builtin_amdgcn_readfirstlane(t >> 6), lane = t & 63, i = lane & 15, ch0 = 2 * b, cc = kw >> 1;
#define KPRIO 3
#define KPRIO2 KPRIO
#define NPRE 3
  __builtin_amdgcn_s_setprio(KPRIO);
  SsmPar sp0, sp1;
  uint32_t ld2, ds2;
  asm volatile("global_load_dword %0, %2, %3\n\tglobal_load_dword %1, %2, %4"
               : "=&v"(ld2), "=&v"(ds2) : "v"((uint32_t)b * 4u), "s"(p.logdt), "s"(p.Dskip) : "memory");
  const uint32_t off = (uint32_t)((ch0 + cc) * NM + 64 * (kw & 1) + 8 * (lane >> 4)) * 2u;
  ssm_load(p, off, sp0);
  ssm_load(p, off + 64u, sp1);
  __builtin_amdgcn_sched_barrier(0);
  BAR();
  __builtin_amdgcn_sched_barrier(0);
  SSM_WAIT(6, sp0);
  asm volatile("" : "+v"(ld2), "+v"(ds2));
  const float dt = rbf(__expf(cc ? bfhi(ld2) : bflo(ld2)));
  const float p0 = (float)((i & 7) + 128 * (i >> 3));
  const float p1 = (i < 8) ? (float)(256 + 2 * (i & 3) + 128 * (i >> 2)) : (float)(512 + 4 * (i & 1) + 128 * ((i - 8) >> 1));
  const float p2 = (i < 8) ? (float)(1024 + 128 * i) : 2048.f;
  const float q = (float)(8 * i);
  f32x4 acc[3] = {{0, 0, 0, 0}, {0, 0, 0, 0}, {0, 0, 0, 0}};
  KgFrag f;
#pragma unroll
  for (int T = 0; T < 3; ++T) f.ah[T] = f.al[T] = (u32x4){0, 0, 0, 0};
  f.bh = f.bl = (u32x4){0, 0, 0, 0};
  TS(1);
  if (0 == NPRE || (0 > NPRE && !aux)) BAR();
  kgen_half<0>(sp0, dt, p0, p1, p2, q, f, acc);
  KTS(3);
  if (1 == NPRE || (1 > NPRE && !aux)) BAR();
  kgen_half<1>(sp0, dt, p0, p1, p2, q, f, acc);
  KTS(4);
  if (2 == NPRE || (2 > NPRE && !aux)) BAR();
  kgen_half<2>(sp0, dt, p0, p1, p2, q, f, acc);
  KTS(5);
  if (3 == NPRE || (3 > NPRE && !aux)) BAR();
  kgen_half<3>(sp0, dt, p0, p1, p2, q, f, acc);
  KTS(6);
  if (4 == NPRE || (4 > NPRE && !aux)) BAR();
  SSM_WAIT(0, sp1);
  kgen_half<4>(sp1, dt, p0, p1, p2, q, f, acc);
  KTS(7);
  if (5 == NPRE || (5 > NPRE && !aux)) BAR();
  kgen_half<5>(sp1, dt, p0, p1, p2, q, f, acc);
  KTS(8);
  if (6 == NPRE || (6 > NPRE && !aux)) BAR();
  kgen_half<6>(sp1, dt, p0, p1, p2, q, f, acc);
  KTS(9);
  if (7 == NPRE || (7 > NPRE && !aux)) BAR();
  kgen_half<7>(sp1, dt, p0, p1, p2, q, f, acc);
  float* sc = (float*)scratch;
  u16* kc = (u16*)(scratch + 12288) + cc * 768;
#pragma unroll
  for (int T = 0; T < 3; ++T) *(f32x4*)(sc + ((kw * 3 + T) * 64 + lane) * 4) = acc[T];
  WAIT_L(0);
  TS(15);
  BAR();
  const int tt = t & 127;
#pragma unroll
  for (int T = 0; T < 3; ++T) {
    if (NPRE >= 4 && T == 2 && !aux) BAR();
#pragma unroll
    for (int hf = 0; hf < 2; ++hf) {
      const int e = hf * 128 + tt, ln = e >> 2, j = e & 3;
      const int ii = 4 * (ln >> 4) + j, bb = ln & 15;
      const float s = sc[((cc * 2) * 3 + T) * 256 + e] + sc[((cc * 2 + 1) * 3 + T) * 256 + e];
      int idx;
      if (T == 0) idx = (ii & 7) + 128 * (ii >> 3) + 8 * bb;
      else if (T == 1) idx = ii < 8 ? 256 + (ii & 3) + 64 * (ii >> 2) + 4 * bb : 384 + (ii & 1) + 32 * ((ii - 8) >> 1) + 2 * bb;
      else idx = ii < 8 ? 512 + 16 * ii + bb : ((ii == 8 && bb == 0) ? 640 : 700);
      kc[idx] = (u16)(pk2(s, 0.f) & 0xffffu);
    }
    __builtin_amdgcn_sched_barrier(0);
  }
  WAIT_L(0);
  BAR();
  if ((t & 127) == 0) {
    const uint32_t dv = cc ? ds2 >> 16 : ds2 & 0xffffu;
    __builtin_amdgcn_raw_buffer_store_b32(dv, __builtin_amdgcn_make_buffer_rsrc(p.E, 0, 0x7fffffff, 0x00020000), (uint32_t)((ch0 + cc) * ES * 2), 0, 16);
  }
#pragma unroll
  for (int hf = 0; hf < 2; ++hf) {
    if (NPRE >= 3 && hf == 1 && !aux) BAR();
    int l0 = 1024 * hf + 8 * tt;
    asm volatile("" : "+v"(l0));
    u32x4 o;
#pragma unroll
    for (int e = 0; e < 4; ++e) {
      const int la = l0 + 2 * e, lb = la + 1;
      const uint32_t pb = pk2((float)la, (float)lb);
      const int ia = la < 128 ? la : (int)(pb & 0xffffu) - 17024, ib = lb < 128 ? lb : (int)(pb >> 16) - 17024;
      o[e] = (uint32_t)kc[ia] | ((uint32_t)kc[ib] << 16);
    }
    st16<WT_AUX>(p.E, ((size_t)(ch0 + cc) * ES + 16 + l0) * 2, o);
    __builtin_amdgcn_sched_barrier(0);
  }
  st16<WT_AUX>(p.yT, ((size_t)b * 4096 + t * 8) * 2, (u32x4){~0u, ~0u, ~0u, ~0u});
  st16<WT_AUX>(p.yT, ((size_t)b * 4096 + 2048 + t * 8) * 2, (u32x4){~0u, ~0u, ~0u, ~0u});
  TS(14);
  if (!aux)
    for (int i_ = 0; i_ < NPRE - 2 - (NPRE >= 4) - (NPRE >= 3); ++i_) BAR();
}

DEV void k0_glds(const P0& p, uint32_t lb, const uint32_t (&voff)[3], int m0, int f0, int k0, int piece) {
  if (piece < 2) glds16(p.u + (size_t)m0 * D + k0, voff[0] + piece * (8 * D * 2), lb + piece * 1024);
  else if (piece < 4) glds16(p.wg + (size_t)k0 * F + f0, voff[piece - 1], lb + 16384 + (piece - 2) * 1024);
  else glds16(p.wu + (size_t)k0 * F + f0, voff[piece - 3], lb + 32768 + (piece - 4) * 1024);
}

__global__ __launch_bounds__(768, 3) void k0_kernel(P0 p) {
  extern __shared__ __attribute__((aligned(16))) char smem[];
  const int tid = threadIdx.x, lane = tid & 63, w = __builtin_amdgcn_readfirstlane(tid >> 6);
  const int b = blockIdx.x, xcd = b & 7, idx = b >> 3;
  TSDECL;
  TS(0);
  if (w >= 8) {
    kgen_waves(p, smem + 3 * STAGE_B, b, idx >= 28, tid TS_PASS);
  } else if (idx < 28) {
    const int mt = (xcd >> 1) * 4 + idx / 7, ft = (xcd & 1) * 7 + idx % 7;
    const int m0 = mt * 128, f0 = ft * 128;
#define K0ROT 2
    const int rot = K0ROT == 0 ? 2 * (idx / 7) + idx % 7 : K0ROT == 1 ? 0 : K0ROT == 2 ? (idx & 1) * 4 : K0ROT == 3 ? 2 * (idx / 7) : idx % 7;
    uint32_t voff[3];
    {
      const int row = 16 * w + (lane >> 3);
      voff[0] = (uint32_t)(row * D + 8 * ((lane & 7) ^ (row & 7))) * 2u;
      const int r0 = 8 * w + (lane >> 4), r1 = r0 + 4;
      voff[1] = (uint32_t)(r0 * F + 8 * ((lane & 15) ^ wswz(r0))) * 2u;
      voff[2] = (uint32_t)(r1 * F + 8 * ((lane & 15) ^ wswz(r1))) * 2u;
    }
    const uint32_t lb = lds_addr(smem) + 2048 * w;
#pragma unroll
    for (int s = 0; s < 3; ++s) {
      if (s == 1) BAR();
#pragma unroll
      for (int pc = 0; pc < 6; ++pc) k0_glds(p, lb + s * STAGE_B, voff, m0, f0, 64 * ((rot + s) & 7), pc);
    }
    const int wm = w >> 2, wn = w & 3, g = lane >> 4, q = (lane & 15) >> 2, pp = lane & 3;
    f32x4 acc[2][2][4];
#pragma unroll
    for (int a = 0; a < 2; ++a)
#pragma unroll
      for (int c = 0; c < 2; ++c)
#pragma unroll
        for (int m = 0; m < 4; ++m) acc[a][c][m] = (f32x4){0, 0, 0, 0};
    u32x4 xf[2][4], wf[2][2][2];
    auto frags = [&](const char* buf, int ss) {
#pragma unroll
      for (int m = 0; m < 4; ++m) {
        const int row = 64 * wm + 16 * m + (lane & 15), c = 4 * ss + g;
        xf[ss][m] = *(const u32x4*)(buf + row * 128 + ((c ^ (row & 7)) << 4));
      }
#pragma unroll
      for (int a = 0; a < 2; ++a)
#pragma unroll
        for (int c = 0; c < 2; ++c) {
          const int ch = 4 * wn + 2 * c + (pp >> 1);
          const int r0 = 32 * ss + 8 * g + q, r1 = r0 + 4;
          const char* wb = buf + 16384 + a * 16384 + 8 * (pp & 1);
          wf[ss][a][c] = tr_read2(wb + 256 * r0 + 16 * (ch ^ wswz(r0)), wb + 256 * r1 + 16 * (ch ^ wswz(r1)));
        }
    };
    auto mm = [&](int ss, bool stage, int sb, int k0, int pc0) {
#pragma unroll
      for (int ac = 0; ac < 4; ++ac) {
#pragma unroll
        for (int m = 0; m < 4; ++m) acc[ac >> 1][ac & 1][m] = mfma16(wf[ss][ac >> 1][ac & 1], xf[ss][m], acc[ac >> 1][ac & 1][m]);
        if (stage && ac < 3) {
          __builtin_amdgcn_sched_barrier(0);
          k0_glds(p, lb + sb * STAGE_B, voff, m0, f0, k0, pc0 + ac);
          __builtin_amdgcn_sched_barrier(0);
        }
      }
    };
#pragma unroll
    for (int s = 0; s < 8; ++s) {
      if (s == 7) WAIT_V(0); else if (s == 0) WAIT_V(12); else WAIT_V(6);
      WAIT_L(0);
      BAR();
      TS(3 + s);
      int bo = (s % 3) * STAGE_B;
      asm volatile("" : "+s"(bo));
      const char* buf = smem + bo;
      const bool st = (s >= 1 && s + 2 < 8);
      const int k0 = 64 * ((rot + s + 2) & 7);
      frags(buf, 0);
      __builtin_amdgcn_sched_barrier(0);
      if (s > 0) mm(1, st, (s + 2) % 3, k0, 0);
      __builtin_amdgcn_sched_barrier(0);
      frags(buf, 1);
      __builtin_amdgcn_sched_barrier(0);
      mm(0, st, (s + 2) % 3, k0, 3);
      __builtin_amdgcn_sched_barrier(0);
    }
    mm(1, false, 0, 0, 0);
    TS(11);
#pragma unroll
    for (int m = 0; m < 4; ++m) {
      u32x4 o;
#pragma unroll
      for (int c = 0; c < 2; ++c)
#pragma unroll
        for (int jj = 0; jj < 2; ++jj) {
          const uint32_t g2 = pk2(acc[0][c][m][2 * jj], acc[0][c][m][2 * jj + 1]), u2 = pk2(acc[1][c][m][2 * jj], acc[1][c][m][2 * jj + 1]);
          const f32x2 gg = {bflo(g2), bfhi(g2)};
          const f32x2 tt = gg * -1.44269504088896340736f;
          const f32x2 dn = (f32x2){__builtin_amdgcn_exp2f(tt[0]), __builtin_amdgcn_exp2f(tt[1])} + 1.f;
          const f32x2 sv = gg * (f32x2){__builtin_amdgcn_rcpf(dn[0]), __builtin_amdgcn_rcpf(dn[1])};
          const uint32_t s2 = pk2(sv[0], sv[1]);
          const f32x2 hv = (f32x2){bflo(s2), bfhi(s2)} * (f32x2){bflo(u2), bfhi(u2)};
          o[2 * c + jj] = pk2(hv[0], hv[1]);
        }
      const int mt64 = (m0 >> 6) + wm, ks = 4 * ft + wn;
      st16<WT_H>(p.h, ((size_t)((mt64 * KS2 + ks) * 4 + m) * 64 + lane) * 16, o);
    }
    TS(12);
  } else {
    BAR();
    const int a = xcd * 4 + (idx - 28);
    constexpr int RS = 1056;
    const int t0 = 64 * a;
    u32x4 uv[8], wv[8];
#pragma unroll
    for (int r = 0; r < 8; ++r) uv[r] = *(const u32x4*)(p.u + (size_t)(t0 + 8 * w + r) * D + lane * 8);
    const int ks2 = a + 32 < KS2 ? a + 32 : a;
#pragma unroll
    for (int r = 0; r < 4; ++r) {
      wv[r] = *(const u32x4*)(p.wd + (size_t)(32 * a + 4 * w + r) * D + lane * 8);
      wv[4 + r] = *(const u32x4*)(p.wd + (size_t)(32 * ks2 + 4 * w + r) * D + lane * 8);
    }
    const u32x4 gwv = *(const u32x4*)(p.gw + lane * 8);
    {
      float s[8];
#pragma unroll
      for (int r = 0; r < 8; ++r) {
        *(u32x4*)(smem + (8 * w + r) * RS + lane * 16) = uv[r];
        s[r] = 0.f;
#pragma unroll
        for (int e = 0; e < 4; ++e) {
          const uint32_t pr = pk2(bflo(uv[r][e]) * bflo(gwv[e]), bfhi(uv[r][e]) * bfhi(gwv[e]));
          s[r] += bflo(pr) + bfhi(pr);
        }
      }
#pragma unroll
      for (int r = 0; r < 4; ++r) { const bool hi = lane & 32; const float keep = hi ? s[r + 4] : s[r], send = hi ? s[r] : s[r + 4]; s[r] = keep + __shfl_xor(send, 32); }
#pragma unroll
      for (int r = 0; r < 2; ++r) { const bool hi = lane & 16; const float keep = hi ? s[r + 2] : s[r], send = hi ? s[r] : s[r + 2]; s[r] = keep + __shfl_xor(send, 16); }
      { const bool hi = lane & 8; const float keep = hi ? s[1] : s[0], send = hi ? s[0] : s[1]; s[0] = keep + __shfl_xor(send, 8); }
      s[0] += __shfl_xor(s[0], 4); s[0] += __shfl_xor(s[0], 2); s[0] += __shfl_xor(s[0], 1);
      if ((lane & 7) == 0) {
        const int r = ((lane >> 5) & 1) * 4 + ((lane >> 4) & 1) * 2 + ((lane >> 3) & 1);
        const float gs = rbf(s[0]);
        p.g[t0 + 8 * w + r] = rbf(1.f / (1.f + __expf(-gs)));
      }
    }
    WAIT_L(0);
    BAR();
    const int g = lane >> 4, q = (lane & 15) >> 2, pp = lane & 3;
#pragma unroll
    for (int c = 0; c < 4; ++c)
#pragma unroll
      for (int hh = 0; hh < 2; ++hh) {
        const int dt_ = 4 * w + c;
        const int r0 = 32 * hh + 8 * g + q;
        const char* base = smem + (16 * dt_ + 4 * pp) * 2;
        const u32x4 v = tr_read2(base + r0 * RS, base + (r0 + 4) * RS);
        st16<WT_AUX>(p.uT, ((size_t)(16 * dt_ + (lane & 15)) * L + t0 + 32 * hh + 8 * g) * 2, v);
      }
    TS(3);
    WAIT_L(0);
    BAR();
#pragma unroll
    for (int r = 0; r < 4; ++r) {
      *(u32x4*)(smem + (4 * w + r) * RS + lane * 16) = wv[r];
      *(u32x4*)(smem + (32 + 4 * w + r) * RS + lane * 16) = wv[4 + r];
    }
    WAIT_L(0);
    BAR();
#pragma unroll
    for (int hh = 0; hh < 2; ++hh) {
      if (hh == 0 || a + 32 < KS2) {
        const int ks = a + 32 * hh;
#pragma unroll
        for (int c = 0; c < 4; ++c) {
          const int nt = 4 * w + c;
          const char* base = smem + 32 * hh * RS + (16 * nt + 4 * pp) * 2;
          const u32x4 v = tr_read2(base + (4 * g + q) * RS, base + (16 + 4 * g + q) * RS);
          st16<WT_AUX>(p.wdt, ((size_t)(((nt >> 2) * KS2 + ks) * 4 + (nt & 3)) * 64 + lane) * 16, v);
        }
      }
    }
    TS(12);
  }
  TSDUMP(0);
}

#define GO_STEP 7
#define FPRIO 1
#define LPRIO 0
struct P1 {
  const u16 *u, *Dskip, *h, *wdt, *uT, *E;
  const float* g;
  u16 *yT, *out;
};

constexpr int CP_STRIDE = 4160;
constexpr int CW_U = 4 * CP_STRIDE, CW_SIZE = CW_U + 4608;
constexpr int K1_XCH = 2 * CW_SIZE;
constexpr int K1_YOFF = K1_XCH + 16384;
constexpr int K1_RED = K1_YOFF + 64 * 144;
constexpr int K1_FLAG = K1_RED + 49152;
constexpr int K1_LDS = K1_FLAG + 64;

DEV void conv_loop(const char* abase, const char* bbase, f32x4 (&y)[8]) {
  u32x4 af[2][8], bf[2][8];
  auto ld = [&](int buf, int it) {
#pragma unroll
    for (int m = 0; m < 8; ++m) bf[buf][m] = *(const u32x4*)(bbase + 512 * m - 64 * it);
#pragma unroll
    for (int m = 0; m < 8; ++m) {
      const char* ap = abase + 512 * m + 64 * it;
      af[buf][m] = tr_read2(ap, ap - 8);
    }
  };
  ld(0, 0);
#pragma unroll
  for (int it = 0; it < 4; ++it) {
    const int cur = it & 1;
    __builtin_amdgcn_sched_barrier(0);
    if (it < 3) ld(cur ^ 1, it + 1);
#pragma unroll
    for (int m = 0; m < 8; ++m)
#pragma unroll
      for (int T = m; T < 8; ++T) y[T] = mfma16(af[cur][m], bf[cur][T - m], y[T]);
    if (it < 3) {
#pragma unroll
      for (int i = 0; i < 12; ++i) {
        __builtin_amdgcn_sched_group_barrier(0x008, 3, 0);
        __builtin_amdgcn_sched_group_barrier(0x100, 2, 0);
      }
    }
  }
  __builtin_amdgcn_sched_barrier(0);
}
DEV void pair_sync(char* smem, int cw, int which) { pair_sync_at(lds_addr(smem + K1_FLAG + cw * 8 + which * 4)); }

__global__ __launch_bounds__(512, 2) void k1_kernel(P1 p) {
  extern __shared__ __attribute__((aligned(16))) char smem[];
  const int tid = threadIdx.x, lane = tid & 63, w = __builtin_amdgcn_readfirstlane(tid >> 6);
  const int b = blockIdx.x, xcd = b & 7, idx = b >> 3;
  const int mt = (xcd >> 1) * 8 + (idx >> 2), nt = (xcd & 1) * 4 + (idx & 3);
  const int t0 = mt * 64, d0 = nt * 64;
  const int g = lane >> 4, n = lane & 15;
  TSDECL;
  TS(0);
  if (w >= 4) __builtin_amdgcn_s_setprio(3);
  if (w == 4) {
    asm volatile("ds_write_b32 %0, %1\n\ts_waitcnt lgkmcnt(0)" ::"v"(lds_addr(smem + K1_FLAG + (lane & 15) * 4)), "v"(0u) : "memory");
  }
  BAR();
  __builtin_amdgcn_sched_barrier(0);
  if (w < 4) {
    __builtin_amdgcn_s_setprio(FPRIO);
    f32x4 acc[4][4];
#pragma unroll
    for (int a = 0; a < 4; ++a)
#pragma unroll
      for (int c = 0; c < 4; ++c) acc[a][c] = (f32x4){0, 0, 0, 0};
    const u16* hp = p.h + ((size_t)mt * KS2 + 14 * w) * 2048 + lane * 8;
    const u16* wp = p.wdt + ((size_t)nt * KS2 + 14 * w) * 2048 + lane * 8;
#define PFD 4
    constexpr int PF = PFD;
    u32x4 hf[PF][4], wf[PF][4];
    const int hrot = w * 512;
#pragma unroll
    for (int s = 0; s < PF; ++s)
#pragma unroll
      for (int a = 0; a < 4; ++a) {
        hf[s][a] = *(const u32x4*)(hp + s * 2048 + ((a * 512 + hrot) & 2047));
        wf[s][a] = *(const u32x4*)(wp + s * 2048 + a * 512);
      }
    const int t = t0 + 16 * w + n;
    float gt;
    __builtin_amdgcn_sched_barrier(0);
    TS(15);
    __builtin_amdgcn_sched_barrier(0);
#pragma unroll
    for (int s = 0; s < 14; ++s) {
#pragma unroll
      for (int a = 0; a < 4; ++a)
#pragma unroll
        for (int c = 0; c < 4; ++c) acc[a][c] = mfma16(wf[s % PF][a], hf[s % PF][c], acc[a][c]);
      __builtin_amdgcn_sched_barrier(0);
      if (s + PF < 14) {
#pragma unroll
        for (int a = 0; a < 4; ++a) {
          hf[s % PF][a] = *(const u32x4*)(hp + (s + PF) * 2048 + ((a * 512 + hrot) & 2047));
          wf[s % PF][a] = *(const u32x4*)(wp + (s + PF) * 2048 + a * 512);
        }
      }
      if (s == GO_STEP) asm volatile("ds_write_b32 %0, %1" ::"v"(lds_addr(smem + K1_FLAG + 32 + w * 4)), "v"(1u) : "memory");
      if (s + PF == 14) gt = p.g[t];
      __builtin_amdgcn_sched_barrier(0);
    }
    TS(1);
#pragma unroll
    for (int c = 1; c < 4; ++c)
#pragma unroll
      for (int a = 0; a < 4; ++a) *(f32x4*)(smem + K1_RED + ((((w + c) & 3) * 3 + (c - 1)) * 4 + a) * 1024 + lane * 16) = acc[a][c];
    WAIT_L(0);
    BAR();
    TS(7);
    const float omg = rbf(1.f - gt);
    const int q = n >> 2, pp = lane & 3;
#pragma unroll
    for (int a = 0; a < 4; ++a) {
      f32x4 bs = acc[a][0];
#pragma unroll
      for (int s = 0; s < 3; ++s) bs += *(const f32x4*)(smem + K1_RED + ((w * 3 + s) * 4 + a) * 1024 + lane * 16);
      const u32x2 yv = tr_read(smem + K1_YOFF + (16 * a + 4 * g + q) * 144 + (16 * w + 4 * pp) * 2);
      u32x2 o;
#pragma unroll
      for (int e = 0; e < 2; ++e) {
        const uint32_t ga = pk2(gt * bflo(yv[e]), gt * bfhi(yv[e]));
        const uint32_t bb = pk2(bs[2 * e], bs[2 * e + 1]);
        const uint32_t gb = pk2(omg * bflo(bb), omg * bfhi(bb));
        o[e] = pk2(bflo(ga) + bflo(gb), bfhi(ga) + bfhi(gb));
      }
      *(u32x2*)(p.out + (size_t)t * D + d0 + 16 * a + 4 * g) = o;
    }
    TS(9);
  } else {
    const int cw = w - 4, c = cw >> 1, half = cw & 1, d = 2 * b + c;
    char* base = smem + c * CW_SIZE;
#define CSLEEP 0
    if (CSLEEP) __builtin_amdgcn_s_sleep(CSLEEP);
    const float dsk = bflo(*(const uint32_t*)(p.E + (size_t)d * ES));
    {
      const u16* eg = p.E + (size_t)d * ES;
      u32x4 cur[3];
      u32x2 prv[3];
#pragma unroll
      for (int it = 0; it < 3; ++it) {
        const int un = it < 2 ? (2 * half + it) * 64 + lane : 256 + (lane & 1);
        asm volatile("global_load_dwordx4 %0, %2, %3\n\tglobal_load_dwordx2 %1, %2, %3 offset:-8"
                     : "=&v"(cur[it]), "=&v"(prv[it]) : "v"(un * 16u), "s"(eg) : "memory");
      }
      const uint32_t lb = lds_addr(base + CW_U + 512) + half * 2048;
#pragma unroll
      for (int i = 0; i < 2; ++i) glds16(p.uT + (size_t)d * L + (2 * half + i) * 512, lane * 16u, lb + i * 1024);
      if (lane < 32 && half == 0) *(u32x4*)(base + CW_U + lane * 16) = (u32x4){0, 0, 0, 0};
      asm volatile("s_waitcnt vmcnt(2)" : "+v"(cur[0]), "+v"(cur[1]), "+v"(cur[2]), "+v"(prv[0]), "+v"(prv[1]), "+v"(prv[2])::"memory");
      TS(2);
      if (half == 0) {
        if (lane < 2) cur[0] = (u32x4){0, 0, 0, 0};
        if (lane < 3) prv[0] = (u32x2){0, 0};
      }
#pragma unroll
      for (int it = 0; it < 3; ++it) {
        const int un = it < 2 ? (2 * half + it) * 64 + lane : 256 + (lane & 1);
        const u32x4 cu = cur[it];
        const u32x2 pv = prv[it];
        u32x4 o1, o2, o3;
        o1[0] = __builtin_amdgcn_alignbyte(cu[0], pv[1], 2); o1[1] = __builtin_amdgcn_alignbyte(cu[1], cu[0], 2);
        o1[2] = __builtin_amdgcn_alignbyte(cu[2], cu[1], 2); o1[3] = __builtin_amdgcn_alignbyte(cu[3], cu[2], 2);
        o2[0] = pv[1]; o2[1] = cu[0]; o2[2] = cu[1]; o2[3] = cu[2];
        o3[0] = __builtin_amdgcn_alignbyte(pv[1], pv[0], 2); o3[1] = o1[0]; o3[2] = o1[1]; o3[3] = o1[2];
        if (it < 2 || (lane < 2 && half == 1)) {
          *(u32x4*)(base + 0 * CP_STRIDE + un * 16) = cu;
          *(u32x4*)(base + 1 * CP_STRIDE + un * 16) = o1;
          *(u32x4*)(base + 2 * CP_STRIDE + un * 16) = o2;
          *(u32x4*)(base + 3 * CP_STRIDE + un * 16) = o3;
        }
      }
    }
    WAIT_V(0);
    pair_sync(smem, cw, 0);
    __builtin_amdgcn_s_setprio(LPRIO);
    TS(3);
    f32x4 y[8];
#pragma unroll
    for (int a = 0; a < 8; ++a) y[a] = (f32x4){0, 0, 0, 0};
    {
      const int q = n >> 2, pp = lane & 3;
      const char* abase = base + q * CP_STRIDE + (16 - 8 * g + 4 * pp) * 2 + 32 * (8 * half + 1);
      const char* bbase = base + CW_U + 512 + (16 * n + 8 * g) * 2 - 32 * (8 * half + 1);
      conv_loop(abase, bbase, y);
    }
    TS(4);
    __builtin_amdgcn_s_setprio(3);
#pragma unroll
    for (int a = 0; a < 4; ++a) *(f32x4*)(smem + K1_XCH + (cw * 4 + a) * 1024 + lane * 16) = half ? y[a] : y[4 + a];
    pair_sync(smem, cw, 1);
    TS(8);
#pragma unroll
    for (int a = 0; a < 4; ++a) {
      const f32x4 o = *(const f32x4*)(smem + K1_XCH + ((cw ^ 1) * 4 + a) * 1024 + lane * 16);
      const f32x4 v = half ? o + y[4 + a] : y[a] + o;
      const int ntile = 4 * half + a;
      uint32_t v0 = pk2(v[0], v[1]), v1 = pk2(v[2], v[3]);
      {
        const u32x2 uu = *(const u32x2*)(base + CW_U + 512 + (256 * ntile + 16 * n + 4 * g) * 2);
        const uint32_t s0 = pk2(bflo(uu[0]) * dsk, bfhi(uu[0]) * dsk), s1 = pk2(bflo(uu[1]) * dsk, bfhi(uu[1]) * dsk);
        v0 = pk2(bflo(v0) + bflo(s0), bfhi(v0) + bfhi(s0));
        v1 = pk2(bflo(v1) + bflo(s1), bfhi(v1) + bfhi(s1));
      }
      v0 = v0 == ~0u ? 0xfffffffeu : v0;
      v1 = v1 == ~0u ? 0xfffffffeu : v1;
      __hip_atomic_store((gu64*)(p.yT + (size_t)d * L + 256 * ntile + 16 * n + 4 * g), (unsigned long long)v0 | ((unsigned long long)v1 << 32), RLX_AGENT);
    }
    __builtin_amdgcn_s_setprio(0);
    TS(5);
    {
      const int dd = 16 * cw + (lane >> 2), ch = lane & 3;
      const uint32_t off = (uint32_t)((d0 + dd) * L + t0 + ch * 8) * 2u;
      u32x4 v0, v1;
      for (unsigned spins = 0; spins < (1u << 22); ++spins) {
        uint32_t x;
        asm volatile("ds_read_b32 %0, %1\n\ts_waitcnt lgkmcnt(0)" : "=v"(x) : "v"(lds_addr(smem + K1_FLAG + 32 + cw * 4)) : "memory");
        if (__builtin_amdgcn_readfirstlane(x) == 1u) break;
        __builtin_amdgcn_s_sleep(2);
      }
      for (unsigned spins = 0; spins < (1u << 20); ++spins) {
        asm volatile("global_load_dwordx4 %0, %2, %3 sc1\n\tglobal_load_dwordx4 %1, %2, %3 offset:64 sc1\n\ts_waitcnt vmcnt(0)"
                     : "=&v"(v0), "=&v"(v1) : "v"(off), "s"(p.yT) : "memory");
        const bool ok = v0[0] != ~0u && v0[1] != ~0u && v0[2] != ~0u && v0[3] != ~0u && v1[0] != ~0u && v1[1] != ~0u && v1[2] != ~0u && v1[3] != ~0u;
        if (__all(ok)) break;
        __builtin_amdgcn_s_sleep(4);
      }
      *(u32x4*)(smem + K1_YOFF + dd * 144 + ch * 16) = v0;
      *(u32x4*)(smem + K1_YOFF + dd * 144 + ch * 16 + 64) = v1;
    }
    TS(6);
    WAIT_L(0);
    BAR();
  }
  TSDUMP(1);
}

extern "C" void kernel_launch(void* const* d_in, const int* in_sizes, int n_in, void* d_out, int out_size, void* d_ws, size_t ws_size,
                              hipStream_t stream) {
  char* ws = (char*)d_ws;
  P0 p0;
  p0.u = (const u16*)d_in[0]; p0.Are = (const u16*)d_in[1]; p0.Aim = (const u16*)d_in[2]; p0.Bre = (const u16*)d_in[3];
  p0.Bim = (const u16*)d_in[4]; p0.Cre = (const u16*)d_in[5]; p0.Cim = (const u16*)d_in[6]; p0.logdt = (const u16*)d_in[7];
  p0.wg = (const u16*)d_in[10]; p0.wu = (const u16*)d_in[11]; p0.wd = (const u16*)d_in[12]; p0.gw = (const u16*)d_in[13];
  p0.h = (u16*)(ws + WS_H); p0.wdt = (u16*)(ws + WS_WDT); p0.uT = (u16*)(ws + WS_UT); p0.E = (u16*)(ws + WS_E);
  p0.yT = (u16*)(ws + WS_YT); p0.g = (float*)(ws + WS_G);
  p0.Dskip = (const u16*)d_in[8];
  P1 p1;
  p1.u = p0.u; p1.Dskip = (const u16*)d_in[8]; p1.h = p0.h; p1.wdt = p0.wdt; p1.uT = p0.uT; p1.E = p0.E; p1.g = p0.g;
  p1.yT = p0.yT; p1.out = (u16*)d_out;
  (void)hipFuncSetAttribute((const void*)k0_kernel, hipFuncAttributeMaxDynamicSharedMemorySize, K0_LDS);
  (void)hipFuncSetAttribute((const void*)k1_kernel, hipFuncAttributeMaxDynamicSharedMemorySize, K1_LDS);
  k0_kernel<<<256, 768, K0_LDS, stream>>>(p0);
  k1_kernel<<<256, 512, K1_LDS, stream>>>(p1);
}
```
